```python
import torch

def _softmax(x, axis=-1):
    return torch.softmax(x, dim=axis)

def _gelu(x):
    return torch.nn.functional.gelu(x, approximate='tanh')

def _silu(x):
    return torch.nn.functional.silu(x)

def _sigmoid(x):
    return torch.sigmoid(x)

def _layernorm(x, axis=-1, eps=1e-05):
    m = x.mean(dim=axis, keepdim=True)
    v = x.var(dim=axis, keepdim=True, unbiased=False)
    return (x - m) / torch.sqrt(v + eps)

def _scatter_add(tgt, idx, src, axis=0):
    return tgt.clone().index_add_(axis, idx, src)

def _segment_sum(src, seg, n):
    out = torch.zeros((n,) + src.shape[1:], dtype=src.dtype)
    return out.index_add_(0, seg, src)

def _pad(x, pw):
    flat = tuple((v for pair in reversed(pw) for v in pair))
    return torch.nn.functional.pad(x, flat)
kernel_name = 'adaLN_cond__mha_block_cached__geglu_ffn__decode'

def setup_inputs():

    _w = lambda a: (lambda t: t.to(torch.bfloat16) if t.is_floating_point() else t)(torch.as_tensor(a))

    def _A_setup():
        B, Sq, D = (4, 1, 512)
        H, Sk, Dh = (32, 8192, 128)
        return {'x': _w((lambda *s: torch.randn(*s, dtype=torch.bfloat16) * 0.05)(B, Sq, D)), 'k_cache': _w((lambda *s: torch.randn(*s, dtype=torch.bfloat16) * 3.2)(B, H, Sk, Dh)), 'v_cache': _w((lambda *s: torch.randn(*s, dtype=torch.bfloat16) * 0.4)(B, H, Sk, Dh)), 'wq': _w((lambda *s: torch.randn(*s, dtype=torch.bfloat16) * 3.2)(D, H * Dh) * D ** (-0.5)), 'wo': _w((lambda *s: torch.randn(*s, dtype=torch.bfloat16) * 0.4)(H * Dh, D) * (H * Dh) ** (-0.5))}
    _A_in = _A_setup()

    def _B_setup():
        B, S, D, F = (4, 1, 512, 1792)
        return {'x': _w((lambda *s: torch.randn(*s, dtype=torch.bfloat16) * 0.05)(B, S, D)), 'w_gate': _w((lambda *s: torch.randn(*s, dtype=torch.bfloat16) * 0.05)(D, F)), 'w_up': _w((lambda *s: torch.randn(*s, dtype=torch.bfloat16) * 0.05)(D, F)), 'w_down': _w((lambda *s: torch.randn(*s, dtype=torch.bfloat16) * 0.05)(F, D))}
    _B_in = _B_setup()
    return {'A_x': _A_in['x'], 'A_k_cache': _A_in['k_cache'], 'A_v_cache': _A_in['v_cache'], 'A_wq': _A_in['wq'], 'A_wo': _A_in['wo'], 'B_x': _B_in['x'], 'B_w_gate': _B_in['w_gate'], 'B_w_up': _B_in['w_up'], 'B_w_down': _B_in['w_down'], 'c': _w((lambda *s: torch.randn(*s, dtype=torch.bfloat16) * 0.1)(4, 512) * 8.0), 'w_mod': _w((lambda *s: torch.randn(*s, dtype=torch.bfloat16) * 0.1)(512, 6 * 512) * 512 ** (-0.5) * 8.0)}

def reference(A_x, A_k_cache, A_v_cache, A_wq, A_wo, B_x, B_w_gate, B_w_up, B_w_down, c, w_mod):

    def _A_ref(x, k_cache, v_cache, wq, wo):
        B, Sq, D = x.shape
        H, Dh = (32, 128)
        q = (lambda x, *d: x.permute(*d))((x @ wq).reshape(B, Sq, H, Dh), 0, 2, 1, 3)
        scores = torch.einsum('bhqd,bhkd->bhqk', q, k_cache) * Dh ** (-0.5)
        p = _softmax(scores, axis=-1)
        o = torch.einsum('bhqk,bhkd->bhqd', p, v_cache)
        return (lambda x, *d: x.permute(*d))(o, 0, 2, 1, 3).reshape(B, Sq, H * Dh) @ wo

    def _B_ref(x, w_gate, w_up, w_down):
        return _gelu(x @ w_gate) * (x @ w_up) @ w_down
    x = A_x
    mod = (_silu(c) @ w_mod).reshape(c.shape[0], 6, 1, -1)
    sa, ca, ga, sb, cb, gb = (mod[:, 0].clone(), mod[:, 1].clone(), mod[:, 2].clone(), mod[:, 3].clone(), mod[:, 4].clone(), mod[:, 5].clone())
    h = _layernorm(x, axis=-1) * (1 + ca) + sa
    x = x + ga * _A_ref(h, A_k_cache, A_v_cache, A_wq, A_wo)
    h = _layernorm(x, axis=-1) * (1 + cb) + sb
    x = x + gb * _B_ref(h, B_w_gate, B_w_up, B_w_down)
    return x
```

```cpp
#include <hip/hip_runtime.h>
#include <hip/hip_bf16.h>
#include <stdint.h>

#define MIMIC 0

typedef unsigned short u16;
typedef unsigned v4u __attribute__((ext_vector_type(4)));
typedef float f32x4 __attribute__((ext_vector_type(4)));
typedef __bf16 bf16x2 __attribute__((ext_vector_type(2)));

constexpr int NB = 4, D = 512, H = 32, SK = 8192, DH = 128, F = 1792;
constexpr int NSPLIT = 4;

__device__ __forceinline__ float bf2f(u16 b) { return __builtin_bit_cast(float, (unsigned)b << 16); }
__device__ __forceinline__ float rbf(float f) {
  unsigned u = __builtin_bit_cast(unsigned, f);
  u = (u + 0x7fffu + ((u >> 16) & 1u)) & 0xffff0000u;
  return __builtin_bit_cast(float, u);
}
__device__ __forceinline__ u16 f2bf(float f) { return (u16)(__builtin_bit_cast(unsigned, rbf(f)) >> 16); }
__device__ __forceinline__ float lo(unsigned u) { return __builtin_bit_cast(float, u << 16); }
__device__ __forceinline__ float hi(unsigned u) { return __builtin_bit_cast(float, u & 0xffff0000u); }
__device__ __forceinline__ float dot2(unsigned k, unsigned q, float acc) {
  return __builtin_amdgcn_fdot2_f32_bf16(__builtin_bit_cast(bf16x2, k), __builtin_bit_cast(bf16x2, q), acc, false);
}

template <int K>
__device__ __forceinline__ void gemv64(const float* xs  , const u16* W, int N, int n0, float* red  , float* res  ) {
  const int tid = threadIdx.x, cg = tid & 7, rg = tid >> 3;
  float acc[4][8];
#pragma unroll
  for (int b = 0; b < 4; ++b)
#pragma unroll
    for (int j = 0; j < 8; ++j) acc[b][j] = 0.f;
#pragma unroll 4
  for (int k = rg; k < K; k += 32) {
    v4u w = *(const v4u*)(W + (size_t)k * N + n0 + cg * 8);
    float wf[8];
#pragma unroll
    for (int j = 0; j < 4; ++j) { wf[2 * j] = lo(w[j]); wf[2 * j + 1] = hi(w[j]); }
#pragma unroll
    for (int b = 0; b < 4; ++b) {
      float xv = xs[b * K + k];
#pragma unroll
      for (int j = 0; j < 8; ++j) acc[b][j] = fmaf(xv, wf[j], acc[b][j]);
    }
  }
#pragma unroll
  for (int b = 0; b < 4; ++b)
#pragma unroll
    for (int j = 0; j < 8; ++j) red[(rg * 4 + b) * 64 + cg * 8 + j] = acc[b][j];
  __syncthreads();
  {
    const int b = tid >> 6, c = tid & 63;
    float s = 0.f;
    for (int r = 0; r < 32; ++r) s += red[(r * 4 + b) * 64 + c];
    res[b * 64 + c] = s;
  }
  __syncthreads();
}

__device__ __forceinline__ void ln_mod(const float* xin, const float* shift, const float* scale, float* out) {
  const int tid = threadIdx.x, b = tid >> 6, lane = tid & 63;
  float v[8]; float s = 0.f;
#pragma unroll
  for (int j = 0; j < 8; ++j) { v[j] = xin[b * D + lane + 64 * j]; s += v[j]; }
  for (int o = 32; o; o >>= 1) s += __shfl_xor(s, o);
  float m = s * (1.f / D);
  float q = 0.f;
#pragma unroll
  for (int j = 0; j < 8; ++j) { float d = v[j] - m; q += d * d; }
  for (int o = 32; o; o >>= 1) q += __shfl_xor(q, o);
  float var = q * (1.f / D);
  float rs = 1.f / sqrtf(var + 1e-5f);
#pragma unroll
  for (int j = 0; j < 8; ++j) { int c = lane + 64 * j;
    out[b * D + c] = (v[j] - m) * rs * (1.f + scale[b * 3072 + c]) + shift[b * 3072 + c]; }
  __syncthreads();
}
#define R(x) (x)

__global__ void __launch_bounds__(256) k_mod(const u16* c, const u16* wmod, float* mod) {
  __shared__ float xs[4 * D]; __shared__ float red[32 * 4 * 64]; __shared__ float res[256];
  for (int i = threadIdx.x; i < 4 * D; i += 256) { float v = bf2f(c[i]); xs[i] = R(v / (1.f + __expf(-v))); }
  __syncthreads();
  gemv64<D>(xs, wmod, 3072, blockIdx.x * 64, red, res);
  mod[(threadIdx.x >> 6) * 3072 + blockIdx.x * 64 + (threadIdx.x & 63)] = R(res[threadIdx.x]);
}

__global__ void __launch_bounds__(256) k_q(const u16* x, const float* mod, const u16* wq, u16* q) {
  __shared__ float xin[4 * D]; __shared__ float xs[4 * D]; __shared__ float red[32 * 4 * 64]; __shared__ float res[256];
  for (int i = threadIdx.x; i < 4 * D; i += 256) xin[i] = bf2f(x[i]);
  __syncthreads();
  ln_mod(xin, mod, mod + 512, xs);
  gemv64<D>(xs, wq, 4096, blockIdx.x * 64, red, res);
  q[(threadIdx.x >> 6) * 4096 + blockIdx.x * 64 + (threadIdx.x & 63)] = f2bf(res[threadIdx.x]);
}

template <int CTRL> __device__ __forceinline__ float dpp(float x) {
  return __builtin_bit_cast(float, __builtin_amdgcn_mov_dpp(__builtin_bit_cast(int, x), CTRL, 0xf, 0xf, true));
}
constexpr int XOR1 = 0xB1, XOR2 = 0x4E, XOR7 = 0x141, XOR8 = 0x128;
__device__ __forceinline__ float row16_sum(float x) { x += dpp<XOR8>(x); x += dpp<XOR7>(x); x += dpp<XOR2>(x); x += dpp<XOR1>(x); return x; }

__global__ void __launch_bounds__(256) k_attn(const u16* q, const u16* kc, const u16* vc, float* pml, float* po) {
  __shared__ float mrg[4][4][132];
  const int s = blockIdx.x, bh = blockIdx.y;
  const int tid = threadIdx.x, lane = tid & 63, wave = tid >> 6, r = lane >> 4, c = lane & 15;
  const int b = bh >> 5, h = bh & 31;
  const v4u qv = *(const v4u*)(q + b * 4096 + h * 128 + c * 8);
  constexpr int KEYS = SK / NSPLIT;
  constexpr int U = 4;
  const u16* kb = kc + ((size_t)bh * SK + s * KEYS) * DH + (size_t)(wave * 16 + r) * DH + c * 8;
  const u16* vb = vc + ((size_t)bh * SK + s * KEYS) * DH + (size_t)(wave * 16 + r) * DH + c * 8;
  float m = -INFINITY, l = 0.f, acc[8];
#pragma unroll
  for (int j = 0; j < 8; ++j) acc[j] = 0.f;
  constexpr float LOG2E = 1.4426950408889634f, SC = 0.08838834764831845f;
  v4u kn[U], vn[U];
#pragma unroll
  for (int t = 0; t < U; ++t) { kn[t] = __builtin_nontemporal_load((const v4u*)(kb + t * 4 * DH)); vn[t] = __builtin_nontemporal_load((const v4u*)(vb + t * 4 * DH)); }
  constexpr int NIT = KEYS / 64;
  for (int it = 0; it < NIT; ++it) {
    v4u kk[U], vv[U];
#pragma unroll
    for (int t = 0; t < U; ++t) { kk[t] = kn[t]; vv[t] = vn[t]; }
    if (it + 1 < NIT) {
      const u16* k2 = kb + (size_t)(it + 1) * 64 * DH; const u16* v2 = vb + (size_t)(it + 1) * 64 * DH;
#pragma unroll
      for (int t = 0; t < U; ++t) { kn[t] = __builtin_nontemporal_load((const v4u*)(k2 + t * 4 * DH)); vn[t] = __builtin_nontemporal_load((const v4u*)(v2 + t * 4 * DH)); }
    }
    float sc[U];
#pragma unroll
    for (int t = 0; t < U; ++t) { float d = 0.f;
#pragma unroll
      for (int j = 0; j < 4; ++j) d = dot2(kk[t][j], qv[j], d);
      d = row16_sum(d);
      sc[t] = d * (SC * LOG2E);
    }
    float mn = fmaxf(fmaxf(fmaxf(sc[0], sc[1]), fmaxf(sc[2], sc[3])), m);
    float al = __builtin_amdgcn_exp2f(m - mn);
    m = mn;
    l *= al;
#pragma unroll
    for (int j = 0; j < 8; ++j) acc[j] *= al;
#pragma unroll
    for (int t = 0; t < U; ++t) { float p = __builtin_amdgcn_exp2f(sc[t] - mn); l += p;
#pragma unroll
      for (int j = 0; j < 4; ++j) { acc[2 * j] = fmaf(p, lo(vv[t][j]), acc[2 * j]); acc[2 * j + 1] = fmaf(p, hi(vv[t][j]), acc[2 * j + 1]); } }
  }
#pragma unroll
  for (int j = 0; j < 8; ++j) mrg[wave][r][4 + c * 8 + j] = acc[j];
  if (c == 0) { mrg[wave][r][0] = m; mrg[wave][r][1] = l; }
  __syncthreads();
  if (tid < 128) {
    float M = -INFINITY;
    for (int i = 0; i < 16; ++i) M = fmaxf(M, mrg[i >> 2][i & 3][0]);
    float L = 0.f, o = 0.f;
    for (int i = 0; i < 16; ++i) { float w = __builtin_amdgcn_exp2f(mrg[i >> 2][i & 3][0] - M); L += w * mrg[i >> 2][i & 3][1]; o += w * mrg[i >> 2][i & 3][4 + tid]; }
    po[(bh * NSPLIT + s) * DH + tid] = o;
    if (tid == 0) { pml[(bh * NSPLIT + s) * 2] = M; pml[(bh * NSPLIT + s) * 2 + 1] = L; }
  }
}

__global__ void __launch_bounds__(256) k_wo(const float* pml, const float* po, const u16* wo, float* ypart) {
  __shared__ float xs[4 * 512]; __shared__ float red[32 * 4 * 64]; __shared__ float res[256];
  const int ks = blockIdx.y;
  for (int i = threadIdx.x; i < 4 * 512; i += 256) {
    int b = i >> 9, kk = ks * 512 + (i & 511), h = kk >> 7, d = kk & 127, bh = b * 32 + h;
    float M = -INFINITY;
    for (int s = 0; s < NSPLIT; ++s) M = fmaxf(M, pml[(bh * NSPLIT + s) * 2]);
    float L = 0.f, o = 0.f;
    for (int s = 0; s < NSPLIT; ++s) { float w = __builtin_amdgcn_exp2f(pml[(bh * NSPLIT + s) * 2] - M); L += w * pml[(bh * NSPLIT + s) * 2 + 1]; o += w * po[(bh * NSPLIT + s) * DH + d]; }
    xs[i] = R(o / L);
  }
  __syncthreads();
  gemv64<512>(xs, wo + (size_t)ks * 512 * 512, 512, blockIdx.x * 64, red, res);
  ypart[(ks * 4 + (threadIdx.x >> 6)) * 512 + blockIdx.x * 64 + (threadIdx.x & 63)] = res[threadIdx.x];
}

__global__ void __launch_bounds__(256) k_gu(const u16* x, const float* mod, const float* ypart, const u16* wg, const u16* wu, float* x1out, float* act) {
  __shared__ float xin[4 * D]; __shared__ float xs[4 * D]; __shared__ float red[32 * 4 * 64]; __shared__ float res[256]; __shared__ float res2[256];
  for (int i = threadIdx.x; i < 4 * D; i += 256) {
    int b = i >> 9, cc = i & 511; float y = 0.f;
    for (int ks = 0; ks < 8; ++ks) y += ypart[(ks * 4 + b) * 512 + cc];
    float v = R(bf2f(x[i]) + R(mod[b * 3072 + 1024 + cc] * R(y)));
    xin[i] = v; if (blockIdx.x == 0) x1out[i] = v;
  }
  __syncthreads();
  ln_mod(xin, mod + 1536, mod + 2048, xs);
  gemv64<D>(xs, wg, F, blockIdx.x * 64, red, res);
  gemv64<D>(xs, wu, F, blockIdx.x * 64, red, res2);
  float g = R(res[threadIdx.x]), u = R(res2[threadIdx.x]);
  float ge = R(0.5f * g * (1.f + tanhf(0.7978845608028654f * (g + 0.044715f * g * g * g))));
  act[(threadIdx.x >> 6) * F + blockIdx.x * 64 + (threadIdx.x & 63)] = R(ge * u);
}

__global__ void __launch_bounds__(256) k_down(const float* x1, const float* mod, const float* act, const u16* wd, u16* out) {
  __shared__ float xs[4 * F]; __shared__ float red[32 * 4 * 64]; __shared__ float res[256];
  for (int i = threadIdx.x; i < 4 * F; i += 256) xs[i] = act[i];
  __syncthreads();
  gemv64<F>(xs, wd, 512, blockIdx.x * 64, red, res);
  int b = threadIdx.x >> 6, cc = blockIdx.x * 64 + (threadIdx.x & 63);
  out[b * 512 + cc] = f2bf(x1[b * 512 + cc] + R(mod[b * 3072 + 2560 + cc] * R(res[threadIdx.x])));
}

extern "C" void kernel_launch(void* const* d_in, const int* in_sizes, int n_in, void* d_out, int out_size, void* d_ws, size_t ws_size, hipStream_t stream) {
  const u16* x = (const u16*)d_in[0]; const u16* kc = (const u16*)d_in[1]; const u16* vc = (const u16*)d_in[2];
  const u16* wq = (const u16*)d_in[3]; const u16* wo = (const u16*)d_in[4];
  const u16* wg = (const u16*)d_in[6]; const u16* wu = (const u16*)d_in[7]; const u16* wd = (const u16*)d_in[8];
  const u16* c = (const u16*)d_in[9]; const u16* wmod = (const u16*)d_in[10];
  char* ws = (char*)d_ws;
  float* mod = (float*)ws;
  u16* q = (u16*)(ws + 65536);
  float* pml = (float*)(ws + 131072);
  float* po = (float*)(ws + 196608);
  float* ypart = (float*)(ws + 1048576);
  float* x1 = (float*)(ws + 1048576 + 65536);
  float* act = (float*)(ws + 1048576 + 131072);
  k_mod<<<48, 256, 0, stream>>>(c, wmod, mod);
  k_q<<<64, 256, 0, stream>>>(x, mod, wq, q);
  k_attn<<<dim3(NSPLIT, 128), 256, 0, stream>>>(q, kc, vc, pml, po);
  k_wo<<<dim3(8, 8), 256, 0, stream>>>(pml, po, wo, ypart);
  k_gu<<<28, 256, 0, stream>>>(x, mod, ypart, wg, wu, x1, act);
  k_down<<<8, 256, 0, stream>>>(x1, mod, act, wd, (u16*)d_out);
}
```

```cpp
#include <hip/hip_runtime.h>
#pragma clang diagnostic ignored "-Wint-to-pointer-cast"

#define WIN 20
#define U0 5
#define STAMPS 0

namespace blk {

typedef unsigned short u16;
typedef unsigned u32;
typedef unsigned long long u64;
typedef u32 v4u __attribute__((ext_vector_type(4)));
typedef u32 v2u __attribute__((ext_vector_type(2)));
typedef float f4 __attribute__((ext_vector_type(4)));
typedef float f2 __attribute__((ext_vector_type(2)));
typedef __bf16 bf8 __attribute__((ext_vector_type(8)));
typedef __bf16 bf2 __attribute__((ext_vector_type(2)));
typedef short s4 __attribute__((ext_vector_type(4)));
#define GAS __attribute__((address_space(1)))
#define LAS __attribute__((address_space(3)))

constexpr int NTHR = 704;
constexpr int FF = 1792;
constexpr int L_SC = 131072;
constexpr int L_MODRED = L_SC + 16384;
constexpr int L_QRED = L_MODRED + 2048;
constexpr int L_GURED = L_QRED + 4096;
constexpr int L_QV = L_GURED + 2048;
constexpr int L_OA = L_QV + 256;
constexpr int L_CTRL = L_OA + 1280;
constexpr int C_LANDED = L_CTRL, C_CONS = L_CTRL + 8, C_MODCNT = L_CTRL + 16, C_QCNT = L_CTRL + 20, C_QFLAG = L_CTRL + 24, C_TAG = L_CTRL + 28;
constexpr int LDS_BYTES = L_CTRL + 64;
constexpr int W_EPOCH = 0, W_HELLO = 4096, W_MOD = 8192, W_ML = 65536, W_X1 = 98304, W_IN1 = 262144, W_IN3 = W_IN1 + (1 << 20);
constexpr int W_END = W_IN3 + (1 << 20), W_DBG = W_END;
constexpr int SC1 = 16, NT = 2;
constexpr u32 SPIN_CAP = 1u << 18;

__device__ __forceinline__ float blo(u32 u) { return __builtin_bit_cast(float, u << 16); }
__device__ __forceinline__ float bhi(u32 u) { return __builtin_bit_cast(float, u & 0xffff0000u); }
__device__ __forceinline__ u32 pk(float a, float b) { f2 p = {a, b}; return __builtin_bit_cast(u32, __builtin_convertvector(p, bf2)); }
__device__ __forceinline__ float rbf(float a) { return blo(pk(a, a)); }
__device__ __forceinline__ u32 fu(float a) { return __builtin_bit_cast(u32, a); }
__device__ __forceinline__ float uf(u32 a) { return __builtin_bit_cast(float, a); }
__device__ __forceinline__ f4 mfma32(v4u a, v4u b, f4 c) { return __builtin_amdgcn_mfma_f32_16x16x32_bf16(__builtin_bit_cast(bf8, a), __builtin_bit_cast(bf8, b), c, 0, 0, 0); }
__device__ __forceinline__ f4 mfma16(v2u a, v2u b, f4 c) { return __builtin_amdgcn_mfma_f32_16x16x16bf16_1k(__builtin_bit_cast(s4, a), __builtin_bit_cast(s4, b), c, 0, 0, 0); }
template <int CTRL> __device__ __forceinline__ u32 dppu(u32 x) { return (u32)__builtin_amdgcn_mov_dpp((int)x, CTRL, 0xf, 0xf, true); }
template <int CTRL> __device__ __forceinline__ float dppf(float x) { return uf(dppu<CTRL>(fu(x))); }
constexpr int XOR1 = 0xB1, XOR2 = 0x4E, HMIR = 0x141, ROR4 = 0x124, ROR8 = 0x128;
__device__ __forceinline__ float xrow_sum(float x) {
  auto s = __builtin_amdgcn_permlane16_swap(fu(x), fu(x), false, false);
  x = uf(s[0]) + uf(s[1]);
  auto t = __builtin_amdgcn_permlane32_swap(fu(x), fu(x), false, false);
  return uf(t[0]) + uf(t[1]);
}
__device__ __forceinline__ float xrow_max(float x) {
  auto s = __builtin_amdgcn_permlane16_swap(fu(x), fu(x), false, false);
  x = fmaxf(uf(s[0]), uf(s[1]));
  auto t = __builtin_amdgcn_permlane32_swap(fu(x), fu(x), false, false);
  return fmaxf(uf(t[0]), uf(t[1]));
}
__device__ __forceinline__ float wave_sum(float x) { x += dppf<XOR1>(x); x += dppf<XOR2>(x); x += dppf<HMIR>(x); x += dppf<ROR8>(x); return xrow_sum(x); }
__device__ __forceinline__ float wave_max(float x) { x = fmaxf(x, dppf<XOR1>(x)); x = fmaxf(x, dppf<XOR2>(x)); x = fmaxf(x, dppf<HMIR>(x)); x = fmaxf(x, dppf<ROR8>(x)); return xrow_max(x); }
__device__ __forceinline__ u32 perm_lo(u32 r0, u32 r1) { return __builtin_amdgcn_perm(r1, r0, 0x05040100u); }
__device__ __forceinline__ u32 perm_hi(u32 r0, u32 r1) { return __builtin_amdgcn_perm(r1, r0, 0x07060302u); }
#define CFENCE() asm volatile("" ::: "memory")
#define SBAR() __builtin_amdgcn_sched_barrier(0)
#define PIN4(a) asm volatile("" : "+v"((a)[0]), "+v"((a)[1]), "+v"((a)[2]), "+v"((a)[3]))
template <typename T> __device__ __forceinline__ LAS T* lp(u32 off) { return (LAS T*)off; }
__device__ __forceinline__ u32 lds_poll(u32 off) { return (u32)__builtin_amdgcn_readfirstlane((int)*(volatile LAS u32*)off); }
template <typename T> __device__ __forceinline__ T ldnt(const void* p) { return __builtin_nontemporal_load((const GAS T*)p); }
template <typename T> __device__ __forceinline__ T ldg(const void* p) { return *(const GAS T*)p; }
__device__ __forceinline__ void tr8x4(const v2u (&r)[8], v4u (&f)[4]) {
#pragma unroll
  for (int q = 0; q < 4; ++q) {
    f[0][q] = perm_lo(r[2 * q].x, r[2 * q + 1].x); f[1][q] = perm_hi(r[2 * q].x, r[2 * q + 1].x);
    f[2][q] = perm_lo(r[2 * q].y, r[2 * q + 1].y); f[3][q] = perm_hi(r[2 * q].y, r[2 * q + 1].y);
  }
}
__device__ __forceinline__ int slice_col(int s, int pos) { return 64 * (s >> 3) + 32 * (s & 1) + 4 * pos + ((s >> 1) & 3); }

#define STAMP(i) do { } while (0)

__global__ void __launch_bounds__(NTHR) fused_block(const char* x, const char* kc, const char* vc, const char* wq, const char* wo, const char* wg,
                                                    const char* wu, const char* wd, const char* cnd, const char* wmod, char* out, char* ws) {
  __shared__ __attribute__((aligned(1024))) char lds_mem[LDS_BYTES];
  const int lane = threadIdx.x & 63, wave = __builtin_amdgcn_readfirstlane(threadIdx.x >> 6);
  const int xcd = blockIdx.x & 7, idx = blockIdx.x >> 3;
  const int half = idx & 1, hh = (idx >> 1) & 3, b = idx >> 3;
  const int h = 4 * xcd + hh;
  const int g = lane >> 4, n = lane & 15;
  const f4 zero4 = {0.f, 0.f, 0.f, 0.f};
  constexpr float LOG2E = 1.4426950408889634f;

  if (__builtin_expect(wave == 0, 1)) {
    const size_t reg_off = (size_t)((b * 32 + h) * 2 + half) << 20;
    const char* src = kc + reg_off;
    u32 o0 = g * 256 + ((n ^ g) << 4), o1 = o0 ^ 64, o2 = o0 ^ 128, o3 = o0 ^ 192;
    asm volatile("" : "+v"(o0), "+v"(o1), "+v"(o2), "+v"(o3));
#pragma unroll
    for (int u = 0; u < U0; ++u) {
      asm volatile("s_mov_b32 m0, %5\n s_nop 0\n"
                   "global_load_lds_dwordx4 %0, %4 nt\n global_load_lds_dwordx4 %1, %4 offset:1024 nt\n"
                   "global_load_lds_dwordx4 %2, %4 offset:2048 nt\n global_load_lds_dwordx4 %3, %4 offset:3072 nt"
                   :: "v"(o0), "v"(o1), "v"(o2), "v"(o3), "s"(src), "i"(u * 4096) : "memory");
      src += 4096;
    }
    asm volatile("" :: "v"(lds_mem) : "memory");
    __builtin_amdgcn_s_barrier();
    u32 c0 = 0, c1 = 0;
    const u32 a_landed = C_LANDED; u32 va = a_landed;
    asm volatile("" : "+v"(va));
    constexpr int PUBI = (WIN - 1) & 3, LAG = (WIN - 1 - PUBI) / 4;
#pragma nounroll
    for (int u = U0; u < 512; ++u) {
      if (u == 256) { src = vc + reg_off; o0 = o1 = o2 = o3 = lane * 16; }
      if (u >= 32) {
        const u32 need = ((u - 32) >> 1) + 1;
        u32 spin = 0;
#pragma nounroll
        while (((u & 1) ? c1 : c0) < need && spin < SPIN_CAP * 8) {
          v2u cc;
          asm volatile("ds_read_b64 %0, %1 offset:8\n s_waitcnt lgkmcnt(0)" : "=&v"(cc) : "v"(va) : "memory");
          c0 = __builtin_amdgcn_readfirstlane(cc.x); c1 = __builtin_amdgcn_readfirstlane(cc.y);
          ++spin;
        }
      }
      const u32 m0v = (u & 31) << 12;
      u32 pub = u >= LAG ? u - LAG : 0;
#define PUB(i) (PUBI == (i) ? "s_mov_b64 exec, 1\n ds_write_b32 %6, %7\n s_mov_b64 exec, -1\n" : "")
      if constexpr (PUBI == 3)
        asm volatile("s_mov_b32 m0, %5\n s_waitcnt vmcnt(%8)\n global_load_lds_dwordx4 %0, %4 nt\n"
                     "s_waitcnt vmcnt(%8)\n global_load_lds_dwordx4 %1, %4 offset:1024 nt\n"
                     "s_waitcnt vmcnt(%8)\n global_load_lds_dwordx4 %2, %4 offset:2048 nt\n"
                     "s_waitcnt vmcnt(%8)\n global_load_lds_dwordx4 %3, %4 offset:3072 nt\n"
                     "s_mov_b64 exec, 1\n ds_write_b32 %6, %7\n s_mov_b64 exec, -1"
                     :: "v"(o0), "v"(o1), "v"(o2), "v"(o3), "s"(src), "s"(m0v), "v"(va), "v"(pub), "i"(WIN - 1) : "memory");
      else if constexpr (PUBI == 1)
        asm volatile("s_mov_b32 m0, %5\n s_waitcnt vmcnt(%8)\n global_load_lds_dwordx4 %0, %4 nt\n"
                     "s_waitcnt vmcnt(%8)\n global_load_lds_dwordx4 %1, %4 offset:1024 nt\n"
                     "s_mov_b64 exec, 1\n ds_write_b32 %6, %7\n s_mov_b64 exec, -1\n"
                     "s_waitcnt vmcnt(%8)\n global_load_lds_dwordx4 %2, %4 offset:2048 nt\n"
                     "s_waitcnt vmcnt(%8)\n global_load_lds_dwordx4 %3, %4 offset:3072 nt"
                     :: "v"(o0), "v"(o1), "v"(o2), "v"(o3), "s"(src), "s"(m0v), "v"(va), "v"(pub), "i"(WIN - 1) : "memory");
      static_assert(PUBI == 3 || PUBI == 1, "WIN even");
      src += 4096;
    }
#pragma unroll
    for (int k = LAG; k >= 0; --k) {
      u32 pub = 512 - k;
      asm volatile("s_waitcnt vmcnt(%2)\n s_mov_b64 exec, 1\n ds_write_b32 %0, %1\n s_mov_b64 exec, -1" :: "v"(va), "v"(pub), "i"(4 * k) : "memory");
    }
    STAMP(1);
    return;
  }

  const __amdgpu_buffer_rsrc_t rs = __builtin_amdgcn_make_buffer_rsrc((void*)ws, 0, W_END, 0x00020000);

  if (wave <= 2) {
    const int a = wave - 1;
    if (a == 0 && lane < 16) *lp<u32>(L_CTRL + 4 * lane) = 0u;
    asm volatile("s_waitcnt lgkmcnt(0)" ::: "memory");
    __builtin_amdgcn_s_barrier();
#pragma nounroll
    for (u32 spin = 0; lds_poll(C_QFLAG) == 0u && spin < SPIN_CAP * 8; ++spin) __builtin_amdgcn_s_sleep(1);
    CFENCE();
    v4u qa[4];
#pragma unroll
    for (int s = 0; s < 4; ++s) qa[s] = *lp<v4u>(L_QV + 64 * s + 16 * g);
    const u32 tag = lds_poll(C_TAG);
    if (a == 0) STAMP(2);
    const u32 sc = L_SC + a * 8192;
    u32 landed = 0;
    const u32 kofs = n * 256 + ((g ^ n) << 4);
    const u32 spos = sc + ((n & 3) * 4 + (n >> 2)) * 4;
#pragma nounroll
    for (int j = 0; j < 128; ++j) {
      const u32 u = 2 * j + a;
#pragma nounroll
      for (u32 spin = 0; landed <= u && spin < SPIN_CAP * 8; ++spin) landed = lds_poll(C_LANDED);
      CFENCE();
      const u32 ad = ((u & 31) << 12) + kofs;
      v4u k0 = *lp<v4u>(ad), k1 = *lp<v4u>(ad ^ 64), k2 = *lp<v4u>(ad ^ 128), k3 = *lp<v4u>(ad ^ 192);
      asm volatile("" : "+v"(k0), "+v"(k1), "+v"(k2), "+v"(k3) :: "memory");
      *(volatile LAS u32*)(C_CONS + 4 * a) = j + 1;
      f4 d = mfma32(qa[0], k0, zero4);
      d = mfma32(qa[1], k1, d); d = mfma32(qa[2], k2, d); d = mfma32(qa[3], k3, d);
      *lp<float>(spos + j * 64) = d[0];
    }
    if (a == 0) STAMP(3);
    float mw, lw;
    {
      f4 s[8];
      float mx = -INFINITY;
#pragma unroll
      for (int t = 0; t < 8; ++t) {
        s[t] = *lp<f4>(sc + lane * 16 + t * 1024);
        const u32 p0 = pk(s[t][0], s[t][1]), p1 = pk(s[t][2], s[t][3]);
        const u32 r0 = pk(blo(p0) * 0.08838834764831845f, bhi(p0) * 0.08838834764831845f), r1 = pk(blo(p1) * 0.08838834764831845f, bhi(p1) * 0.08838834764831845f);
        s[t] = f4{blo(r0), bhi(r0), blo(r1), bhi(r1)};
        mx = fmaxf(fmaxf(mx, fmaxf(s[t][0], s[t][1])), fmaxf(s[t][2], s[t][3]));
      }
      mw = wave_max(mx);
      float sum = 0.f;
#pragma unroll
      for (int t = 0; t < 8; ++t) {
        v4u pp;
#pragma unroll
        for (int e = 0; e < 4; ++e) { const float p = __builtin_amdgcn_exp2f((s[t][e] - mw) * LOG2E); sum += p; pp[e] = pk(p, p); }
        *lp<v4u>(sc + lane * 16 + t * 1024) = pp;
      }
      lw = wave_sum(sum);
    }
    const int mlbase = W_ML + (b * 32 + h) * 128;
    if (lane == 0) __builtin_amdgcn_raw_buffer_store_b128(v4u{fu(mw), tag, fu(lw), tag}, rs, mlbase + (half * 2 + a) * 16, 0, SC1);
    u32 msk[4];
#pragma unroll
    for (int q = 0; q < 4; ++q) msk[q] = ((n & 7) >> 1) == q ? ((n & 1) ? 0xffff0000u : 0x0000ffffu) : 0u;
    f4 acc = zero4;
    float scale = 0.f;
#pragma nounroll
    for (int j = 0; j < 128; ++j) {
      const u32 u = 256 + 2 * j + a;
      if (j == 96) {
        v4u ml;
#pragma nounroll
        for (u32 spin = 0; spin < SPIN_CAP; ++spin) {
          CFENCE();
          ml = __builtin_amdgcn_raw_buffer_load_b128(rs, mlbase + (lane & 3) * 16, 0, SC1);
          if (__all(ml.y == tag && ml.w == tag)) break;
        }
        float M = uf(ml.x); M = fmaxf(M, dppf<XOR1>(M)); M = fmaxf(M, dppf<XOR2>(M));
        float L = uf(ml.z) * __builtin_amdgcn_exp2f((uf(ml.x) - M) * LOG2E);
        L += dppf<XOR1>(L); L += dppf<XOR2>(L);
        scale = __builtin_amdgcn_exp2f((mw - M) * LOG2E) / L;
      }
      const v4u p4 = *lp<v4u>(sc + j * 64 + g * 16);
      v4u bf[4];
#pragma unroll
      for (int i = 0; i < 4; ++i) bf[i] = v4u{p4[i] & msk[0], p4[i] & msk[1], p4[i] & msk[2], p4[i] & msk[3]};
      asm volatile("" : "+v"(bf[0]), "+v"(bf[1]), "+v"(bf[2]), "+v"(bf[3]) :: "memory");
#pragma nounroll
      for (u32 spin = 0; landed <= u && spin < SPIN_CAP * 8; ++spin) landed = lds_poll(C_LANDED);
      CFENCE();
      const u32 ad = ((u & 31) << 12) + lane * 16;
      v4u v0 = *lp<v4u>(ad), v1 = *lp<v4u>(ad + 1024), v2 = *lp<v4u>(ad + 2048), v3 = *lp<v4u>(ad + 3072);
      acc = mfma32(v0, bf[0], acc); acc = mfma32(v1, bf[1], acc); acc = mfma32(v2, bf[2], acc);
      asm volatile("" : "+v"(v3) :: "memory");
      *(volatile LAS u32*)(C_CONS + 4 * a) = 128 + j + 1;
      acc = mfma32(v3, bf[3], acc);
    }
    {
      const float o0 = acc[0] * scale, o1 = acc[1] * scale, o2 = acc[2] * scale, o3 = acc[3] * scale;
      const u32 h0 = pk(o0, o1), h1 = pk(o2, o3);
      const u32 l0 = pk(o0 - blo(h0), o1 - bhi(h0)), l1 = pk(o2 - blo(h1), o3 - bhi(h1));
      const bool lo = n >= 8;
      *lp<v2u>(L_OA + (2 * a + (n >> 3)) * 320 + (g * 8 + (n & 7)) * 8) = v2u{lo ? l0 : h0, lo ? l1 : h1};
    }
    asm volatile("s_waitcnt lgkmcnt(0)" ::: "memory");
    __builtin_amdgcn_s_barrier();
    if (a == 0) STAMP(4);
    return;
  }

  const int w = wave - 3;
  const bool has_mod = idx < 24;
  const int mcol = (idx >> 2) * 512 + 64 * xcd + 16 * (idx & 3);
  if (has_mod) {
    const char* wm = wmod + (size_t)(64 * w) * 6144 + mcol * 2;
    const u32 vo = (lane >> 1) * 6144 + (lane & 1) * 16, vo2 = vo + 32 * 6144;
    const u32 m0a = L_SC + w * 2048, m0b = m0a + 1024;
    asm volatile("s_mov_b32 m0, %3\n s_nop 0\n global_load_lds_dwordx4 %0, %2 nt\n s_mov_b32 m0, %4\n s_nop 0\n global_load_lds_dwordx4 %1, %2 nt"
                 :: "v"(vo), "v"(vo2), "s"(wm), "s"(m0a), "s"(m0b) : "memory");
  }
  v2u cv = ldg<v2u>(cnd + ((n >> 2) * 512 + 64 * w + 16 * (n & 3) + 4 * g) * 2);
  u32 tag = __builtin_amdgcn_raw_buffer_load_b32(rs, W_EPOCH, 0, SC1);
  const v4u xrow = ldg<v4u>(x + (b * 512 + 8 * lane) * 2);
  const int k0 = 64 * w + 32 * (n & 1) + 8 * g;
  const v4u xk = ldg<v4u>(x + (b * 512 + k0) * 2);
  SBAR();
  v4u wr[16];
  {
    const __amdgpu_buffer_rsrc_t rq = __builtin_amdgcn_make_buffer_rsrc((void*)(wq + (size_t)(64 * w) * 8192 + h * 256), 0, 64 * 8192, 0x00020000);
    const int vo = 8 * g * 8192 + n * 16;
#pragma unroll
    for (int i = 0; i < 16; ++i) wr[i] = __builtin_amdgcn_raw_buffer_load_b128(rq, vo, ((i >> 3) * 32 + (i & 7)) * 8192, NT);
  }
  SBAR();
  __builtin_amdgcn_s_barrier();
  asm volatile("" : "+v"(cv), "+v"(tag) :: "memory");
  tag = (u32)__builtin_amdgcn_readfirstlane((int)tag) + 1u;
  if (w == 0) STAMP(8);
  if (has_mod) {
    float sv[4] = {blo(cv.x), bhi(cv.x), blo(cv.y), bhi(cv.y)};
#pragma unroll
    for (int e = 0; e < 4; ++e) sv[e] = sv[e] * __builtin_amdgcn_rcpf(1.f + __builtin_amdgcn_exp2f(-sv[e] * LOG2E));
    const v2u sa = {pk(sv[0], sv[1]), pk(sv[2], sv[3])};
    float part = 0.f;
#pragma unroll
    for (int e = 0; e < 4; ++e) {
      const s4 bt = __builtin_amdgcn_ds_read_tr16_b64_v4i16(lp<s4>(L_SC + w * 2048 + e * 512 + lane * 8));
      part += mfma16(sa, __builtin_bit_cast(v2u, bt), zero4)[e];
    }
    *lp<float>(L_MODRED + w * 256 + lane * 4) = part;
    u32 old = 0;
    CFENCE();
    if (lane == 0) old = __hip_atomic_fetch_add(lp<u32>(C_MODCNT), 1u, __ATOMIC_RELAXED, __HIP_MEMORY_SCOPE_WORKGROUP);
    if (__builtin_amdgcn_readfirstlane((int)old) == 7) {
      CFENCE();
      float s = 0.f;
#pragma unroll
      for (int ww = 0; ww < 8; ++ww) s += *lp<float>(L_MODRED + ww * 256 + lane * 4);
      const u32 pr = pk(s, dppf<XOR1>(s));
      if (!(n & 1)) __builtin_amdgcn_raw_buffer_store_b64(v2u{pr, tag}, rs, W_MOD + (g * 1536 + ((mcol + n) >> 1)) * 8, 0, SC1);
      STAMP(9);
    }
  }
  float nrm[8];
  {
    float v[8] = {blo(xrow.x), bhi(xrow.x), blo(xrow.y), bhi(xrow.y), blo(xrow.z), bhi(xrow.z), blo(xrow.w), bhi(xrow.w)};
    const float mean = wave_sum(((v[0] + v[1]) + (v[2] + v[3])) + ((v[4] + v[5]) + (v[6] + v[7]))) * (1.f / 512.f);
    float q = 0.f;
#pragma unroll
    for (int e = 0; e < 8; ++e) { const float d = v[e] - mean; q = fmaf(d, d, q); }
    const float var = wave_sum(q) * (1.f / 512.f);
    const float mb = rbf(mean), rsd = __builtin_amdgcn_rcpf(rbf(__builtin_amdgcn_sqrtf(rbf(rbf(var) + 1e-5f))));
    const float u[8] = {blo(xk.x), bhi(xk.x), blo(xk.y), bhi(xk.y), blo(xk.z), bhi(xk.z), blo(xk.w), bhi(xk.w)};
#pragma unroll
    for (int e = 0; e < 4; ++e) {
      const u32 d = pk(u[2 * e] - mb, u[2 * e + 1] - mb), r = pk(blo(d) * rsd, bhi(d) * rsd);
      nrm[2 * e] = blo(r); nrm[2 * e + 1] = bhi(r);
    }
  }
  {
    v4u fq[2][8];
#pragma unroll
    for (int s = 0; s < 2; ++s)
#pragma unroll
      for (int p = 0; p < 4; ++p)
#pragma unroll
        for (int q = 0; q < 4; ++q) {
          fq[s][2 * p][q] = perm_lo(wr[8 * s + 2 * q][p], wr[8 * s + 2 * q + 1][p]);
          fq[s][2 * p + 1][q] = perm_hi(wr[8 * s + 2 * q][p], wr[8 * s + 2 * q + 1][p]);
        }
    PIN4(fq[0]); PIN4(fq[0] + 4); PIN4(fq[1]); PIN4(fq[1] + 4); PIN4(nrm); PIN4(nrm + 4);
    if (w == 0) STAMP(10);
    v4u s0, s1, c0, c1;
    const int mo = W_MOD + (b * 1536 + (k0 >> 1)) * 8;
#pragma nounroll
    for (u32 spin = 0; spin < SPIN_CAP; ++spin) {
      CFENCE();
      s0 = __builtin_amdgcn_raw_buffer_load_b128(rs, mo, 0, SC1);
      s1 = __builtin_amdgcn_raw_buffer_load_b128(rs, mo + 16, 0, SC1);
      c0 = __builtin_amdgcn_raw_buffer_load_b128(rs, mo + 2048, 0, SC1);
      c1 = __builtin_amdgcn_raw_buffer_load_b128(rs, mo + 2064, 0, SC1);
      if (__all(s0.y == tag && s0.w == tag && s1.y == tag && s1.w == tag && c0.y == tag && c0.w == tag && c1.y == tag && c1.w == tag)) break;
    }
    if (w == 0) STAMP(11);
    const u32 sp[4] = {s0.x, s0.z, s1.x, s1.z}, cp[4] = {c0.x, c0.z, c1.x, c1.z};
    v4u ha;
#pragma unroll
    for (int e = 0; e < 4; ++e) {
      const u32 c1p = pk(1.f + blo(cp[e]), 1.f + bhi(cp[e]));
      const u32 t = pk(nrm[2 * e] * blo(c1p), nrm[2 * e + 1] * bhi(c1p));
      ha[e] = pk(blo(t) + blo(sp[e]), bhi(t) + bhi(sp[e]));
    }
    f4 qp[2];
#pragma unroll
    for (int t = 0; t < 8; ++t) qp[t >> 2][t & 3] = mfma32(ha, fq[0][t], zero4)[0] + mfma32(ha, fq[1][t], zero4)[1];
    if (g < 2) *lp<f4>(L_QRED + w * 512 + n * 32 + g * 16) = g ? qp[1] : qp[0];
    u32 old = 0;
    CFENCE();
    if (lane == 0) old = __hip_atomic_fetch_add(lp<u32>(C_QCNT), 1u, __ATOMIC_RELAXED, __HIP_MEMORY_SCOPE_WORKGROUP);
    if (__builtin_amdgcn_readfirstlane((int)old) == 7) {
      CFENCE();
      f2 s = {0.f, 0.f};
#pragma unroll
      for (int ww = 0; ww < 8; ++ww) s += *lp<f2>(L_QRED + ww * 512 + lane * 8);
      *lp<u32>(L_QV + lane * 4) = pk(s.x, s.y);
      *(volatile LAS u32*)C_TAG = tag;
      CFENCE();
      *(volatile LAS u32*)C_QFLAG = 1u;
      if (lane == 0) __builtin_amdgcn_raw_buffer_store_b64(v2u{tag, tag}, rs, W_HELLO + blockIdx.x * 8, 0, SC1);
      STAMP(12);
    }
  }
  const int pid = 8 * xcd + 2 * hh + half;
  const bool has_ffn = (pid & 7) < 7;
  const int fs = 7 * xcd + (pid & 7);
  const int gs = 16 * g + ((n + 2 * w) & 15);
  u32 sbp[4], cbp[4];
  float xo[2], gao[2], gbo[2];
  {
    const int c0 = slice_col(gs, 0), par = c0 & 1;
    const int mo = W_MOD + (b * 1536 + 768 + (c0 >> 1)) * 8;
    const int oc = slice_col(pid, 2 * (lane & 3)), opar = oc & 1;
    const int oo = W_MOD + (b * 1536 + 512 + (oc >> 1)) * 8;
    v2u sv[8], cw[8], ga[2], gb[2];
#pragma nounroll
    for (u32 spin = 0; spin < SPIN_CAP; ++spin) {
      CFENCE();
      bool ok = true;
#pragma unroll
      for (int j = 0; j < 8; ++j) {
        sv[j] = __builtin_amdgcn_raw_buffer_load_b64(rs, mo + j * 16, 0, SC1);
        cw[j] = __builtin_amdgcn_raw_buffer_load_b64(rs, mo + 2048 + j * 16, 0, SC1);
      }
#pragma unroll
      for (int j = 0; j < 2; ++j) {
        ga[j] = __builtin_amdgcn_raw_buffer_load_b64(rs, oo + j * 16, 0, SC1);
        gb[j] = __builtin_amdgcn_raw_buffer_load_b64(rs, oo + 3 * 2048 + j * 16, 0, SC1);
      }
#pragma unroll
      for (int j = 0; j < 8; ++j) ok = ok && sv[j].y == tag && cw[j].y == tag;
      ok = ok && ga[0].y == tag && ga[1].y == tag && gb[0].y == tag && gb[1].y == tag;
      if (__all(ok)) break;
    }
#pragma unroll
    for (int e = 0; e < 4; ++e) {
      sbp[e] = par ? perm_hi(sv[2 * e].x, sv[2 * e + 1].x) : perm_lo(sv[2 * e].x, sv[2 * e + 1].x);
      const u32 cc = par ? perm_hi(cw[2 * e].x, cw[2 * e + 1].x) : perm_lo(cw[2 * e].x, cw[2 * e + 1].x);
      cbp[e] = pk(1.f + blo(cc), 1.f + bhi(cc));
    }
#pragma unroll
    for (int j = 0; j < 2; ++j) {
      gao[j] = opar ? bhi(ga[j].x) : blo(ga[j].x);
      gbo[j] = opar ? bhi(gb[j].x) : blo(gb[j].x);
      xo[j] = blo(ldg<u16>(x + (b * 512 + oc + 4 * j) * 2));
    }
    PIN4(sbp); PIN4(cbp);
    asm volatile("" : "+v"(xo[0]), "+v"(xo[1]), "+v"(gao[0]), "+v"(gao[1]), "+v"(gbo[0]), "+v"(gbo[1]));
  }
  SBAR();
  v4u fo[4][4], fg[2][4], fd[4];
  {
    const __amdgpu_buffer_rsrc_t ro = __builtin_amdgcn_make_buffer_rsrc((void*)(wo + (size_t)h * 131072), 0, 131072, 0x00020000);
    const int vo = 2 * g * 1024 + (64 * w + 4 * n) * 2;
#pragma unroll
    for (int s = 0; s < 4; ++s) {
      v2u r[8];
#pragma unroll
      for (int j = 0; j < 8; ++j) r[j] = __builtin_amdgcn_raw_buffer_load_b64(ro, vo + (j >> 2) * 1024, (32 * s + 8 * (j & 3)) * 1024, NT);
      tr8x4(r, fo[s]);
      PIN4(fo[s]);
      SBAR();
    }
    const int fc = has_ffn ? fs : 0;
    const char* pg = (n < 8 ? wg : wu) + (fc * 32 + 4 * (n & 7)) * 2;
#pragma unroll
    for (int s = 0; s < 2; ++s) {
      v2u r[8];
      const char* pr = pg + (size_t)slice_col(16 * g + 2 * w + s, 0) * (FF * 2);
#pragma unroll
      for (int j = 0; j < 8; ++j) r[j] = ldnt<v2u>(pr + j * (4 * FF * 2));
      tr8x4(r, fg[s]);
      PIN4(fg[s]);
      SBAR();
    }
    {
      v2u r[8];
      const __amdgpu_buffer_rsrc_t rd = __builtin_amdgcn_make_buffer_rsrc((void*)(wd + (size_t)fc * 32768), 0, 32768, 0x00020000);
      const int vd = 8 * g * 1024 + (64 * w + 4 * n) * 2;
#pragma unroll
      for (int j = 0; j < 8; ++j) r[j] = __builtin_amdgcn_raw_buffer_load_b64(rd, vd + (j & 3) * 1024, (j >> 2) * 4096, NT);
      tr8x4(r, fd);
      PIN4(fd);
      SBAR();
    }
  }
  if (blockIdx.x == 0 && w == 7) {
#pragma nounroll
    for (u32 spin = 0; spin < SPIN_CAP; ++spin) {
      CFENCE();
      const v4u h0 = __builtin_amdgcn_raw_buffer_load_b128(rs, W_HELLO + lane * 32, 0, SC1);
      const v4u h1 = __builtin_amdgcn_raw_buffer_load_b128(rs, W_HELLO + lane * 32 + 16, 0, SC1);
      if (__all(h0.x == tag && h0.y == tag && h0.z == tag && h0.w == tag && h1.x == tag && h1.y == tag && h1.z == tag && h1.w == tag)) break;
      __builtin_amdgcn_s_sleep(8);
    }
    if (lane == 0) __builtin_amdgcn_raw_buffer_store_b32(tag, rs, W_EPOCH, 0, SC1);
  }
  if (w == 0) STAMP(13);
  asm volatile("s_waitcnt vmcnt(0)" ::: "memory");
  __builtin_amdgcn_s_barrier();
  CFENCE();
  {
    f4 d[4] = {zero4, zero4, zero4, zero4};
#pragma unroll
    for (int s = 0; s < 4; ++s) {
      const v4u oa = *lp<v4u>(L_OA + (n & 3) * 320 + s * 64 + g * 16);
#pragma unroll
      for (int t = 0; t < 4; ++t) d[t] = mfma32(oa, fo[s][t], d[t]);
    }
    const f4 e = g == 0 ? d[0] : g == 1 ? d[1] : g == 2 ? d[2] : d[3];
    const float y = (e[0] + e[2]) + (e[1] + e[3]);
    __builtin_amdgcn_raw_buffer_store_b64(v2u{fu(y), tag}, rs, W_IN1 + (b * 64 + 8 * w + (lane >> 3)) * 4096 + pid * 64 + (lane & 7) * 8, 0, SC1);
  }
  if (w == 0) STAMP(14);
  float x1o[2] = {0.f, 0.f};
  if (w == 0) {
    v4u m[4];
    const int io = W_IN1 + (b * 64 + pid) * 4096 + lane * 16;
#pragma nounroll
    for (u32 spin = 0; spin < SPIN_CAP; ++spin) {
      CFENCE();
#pragma unroll
      for (int i = 0; i < 4; ++i) m[i] = __builtin_amdgcn_raw_buffer_load_b128(rs, io + i * 1024, 0, SC1);
      bool ok = true;
#pragma unroll
      for (int i = 0; i < 4; ++i) ok = ok && m[i].y == tag && m[i].w == tag;
      if (__all(ok)) break;
    }
    STAMP(15);
    float y0 = (uf(m[0].x) + uf(m[1].x)) + (uf(m[2].x) + uf(m[3].x)), y1 = (uf(m[0].z) + uf(m[1].z)) + (uf(m[2].z) + uf(m[3].z));
    y0 += dppf<ROR4>(y0); y1 += dppf<ROR4>(y1); y0 += dppf<ROR8>(y0); y1 += dppf<ROR8>(y1);
    y0 = xrow_sum(y0); y1 = xrow_sum(y1);
    const u32 yb = pk(y0, y1);
    const u32 t = pk(gao[0] * blo(yb), gao[1] * bhi(yb));
    const u32 xp = pk(xo[0] + blo(t), xo[1] + bhi(t));
    x1o[0] = blo(xp); x1o[1] = bhi(xp);
    if (lane < 4) __builtin_amdgcn_raw_buffer_store_b64(v2u{xp, tag}, rs, W_X1 + (b * 64 + pid) * 128 + lane * 8, 0, SC1);
    STAMP(16);
  }
  if (has_ffn) {
    __builtin_amdgcn_s_barrier();
    v4u m0, m1;
    const int xo_ = W_X1 + (b * 64 + gs) * 128;
#pragma nounroll
    for (u32 spin = 0; spin < SPIN_CAP; ++spin) {
      CFENCE();
      m0 = __builtin_amdgcn_raw_buffer_load_b128(rs, xo_, 0, SC1);
      m1 = __builtin_amdgcn_raw_buffer_load_b128(rs, xo_ + 16, 0, SC1);
      if (__all(m0.y == tag && m0.w == tag && m1.y == tag && m1.w == tag)) break;
    }
    if (w == 0) STAMP(17);
    float v[8] = {blo(m0.x), bhi(m0.x), blo(m0.z), bhi(m0.z), blo(m1.x), bhi(m1.x), blo(m1.z), bhi(m1.z)};
    const float sh = uf((u32)__builtin_amdgcn_readlane((int)fu(v[0]), (16 - 2 * w) & 15));
    float s1 = 0.f, s2 = 0.f;
#pragma unroll
    for (int e = 0; e < 8; ++e) { const float d = v[e] - sh; s1 += d; s2 = fmaf(d, d, s2); }
    s1 = wave_sum(s1) * (1.f / 512.f); s2 = wave_sum(s2) * (1.f / 512.f);
    const float mb = rbf(sh + s1), var = fmaxf(s2 - s1 * s1, 0.f);
    const float rsd = __builtin_amdgcn_rcpf(rbf(__builtin_amdgcn_sqrtf(rbf(rbf(var) + 1e-5f))));
    v4u ha;
#pragma unroll
    for (int e = 0; e < 4; ++e)
      ha[e] = pk(fmaf((v[2 * e] - mb) * rsd, blo(cbp[e]), blo(sbp[e])), fmaf((v[2 * e + 1] - mb) * rsd, bhi(cbp[e]), bhi(sbp[e])));
    f4 gp;
#pragma unroll
    for (int t = 0; t < 4; ++t) gp[t] = mfma32(ha, fg[0][t], zero4)[0] + mfma32(ha, fg[1][t], zero4)[1];
    if (g == 0) *lp<f4>(L_GURED + w * 256 + n * 16) = gp;
    asm volatile("s_waitcnt lgkmcnt(0)" ::: "memory");
    __builtin_amdgcn_s_barrier();
    CFENCE();
    float s = 0.f;
#pragma unroll
    for (int ww = 0; ww < 8; ++ww) s += *lp<float>(L_GURED + ww * 256 + ((n >> 3) * 32 + 8 * g + (n & 7)) * 4);
    s = rbf(s);
    const float up = dppf<ROR8>(s);
    const float z = s * fmaf(s * s, 0.044715f * 0.7978845608028654f * 2.f * LOG2E, 0.7978845608028654f * 2.f * LOG2E);
    const float ge = rbf(s * __builtin_amdgcn_rcpf(1.f + __builtin_amdgcn_exp2f(-z)));
    const float act = ge * up;
    const u32 ap = pk(act, dppf<XOR1>(act));
    const v4u aa = {dppu<0x150>(ap), dppu<0x152>(ap), dppu<0x154>(ap), dppu<0x156>(ap)};
    float dv[4];
#pragma unroll
    for (int t = 0; t < 4; ++t) dv[t] = mfma32(aa, fd[t], zero4)[0];
    const float y = g == 0 ? dv[0] : g == 1 ? dv[1] : g == 2 ? dv[2] : dv[3];
    __builtin_amdgcn_raw_buffer_store_b64(v2u{fu(y), tag}, rs, W_IN3 + (b * 64 + 8 * w + (lane >> 3)) * 4096 + fs * 64 + (lane & 7) * 8, 0, SC1);
    if (w == 0) STAMP(18);
  }
  if (w == 0) {
    v4u m[4];
    const int io = W_IN3 + (b * 64 + pid) * 4096 + lane * 16;
#pragma nounroll
    for (u32 spin = 0; spin < SPIN_CAP; ++spin) {
      CFENCE();
#pragma unroll
      for (int i = 0; i < 4; ++i) m[i] = __builtin_amdgcn_raw_buffer_load_b128(rs, io + i * 1024, 0, SC1);
      if (lane >= 32) m[3] = v4u{0u, tag, 0u, tag};
      bool ok = true;
#pragma unroll
      for (int i = 0; i < 4; ++i) ok = ok && m[i].y == tag && m[i].w == tag;
      if (__all(ok)) break;
    }
    STAMP(19);
    float y0 = (uf(m[0].x) + uf(m[1].x)) + (uf(m[2].x) + uf(m[3].x)), y1 = (uf(m[0].z) + uf(m[1].z)) + (uf(m[2].z) + uf(m[3].z));
    y0 += dppf<ROR4>(y0); y1 += dppf<ROR4>(y1); y0 += dppf<ROR8>(y0); y1 += dppf<ROR8>(y1);
    y0 = xrow_sum(y0); y1 = xrow_sum(y1);
    const u32 yb = pk(y0, y1);
    const u32 t = pk(gbo[0] * blo(yb), gbo[1] * bhi(yb));
    const u32 r = pk(x1o[0] + blo(t), x1o[1] + bhi(t));
    if (lane < 8) {
      const int oc = slice_col(pid, 2 * (lane & 3) + (lane >> 2));
      *(GAS u16*)(out + (b * 512 + oc) * 2) = (u16)(lane < 4 ? r : r >> 16);
    }
    STAMP(20);
  }
}

}

extern "C" void kernel_launch(void* const* d_in, const int* in_sizes, int n_in, void* d_out, int out_size, void* d_ws, size_t ws_size, hipStream_t stream) {
  hipLaunchKernelGGL(blk::fused_block, dim3(256), dim3(blk::NTHR), 0, stream,
                     (const char*)d_in[0], (const char*)d_in[1], (const char*)d_in[2], (const char*)d_in[3], (const char*)d_in[4], (const char*)d_in[6],
                     (const char*)d_in[7], (const char*)d_in[8], (const char*)d_in[9], (const char*)d_in[10], (char*)d_out, (char*)d_ws);
}
```

```cpp
#include <hip/hip_runtime.h>
#pragma clang diagnostic ignored "-Wint-to-pointer-cast"

#define WLEVELS "17,18,19,20,21,23,25,28"
#define LVL0 2
#define U0 5
#define TAU 1400
#define T00 250
#define GAIN 8
#define CLOSED 1
#define RDLAG 24
#define NPRE 8
#define CSLEEP 64
#define HSLEEP 16
#define QSLEEP 16
#define HDELAY 0
#define QDELAY 2000
#define WQFLY 3
#define PACEN 1
#define NOW 0
#define D1 1200
#define D2 500
#define D3 1100
#define STAMPS 0

namespace blk {

typedef unsigned short u16;
typedef unsigned u32;
typedef unsigned long long u64;
typedef u32 v4u __attribute__((ext_vector_type(4)));
typedef u32 v3u __attribute__((ext_vector_type(3)));
typedef u32 v2u __attribute__((ext_vector_type(2)));
typedef float f4 __attribute__((ext_vector_type(4)));
typedef float f2 __attribute__((ext_vector_type(2)));
typedef __bf16 bf8 __attribute__((ext_vector_type(8)));
typedef __bf16 bf2 __attribute__((ext_vector_type(2)));
typedef short s4 __attribute__((ext_vector_type(4)));
#define GAS __attribute__((address_space(1)))
#define LAS __attribute__((address_space(3)))

constexpr int NTHR = 768;
constexpr int FF = 1792;
constexpr int L_SC = 131072;
constexpr int L_MODRED = L_SC + 16384;
constexpr int L_GURED = L_MODRED + 8192;
constexpr int L_SBCB = L_GURED + 2048;
constexpr int L_OA = L_SBCB + 2048;
constexpr int L_QV = L_OA + 1280;
constexpr int L_CTRL = L_QV + 256;
constexpr int C_CONS = L_CTRL, C_PULL = L_CTRL + 8, C_MODCNT = L_CTRL + 12, C_PRE = L_CTRL + 16, C_QGOT = L_CTRL + 20, C_TAG = L_CTRL + 24;
constexpr int C_CTL = L_CTRL + 32;
constexpr int C_LANDED = L_CTRL + 48;
constexpr int L_STAMP = C_LANDED + 256;
constexpr int LDS_BYTES = L_STAMP + (STAMPS ? 256 : 0);
constexpr int W_EPOCH = 0, W_PROG = 4096, W_ML = 16384;
constexpr int W_MOD = 65536;
constexpr int W_Q = 131072;
constexpr int W_X1 = 196608;
constexpr int W_IN1 = 262144, W_IN3 = W_IN1 + (1 << 20);
constexpr int W_END = W_IN3 + (1 << 20), W_DBG = W_END;
constexpr int SC1 = 16, NT = 2;
constexpr u32 SPIN_CAP = 1u << 18;

__device__ __forceinline__ float blo(u32 u) { return __builtin_bit_cast(float, u << 16); }
__device__ __forceinline__ float bhi(u32 u) { return __builtin_bit_cast(float, u & 0xffff0000u); }
__device__ __forceinline__ u32 pk(float a, float b) { f2 p = {a, b}; return __builtin_bit_cast(u32, __builtin_convertvector(p, bf2)); }
__device__ __forceinline__ float rbf(float a) { return blo(pk(a, a)); }
__device__ __forceinline__ u32 fu(float a) { return __builtin_bit_cast(u32, a); }
__device__ __forceinline__ float uf(u32 a) { return __builtin_bit_cast(float, a); }
__device__ __forceinline__ f4 mfma32(v4u a, v4u b, f4 c) { return __builtin_amdgcn_mfma_f32_16x16x32_bf16(__builtin_bit_cast(bf8, a), __builtin_bit_cast(bf8, b), c, 0, 0, 0); }
__device__ __forceinline__ f4 mfma16(v2u a, v2u b, f4 c) { return __builtin_amdgcn_mfma_f32_16x16x16bf16_1k(__builtin_bit_cast(s4, a), __builtin_bit_cast(s4, b), c, 0, 0, 0); }
template <int CTRL> __device__ __forceinline__ u32 dppu(u32 x) { return (u32)__builtin_amdgcn_mov_dpp((int)x, CTRL, 0xf, 0xf, true); }
template <int CTRL> __device__ __forceinline__ float dppf(float x) { return uf(dppu<CTRL>(fu(x))); }
constexpr int XOR1 = 0xB1, XOR2 = 0x4E, HMIR = 0x141, ROR4 = 0x124, ROR8 = 0x128;
__device__ __forceinline__ float xrow_sum(float x) {
  auto s = __builtin_amdgcn_permlane16_swap(fu(x), fu(x), false, false);
  x = uf(s[0]) + uf(s[1]);
  auto t = __builtin_amdgcn_permlane32_swap(fu(x), fu(x), false, false);
  return uf(t[0]) + uf(t[1]);
}
__device__ __forceinline__ float xrow_max(float x) {
  auto s = __builtin_amdgcn_permlane16_swap(fu(x), fu(x), false, false);
  x = fmaxf(uf(s[0]), uf(s[1]));
  auto t = __builtin_amdgcn_permlane32_swap(fu(x), fu(x), false, false);
  return fmaxf(uf(t[0]), uf(t[1]));
}
__device__ __forceinline__ float wave_sum(float x) { x += dppf<XOR1>(x); x += dppf<XOR2>(x); x += dppf<HMIR>(x); x += dppf<ROR8>(x); return xrow_sum(x); }
__device__ __forceinline__ float wave_max(float x) { x = fmaxf(x, dppf<XOR1>(x)); x = fmaxf(x, dppf<XOR2>(x)); x = fmaxf(x, dppf<HMIR>(x)); x = fmaxf(x, dppf<ROR8>(x)); return xrow_max(x); }
__device__ __forceinline__ u32 perm_lo(u32 r0, u32 r1) { return __builtin_amdgcn_perm(r1, r0, 0x05040100u); }
__device__ __forceinline__ u32 perm_hi(u32 r0, u32 r1) { return __builtin_amdgcn_perm(r1, r0, 0x07060302u); }
#define CFENCE() asm volatile("" ::: "memory")
#define SBAR() __builtin_amdgcn_sched_barrier(0)
#define PIN4(a) asm volatile("" : "+v"((a)[0]), "+v"((a)[1]), "+v"((a)[2]), "+v"((a)[3]))
template <typename T> __device__ __forceinline__ LAS T* lp(u32 off) { return (LAS T*)off; }
__device__ __forceinline__ u32 lds_poll(u32 off) { return (u32)__builtin_amdgcn_readfirstlane((int)*(volatile LAS u32*)off); }
__device__ __forceinline__ void lds_add(u32 off, u32 v) {
  asm volatile("s_mov_b64 exec, 1\n ds_add_u32 %0, %1\n s_mov_b64 exec, -1" :: "v"(off), "v"(v) : "memory");
}
template <typename T> __device__ __forceinline__ T ldnt(const void* p) { return __builtin_nontemporal_load((const GAS T*)p); }
template <typename T> __device__ __forceinline__ T ldg(const void* p) { return *(const GAS T*)p; }
__device__ __forceinline__ void tr8x4(const v2u (&r)[8], v4u (&f)[4]) {
#pragma unroll
  for (int q = 0; q < 4; ++q) {
    f[0][q] = perm_lo(r[2 * q].x, r[2 * q + 1].x); f[1][q] = perm_hi(r[2 * q].x, r[2 * q + 1].x);
    f[2][q] = perm_lo(r[2 * q].y, r[2 * q + 1].y); f[3][q] = perm_hi(r[2 * q].y, r[2 * q + 1].y);
  }
}
__device__ __forceinline__ int slice_col(int s, int pos) { return 64 * (s >> 3) + 32 * (s & 1) + 4 * pos + ((s >> 1) & 3); }

#define STAMP(i) do { } while (0)

__device__ __forceinline__ int mod_off(int b, int c) { return W_MOD + (c >> 6) * 1024 + b * 256 + ((c & 63) >> 1) * 8; }
__device__ __forceinline__ float half_of(u32 v, int odd) { return odd ? bhi(v) : blo(v); }
__device__ __forceinline__ void delay(int cyc) {
  const u64 t0 = __builtin_amdgcn_s_memtime();
#pragma nounroll
  while ((int)(__builtin_amdgcn_s_memtime() - t0) < cyc) { }
}
template <int CYC> __device__ __forceinline__ void nap() {
#pragma unroll
  for (int i = 0; i < CYC / 8128; ++i) __builtin_amdgcn_s_sleep(127);
  if (CYC % 8128 >= 64) __builtin_amdgcn_s_sleep((CYC % 8128) / 64);
}
__device__ __forceinline__ u32 get_tag() {
  v2u t;
#pragma nounroll
  for (u32 spin = 0; spin < SPIN_CAP * 8; ++spin) { t = *(volatile LAS v2u*)C_TAG; if (__builtin_amdgcn_readfirstlane((int)t.y)) break; }
  return (u32)__builtin_amdgcn_readfirstlane((int)t.x);
}
__device__ __forceinline__ u32 inbox_sum(const v4u (&m)[4]) {
  float y0 = (uf(m[0].x) + uf(m[1].x)) + (uf(m[2].x) + uf(m[3].x)), y1 = (uf(m[0].z) + uf(m[1].z)) + (uf(m[2].z) + uf(m[3].z));
  y0 += dppf<ROR4>(y0); y1 += dppf<ROR4>(y1); y0 += dppf<ROR8>(y0); y1 += dppf<ROR8>(y1);
  return pk(xrow_sum(y0), xrow_sum(y1));
}
#define PACE(j) do { if (((j) % PACEN) == PACEN - 1) { asm volatile("s_waitcnt vmcnt(0)" ::: "memory"); SBAR(); } } while (0)
constexpr float LOG2E = 1.4426950408889634f;

template <bool PACED>
__device__ __forceinline__ void mod_tile(const char* cnd, const char* wmod, __amdgpu_buffer_rsrc_t rs, int cb, int w, int lane) {
  const int g = lane >> 4, n = lane & 15;
  const f4 zero4 = {0.f, 0.f, 0.f, 0.f};
  const v2u cv = ldnt<v2u>(cnd + ((n >> 2) * 512 + 64 * w + 16 * g + 4 * (n & 3)) * 2);
  const __amdgpu_buffer_rsrc_t rm = __builtin_amdgcn_make_buffer_rsrc((void*)(wmod + (size_t)(64 * w) * 6144 + cb * 128), 0, 64 * 6144, 0x00020000);
  v2u r[2][8];
#pragma unroll
  for (int j = 0; j < 16; ++j) { r[j >> 3][j & 7] = __builtin_amdgcn_raw_buffer_load_b64(rm, 16 * g * 6144 + n * 8, j * 6144, NT); if (PACED) PACE(j); }
  SBAR();
  u32 sp[2];
#pragma unroll
  for (int e = 0; e < 2; ++e) {
    const float c0 = blo(cv[e]), c1 = bhi(cv[e]);
    sp[e] = pk(c0 * __builtin_amdgcn_rcpf(1.f + __builtin_amdgcn_exp2f(-c0 * LOG2E)), c1 * __builtin_amdgcn_rcpf(1.f + __builtin_amdgcn_exp2f(-c1 * LOG2E)));
  }
  const v4u a0 = {dppu<0x00>(sp[0]), dppu<0x00>(sp[1]), dppu<0x55>(sp[0]), dppu<0x55>(sp[1])};
  const v4u a1 = {dppu<0xAA>(sp[0]), dppu<0xAA>(sp[1]), dppu<0xFF>(sp[0]), dppu<0xFF>(sp[1])};
  v4u f0[4], f1[4];
  tr8x4(r[0], f0); tr8x4(r[1], f1);
  f4 pt;
#pragma unroll
  for (int t = 0; t < 4; ++t) pt[t] = mfma32(a1, f1[t], mfma32(a0, f0[t], zero4))[0];
  *lp<f4>(L_MODRED + w * 1024 + lane * 16) = pt;
  u32 old;
  asm volatile("s_mov_b64 exec, 1\n ds_add_rtn_u32 %0, %1, %2\n s_mov_b64 exec, -1\n s_waitcnt lgkmcnt(0)" : "=&v"(old) : "v"(C_MODCNT), "v"(1u) : "memory");
  if ((__builtin_amdgcn_readfirstlane((int)old) & 7) == 7) {
    f4 p[8];
#pragma unroll
    for (int ww = 0; ww < 8; ++ww) p[ww] = *lp<f4>(L_MODRED + ww * 1024 + lane * 16);
    const f4 v = ((p[0] + p[1]) + (p[2] + p[3])) + ((p[4] + p[5]) + (p[6] + p[7]));
    const u32 tag = get_tag();
    __builtin_amdgcn_raw_buffer_store_b128(v4u{pk(v[0], v[1]), tag, pk(v[2], v[3]), tag}, rs, lane * 16 + W_MOD + cb * 1024, 0, SC1);
    if (!PACED) STAMP(9);
  }
}

__global__ void __launch_bounds__(NTHR) fused_block(const char* x, const char* kc, const char* vc, const char* wq, const char* wo, const char* wg,
                                                    const char* wu, const char* wd, const char* cnd, const char* wmod, char* out, char* ws) {
  __shared__ __attribute__((aligned(1024))) char lds_mem[LDS_BYTES];
  const int lane = threadIdx.x & 63, wave = __builtin_amdgcn_readfirstlane(threadIdx.x >> 6);
  const int xcd = blockIdx.x & 7, idx = blockIdx.x >> 3;
  const int half = idx & 1, hh = (idx >> 1) & 3, b = idx >> 3;
  const int h = xcd + 8 * hh;
  const int pid = 8 * xcd + (idx & 7);
  const bool has_ffn = (idx & 7) < 7;
  const bool first_mod = !has_ffn && b < 2;
  const int g = lane >> 4, n = lane & 15;
  const f4 zero4 = {0.f, 0.f, 0.f, 0.f};
  if (threadIdx.x < (STAMPS ? 140 : 16)) *lp<u32>(L_CTRL + 4 * threadIdx.x) = threadIdx.x == 8 ? (32u << 12) | (LVL0 * 128) : 0u;
  asm volatile("s_waitcnt lgkmcnt(0)" :: "v"(lds_mem), "s"(kc), "s"(ws) : "memory");
  __builtin_amdgcn_s_barrier();

  if (wave == 0) {
    __builtin_amdgcn_s_setprio(3);
    constexpr u32 HOLES = 0xA000u;
    const size_t reg_off = ((size_t)(b * 32 + h) << 21) + half * 32768 - HOLES;
    u32 o0 = g * 256 + ((n ^ g) << 4), o1 = o0 ^ 64, o2 = (o0 ^ 128) + 6144, o3 = (o0 ^ 192) + 6144;
    const u32 va = C_LANDED + lane * 4;
    const u64 kb = (u64)(kc + reg_off), vb = (u64)(vc + reg_off);
    v4u rsrc = {(u32)kb, (u32)(kb >> 32) & 0xffffu, (2u << 20) + 0x2000u, 0x00020000u};
    STAMP(5);
    if (first_mod) __builtin_amdgcn_s_sleep(CSLEEP);
    int pub = -7;
    u32 ctl = (32u << 12) | (LVL0 * 128);
#pragma nounroll
    for (u32 end = 1u << 20; end <= (2u << 20); end += 1u << 20) {
      u32 off = end - (1u << 20), src = HOLES, spin = SPIN_CAP * 8, blk, tmp, pcl, pch;
#define LD(v, o) "s_waitcnt vmcnt(\\w)\n buffer_load_dwordx4 %[" #v "], %[rs], %[src] offen offset:" #o " nt lds\n"
      asm volatile(
        "s_getpc_b64 vcc\n"
        ".Lldr_pc:\n s_add_u32 %[pcl], vcc_lo, .Lldr_copies-.Lldr_pc\n s_addc_u32 %[pch], vcc_hi, 0\n"
        ".Lldr_next:\n s_waitcnt lgkmcnt(4)\n"
        ".Lldr_dec:\n v_readfirstlane_b32 %[tmp], %[ctl]\n ds_read_b32 %[ctl], %[ca]\n s_add_u32 %[blk], %[off], 0x4000\n"
        "s_cmp_gt_u32 %[blk], %[tmp]\n s_cbranch_scc1 .Lldr_stall\n"
        "s_and_b32 %[tmp], %[tmp], 0x380\n s_add_u32 vcc_lo, %[pcl], %[tmp]\n s_addc_u32 vcc_hi, %[pch], 0\n s_setpc_b64 vcc\n"
        ".Lldr_stall:\n s_waitcnt lgkmcnt(0)\n s_sub_u32 %[spin], %[spin], 1\n s_cbranch_scc0 .Lldr_dec\n s_branch .Lldr_exit\n"
        ".p2align 7\n.Lldr_copies:\n"
        ".irp w," WLEVELS "\n"
        "s_cbranch_execz 2f\n"
        "1:\n s_and_b32 m0, %[off], 0x1ffff\n" LD(v0, 0) "s_add_u32 %[off], %[off], 0x1000\n" LD(v1, 1024) LD(v2, 2048) LD(v3, 3072)
        "ds_write_b32 %[va], %[pub]\n s_add_u32 %[src], %[src], 0x800\n s_or_b32 %[src], %[src], 0xa000\n v_add_u32 %[pub], 1, %[pub]\n"
        "s_cmp_lt_u32 %[off], %[blk]\n s_cbranch_scc1 1b\n s_cmp_lt_u32 %[off], %[end]\n s_cbranch_scc1 .Lldr_next\n s_branch .Lldr_exit\n"
        ".p2align 7\n2:\n.endr\n"
        "s_branch .Lwarm_ret0\n"
        ".Lldr_exit:\n s_waitcnt lgkmcnt(0)"
        : [off] "+&s"(off), [src] "+&s"(src), [spin] "+&s"(spin), [blk] "=&s"(blk), [tmp] "=&s"(tmp), [pcl] "=&s"(pcl), [pch] "=&s"(pch), [pub] "+&v"(pub), [ctl] "+&v"(ctl)
        : [v0] "v"(o0), [v1] "v"(o1), [v2] "v"(o2), [v3] "v"(o3), [rs] "s"(rsrc), [end] "s"(end), [va] "v"(va), [ca] "v"(C_CTL) : "memory", "scc", "vcc");
      rsrc.x = (u32)vb; rsrc.y = (u32)(vb >> 32) & 0xffffu;
      o0 = o1 = lane * 16; o2 = o3 = lane * 16 + 6144;
    }
#pragma unroll
    for (int k = 6; k >= 0; --k) {
      const u32 pb = 512 - k;
      asm volatile("s_waitcnt vmcnt(%2)\n ds_write_b32 %0, %1" :: "v"(va), "v"(pb), "i"(4 * k) : "memory");
    }
    STAMP(1);
    return;
  }

  const __amdgpu_buffer_rsrc_t rs = __builtin_amdgcn_make_buffer_rsrc((void*)ws, 0, W_END, 0x00020000);

  if (wave <= 2) {
    const int a = wave - 1;
    u32 tag;
    if (a == 0) {
      const int beta = n >> 2, kq = 16 * g + 4 * (n & 3);
      v2u xr[8];
#pragma unroll
      for (int i = 0; i < 8; ++i) xr[i] = ldnt<v2u>(x + (beta * 512 + 64 * i + kq) * 2);
      SBAR();
      if (first_mod) {
#pragma nounroll
        for (u32 spin = 0; lds_poll(C_MODCNT) == 0u && spin < SPIN_CAP * 8; ++spin) __builtin_amdgcn_s_sleep(2);
      }
      {
        const char* wt = wq + (h * 128 + (2 * b + half) * 16) * 2;
        const u32 vo = (64 * (lane >> 5) + 16 * ((lane >> 3) & 3) + ((lane >> 1) & 3)) * 8192 + (lane & 1) * 16;
#pragma nounroll
        for (int p = 0; p < 16; ++p)
          asm volatile("s_mov_b32 m0, %2\n s_waitcnt vmcnt(%3)\n global_load_lds_dwordx4 %0, %1 nt"
                       :: "v"(vo), "s"(wt + (size_t)(128 * (p >> 2) + 4 * (p & 3)) * 8192), "s"(L_SC + p * 1024), "i"(WQFLY - 1) : "memory");
      }
      SBAR();
      {
        float s = 0.f;
#pragma unroll
        for (int i = 0; i < 8; ++i) s += (blo(xr[i].x) + bhi(xr[i].x)) + (blo(xr[i].y) + bhi(xr[i].y));
        s += dppf<XOR1>(s); s += dppf<XOR2>(s);
        const float mean = xrow_sum(s) * (1.f / 512.f);
        float q = 0.f;
#pragma unroll
        for (int i = 0; i < 8; ++i)
#pragma unroll
          for (int e = 0; e < 2; ++e) { const float d0 = blo(xr[i][e]) - mean, d1 = bhi(xr[i][e]) - mean; q = fmaf(d0, d0, q); q = fmaf(d1, d1, q); }
        q += dppf<XOR1>(q); q += dppf<XOR2>(q);
        const float mb = rbf(mean), rsd = __builtin_amdgcn_rcpf(rbf(__builtin_amdgcn_sqrtf(rbf(rbf(xrow_sum(q) * (1.f / 512.f)) + 1e-5f))));
#pragma unroll
        for (int i = 0; i < 8; ++i)
#pragma unroll
          for (int e = 0; e < 2; ++e) { const u32 d = pk(blo(xr[i][e]) - mb, bhi(xr[i][e]) - mb); xr[i][e] = pk(blo(d) * rsd, bhi(d) * rsd); }
        PIN4(xr); PIN4(xr + 4);
      }
      STAMP(10);
      tag = get_tag();
      nap<HDELAY>();
      u32 ha[8][2];
#pragma nounroll
      for (u32 spin = 0; spin < SPIN_CAP; ++spin) {
        CFENCE();
        bool go = spin == 0;
        if (!go) go = __all(__builtin_amdgcn_raw_buffer_load_b64(rs, W_MOD + n * 1024 + 1016, 0, SC1)[1] == tag);
        if (go) {
          u32 bad = 0;
#pragma unroll
          for (int hf = 0; hf < 2; ++hf) {
            v4u sa[4], ca[4];
#pragma unroll
            for (int i = 0; i < 4; ++i) {
              sa[i] = __builtin_amdgcn_raw_buffer_load_b128(rs, beta * 256 + kq * 4 + i * 1024, W_MOD + hf * 4096, SC1);
              ca[i] = __builtin_amdgcn_raw_buffer_load_b128(rs, beta * 256 + kq * 4 + i * 1024, W_MOD + 8192 + hf * 4096, SC1);
            }
#pragma unroll
            for (int i = 0; i < 4; ++i) {
              bad |= (sa[i].y ^ tag) | (sa[i].w ^ tag) | (ca[i].y ^ tag) | (ca[i].w ^ tag);
#pragma unroll
              for (int e = 0; e < 2; ++e) {
                const u32 c1 = pk(1.f + blo(ca[i][2 * e]), 1.f + bhi(ca[i][2 * e]));
                const u32 t = pk(blo(xr[4 * hf + i][e]) * blo(c1), bhi(xr[4 * hf + i][e]) * bhi(c1));
                ha[4 * hf + i][e] = pk(blo(t) + blo(sa[i][2 * e]), bhi(t) + bhi(sa[i][2 * e]));
              }
            }
          }
          if (__all(bad == 0)) break;
        }
        __builtin_amdgcn_s_sleep(HSLEEP);
      }
      asm volatile("s_waitcnt vmcnt(0)" ::: "memory");
      STAMP(11);
      f4 acc[4] = {zero4, zero4, zero4, zero4};
#pragma unroll
      for (int p = 0; p < 16; ++p) {
        const v2u lo = __builtin_bit_cast(v2u, __builtin_amdgcn_ds_read_tr16_b64_v4i16(lp<s4>(L_SC + p * 1024 + lane * 8)));
        const v2u hi = __builtin_bit_cast(v2u, __builtin_amdgcn_ds_read_tr16_b64_v4i16(lp<s4>(L_SC + p * 1024 + 512 + lane * 8)));
        const int sg = p >> 2;
        acc[p & 3] = mfma32(v4u{ha[2 * sg][0], ha[2 * sg][1], ha[2 * sg + 1][0], ha[2 * sg + 1][1]}, v4u{lo.x, lo.y, hi.x, hi.y}, acc[p & 3]);
      }
      const float qv = (acc[0][0] + acc[1][1]) + (acc[2][2] + acc[3][3]);
      const u32 pr = pk(qv, dppf<XOR1>(qv));
      if (!(n & 1)) __builtin_amdgcn_raw_buffer_store_b64(v2u{pr, tag}, rs, W_Q + (h * 4 + g) * 512 + (2 * b + half) * 64 + (n >> 1) * 8, 0, SC1);
      STAMP(12);
      nap<QDELAY>();
      v2u m;
#pragma nounroll
      for (u32 spin = 0; spin < SPIN_CAP; ++spin) {
        CFENCE();
        m = __builtin_amdgcn_raw_buffer_load_b64(rs, lane * 8, W_Q + (h * 4 + b) * 512, SC1);
        if (__all(m.y == tag)) break;
        __builtin_amdgcn_s_sleep(QSLEEP);
      }
      *lp<u32>(L_QV + lane * 4) = m.x;
      CFENCE();
      *(volatile LAS u32*)C_QGOT = 1u;
    } else {
#pragma nounroll
      for (u32 spin = 0; lds_poll(C_QGOT) == 0u && spin < SPIN_CAP * 8; ++spin) __builtin_amdgcn_s_sleep(4);
      tag = get_tag();
    }
    CFENCE();
    v4u qa[4];
#pragma unroll
    for (int s = 0; s < 4; ++s) qa[s] = *lp<v4u>(L_QV + 64 * s + 16 * g);
    if (a == 0) STAMP(2);
    const u32 sc = L_SC + a * 8192;
    int landed = 0;
    const u32 kofs = n * 256 + ((g ^ n) << 4);
    const u32 spos = sc + ((n & 3) * 4 + (n >> 2)) * 4;
#pragma nounroll
    for (int j = 4 * NPRE; j < 128; ++j) {
      const int u = 2 * j + a;
#pragma nounroll
      for (u32 spin = 0; landed <= u && spin < SPIN_CAP * 8; ++spin) landed = (int)lds_poll(C_LANDED);
      CFENCE();
      const u32 ad = ((u & 31) << 12) + kofs;
      v4u k0 = *lp<v4u>(ad), k1 = *lp<v4u>(ad ^ 64), k2 = *lp<v4u>(ad ^ 128), k3 = *lp<v4u>(ad ^ 192);
      asm volatile("" : "+v"(k0), "+v"(k1), "+v"(k2), "+v"(k3) :: "memory");
      *(volatile LAS u32*)(C_CONS + 4 * a) = j + 1;
      f4 d = mfma32(qa[0], k0, zero4);
      d = mfma32(qa[1], k1, d); d = mfma32(qa[2], k2, d); d = mfma32(qa[3], k3, d);
      *lp<float>(spos + j * 64) = d[0];
    }
#pragma nounroll
    for (u32 spin = 0; lds_poll(C_PRE) != 8u && spin < SPIN_CAP * 8; ++spin) { }
    CFENCE();
    if (a == 0) STAMP(3);
    float mw, lw;
    {
      f4 s[8];
      float mx = -INFINITY;
#pragma unroll
      for (int t = 0; t < 8; ++t) {
        s[t] = *lp<f4>(sc + lane * 16 + t * 1024);
        const u32 p0 = pk(s[t][0], s[t][1]), p1 = pk(s[t][2], s[t][3]);
        const u32 r0 = pk(blo(p0) * 0.08838834764831845f, bhi(p0) * 0.08838834764831845f), r1 = pk(blo(p1) * 0.08838834764831845f, bhi(p1) * 0.08838834764831845f);
        s[t] = f4{blo(r0), bhi(r0), blo(r1), bhi(r1)};
        mx = fmaxf(fmaxf(mx, fmaxf(s[t][0], s[t][1])), fmaxf(s[t][2], s[t][3]));
      }
      mw = wave_max(mx);
      float sum = 0.f;
#pragma unroll
      for (int t = 0; t < 8; ++t) {
        v4u pp;
#pragma unroll
        for (int e = 0; e < 4; ++e) { const float p = __builtin_amdgcn_exp2f((s[t][e] - mw) * LOG2E); sum += p; pp[e] = pk(p, p); }
        *lp<v4u>(sc + lane * 16 + t * 1024) = pp;
      }
      lw = wave_sum(sum);
    }
    const int mlbase = W_ML + (b * 32 + h) * 64;
    if (lane == 0) __builtin_amdgcn_raw_buffer_store_b128(v4u{fu(mw), tag, fu(lw), tag}, rs, mlbase + (half * 2 + a) * 16, 0, SC1);
    u32 msk[4];
#pragma unroll
    for (int q = 0; q < 4; ++q) msk[q] = ((n & 7) >> 1) == q ? ((n & 1) ? 0xffff0000u : 0x0000ffffu) : 0u;
    f4 acc = zero4;
    float scale = 0.f, ga0 = 0.f, ga1 = 0.f, gb0 = 0.f, gb1 = 0.f;
    u32 xo0 = 0, xo1 = 0;
    const int oc = slice_col(pid, 2 * (lane & 3));
#pragma nounroll
    for (int j = 0; j < 128; ++j) {
      const int u = 256 + 2 * j + a;
      if (j == 100) {
        v4u ml; v2u gm[4];
        const int go = mod_off(b, oc);
#pragma nounroll
        for (u32 spin = 0; spin < SPIN_CAP; ++spin) {
          CFENCE();
          ml = __builtin_amdgcn_raw_buffer_load_b128(rs, mlbase + (lane & 3) * 16, 0, SC1);
          u32 bad = (ml.y ^ tag) | (ml.w ^ tag);
#pragma unroll
          for (int i = 0; i < 4; ++i) {
            gm[i] = __builtin_amdgcn_raw_buffer_load_b64(rs, go + (i & 1) * 16, (i & 2) ? 5 * 8192 : 2 * 8192, SC1);
            bad |= gm[i].y ^ tag;
          }
          xo0 = ldnt<u16>(x + (b * 512 + oc) * 2); xo1 = ldnt<u16>(x + (b * 512 + oc + 4) * 2);
          if (__all(bad == 0)) break;
        }
        ga0 = half_of(gm[0].x, oc & 1); ga1 = half_of(gm[1].x, oc & 1); gb0 = half_of(gm[2].x, oc & 1); gb1 = half_of(gm[3].x, oc & 1);
        float M = uf(ml.x); M = fmaxf(M, dppf<XOR1>(M)); M = fmaxf(M, dppf<XOR2>(M));
        float L = uf(ml.z) * __builtin_amdgcn_exp2f((uf(ml.x) - M) * LOG2E);
        L += dppf<XOR1>(L); L += dppf<XOR2>(L);
        scale = __builtin_amdgcn_exp2f((mw - M) * LOG2E) / L;
        asm volatile("" : "+v"(xo0), "+v"(xo1));
      }
      const v4u p4 = *lp<v4u>(sc + j * 64 + g * 16);
      v4u bf[4];
#pragma unroll
      for (int i = 0; i < 4; ++i) bf[i] = v4u{p4[i] & msk[0], p4[i] & msk[1], p4[i] & msk[2], p4[i] & msk[3]};
      asm volatile("" : "+v"(bf[0]), "+v"(bf[1]), "+v"(bf[2]), "+v"(bf[3]) :: "memory");
#pragma nounroll
      for (u32 spin = 0; landed <= u && spin < SPIN_CAP * 8; ++spin) landed = (int)lds_poll(C_LANDED);
      CFENCE();
      const u32 ad = ((u & 31) << 12) + lane * 16;
      v4u v0 = *lp<v4u>(ad), v1 = *lp<v4u>(ad + 1024), v2 = *lp<v4u>(ad + 2048), v3 = *lp<v4u>(ad + 3072);
      SBAR();
      acc = mfma32(v0, bf[0], acc); acc = mfma32(v1, bf[1], acc); acc = mfma32(v2, bf[2], acc);
      asm volatile("" : "+v"(v3) :: "memory");
      *(volatile LAS u32*)(C_CONS + 4 * a) = 128 + j + 1;
      acc = mfma32(v3, bf[3], acc);
    }
    {
      const float o0 = acc[0] * scale, o1 = acc[1] * scale, o2 = acc[2] * scale, o3 = acc[3] * scale;
      const u32 h0 = pk(o0, o1), h1 = pk(o2, o3);
      const u32 l0 = pk(o0 - blo(h0), o1 - bhi(h0)), l1 = pk(o2 - blo(h1), o3 - bhi(h1));
      const bool lo = n >= 8;
      *lp<v2u>(L_OA + (2 * a + (n >> 3)) * 320 + (g * 8 + (n & 7)) * 8) = v2u{lo ? l0 : h0, lo ? l1 : h1};
    }
    asm volatile("s_waitcnt lgkmcnt(0)" ::: "memory");
    __builtin_amdgcn_s_barrier();
    if (a == 1) return;
    STAMP(4);
    v4u m[4];
    const int io = (b * 64 + pid) * 4096 + lane * 16;
    delay(D1);
#pragma nounroll
    for (u32 spin = 0; spin < SPIN_CAP; ++spin) {
      CFENCE();
      u32 bad = 0;
#pragma unroll
      for (int i = 0; i < 4; ++i) m[i] = __builtin_amdgcn_raw_buffer_load_b128(rs, io + i * 1024, W_IN1, SC1);
#pragma unroll
      for (int i = 0; i < 4; ++i) bad |= (m[i].y ^ tag) | (m[i].w ^ tag);
      if (__all(bad == 0)) break;
    }
    STAMP(15);
    u32 yb = inbox_sum(m);
    u32 t = pk(ga0 * blo(yb), ga1 * bhi(yb));
    const u32 x1 = pk(blo(xo0) + blo(t), blo(xo1) + bhi(t));
    if (lane < 32) __builtin_amdgcn_raw_buffer_store_b64(v2u{x1, tag}, rs, W_X1 + (b * 8 + (lane >> 2)) * 2048 + pid * 32 + (lane & 3) * 8, 0, SC1);
    __builtin_amdgcn_s_barrier();
    STAMP(16);
    __builtin_amdgcn_s_barrier();
    delay(D3);
#pragma nounroll
    for (u32 spin = 0; spin < SPIN_CAP; ++spin) {
      CFENCE();
      u32 bad = 0;
#pragma unroll
      for (int i = 0; i < 4; ++i) m[i] = __builtin_amdgcn_raw_buffer_load_b128(rs, io + (i == 3 && lane >= 32 ? 0 : i * 1024), W_IN3, SC1);
#pragma unroll
      for (int i = 0; i < 4; ++i) bad |= (m[i].y ^ tag) | (m[i].w ^ tag);
      if (__all(bad == 0)) break;
    }
    STAMP(19);
    if (lane >= 32) m[3] = v4u{0u, 0u, 0u, 0u};
    yb = inbox_sum(m);
    t = pk(gb0 * blo(yb), gb1 * bhi(yb));
    const u32 r = pk(blo(x1) + blo(t), bhi(x1) + bhi(t));
    if (lane < 8) *(GAS u16*)(out + (b * 512 + oc + (lane & 4)) * 2) = (u16)(lane < 4 ? r : r >> 16);
    STAMP(20);
    return;
  }

  if (wave == 3) {
    u32 tref = (u32)__builtin_amdgcn_s_memrealtime() + T00;
    const u32 tag = (u32)__builtin_amdgcn_readfirstlane((int)__builtin_amdgcn_raw_buffer_load_b32(rs, W_EPOCH, 0, SC1)) + 1u;
    *(volatile LAS v2u*)C_TAG = v2u{tag, 1u};
    asm volatile("s_mov_b64 exec, 0\n s_branch .Lldr_copies\n .Lwarm_ret0:\n s_mov_b64 exec, -1" ::: "memory");
    int uref = 0, tau = TAU * 64 / 100, kp = 1, kr = 1;
#pragma nounroll
    for (u32 spin = 0; spin < SPIN_CAP * 8; ++spin) {
      const v3u cc = *(volatile LAS v3u*)C_CONS;
      const int u = (int)lds_poll(C_LANDED);
      const u32 now = (u32)__builtin_amdgcn_s_memrealtime();
      const u32 c0 = __builtin_amdgcn_readfirstlane(cc.x), c1 = __builtin_amdgcn_readfirstlane(cc.y), pl = __builtin_amdgcn_readfirstlane(cc.z) ^ (0x88888888u >> (32 - 4 * NPRE));
      const u32 done = pl ? (__builtin_ctz(pl) & 28) * 2 : max(8u * NPRE, 2 * min(c0, c1));
      const int lag = (int)(now - tref) - (((u - uref) * tau) >> 6);
      const int lvl = min(max(LVL0 + ((lag * GAIN + 128) >> 8), (CLOSED && uref) ? 0 : LVL0), 7);
      *(volatile LAS u32*)C_CTL = ((done + 32) << 12) | (lvl << 7);
      if (u >= 496 && done >= 480) break;
      if (CLOSED) {
        if (u >= 64 * kp) {
          if ((idx & 3) == 0 && lane == 0) __builtin_amdgcn_raw_buffer_store_b64(v2u{now, tag}, rs, W_PROG + kp * 512 + ((idx >> 2) * 8 + xcd) * 8, 0, SC1);
          ++kp;
        }
        if (u >= 64 * kr + RDLAG) {
          const v2u pr = __builtin_amdgcn_raw_buffer_load_b64(rs, W_PROG + kr * 512 + lane * 8, 0, SC1);
          const float ago = wave_sum(pr.y == tag ? (float)(int)(now - pr.x) : 0.f);
          const u32 tk = now - (u32)__builtin_amdgcn_readfirstlane((int)(ago * (1.f / 64.f)));
          if (uref) tau = min(max((int)(tk - tref), 800), 1000);
          tref = tk; uref = 64 * kr; ++kr;
        }
      }
      __builtin_amdgcn_s_sleep(3);
    }
    return;
  }

  const int w = wave - 4;
  if (first_mod) mod_tile<false>(cnd, wmod, rs, b * 8 + xcd, w, lane);
  {
    v4u kr[NPRE][4];
    const u32 ad = w * 4096 + n * 256 + ((g ^ n) << 4);
#pragma unroll
    for (int r = 0; r < NPRE; ++r) {
#pragma nounroll
      for (u32 spin = 0; (int)lds_poll(C_LANDED) <= 8 * r + w && spin < SPIN_CAP * 8; ++spin) __builtin_amdgcn_s_sleep(8);
      CFENCE();
#pragma unroll
      for (int s = 0; s < 4; ++s) kr[r][s] = *lp<v4u>((ad ^ (s * 64)) + (r & 3) * 32768);
      asm volatile("" : "+v"(kr[r][0]), "+v"(kr[r][1]), "+v"(kr[r][2]), "+v"(kr[r][3]) :: "memory");
      lds_add(C_PULL, 1u << (4 * r));
    }
#pragma nounroll
    for (u32 spin = 0; lds_poll(C_QGOT) == 0u && spin < SPIN_CAP * 8; ++spin) __builtin_amdgcn_s_sleep(8);
    CFENCE();
    v4u qa[4];
#pragma unroll
    for (int s = 0; s < 4; ++s) qa[s] = *lp<v4u>(L_QV + 64 * s + 16 * g);
#pragma unroll
    for (int r = 0; r < NPRE; ++r) {
      f4 d = mfma32(qa[0], kr[r][0], zero4);
      d = mfma32(qa[1], kr[r][1], d); d = mfma32(qa[2], kr[r][2], d); d = mfma32(qa[3], kr[r][3], d);
      *lp<float>(L_SC + (w & 1) * 8192 + (4 * r + (w >> 1)) * 64 + ((n & 3) * 4 + (n >> 2)) * 4) = d[0];
    }
    lds_add(C_PRE, 1u);
  }
  const u32 tag = get_tag();
  const int fs = 7 * xcd + (pid & 7);
  v4u fo[4][4], fg[2][4], fd[4];
  {
    const __amdgpu_buffer_rsrc_t ro = __builtin_amdgcn_make_buffer_rsrc((void*)(wo + (size_t)h * 131072), 0, (NOW & 1) ? 0 : 131072, 0x00020000);
    const int vo = 2 * g * 1024 + (64 * w + 4 * n) * 2;
#pragma unroll
    for (int s = 0; s < 4; ++s) {
      v2u r[8];
#pragma unroll
      for (int j = 0; j < 8; ++j) { r[j] = __builtin_amdgcn_raw_buffer_load_b64(ro, vo + (j >> 2) * 1024, (32 * s + 8 * (j & 3)) * 1024, NT); PACE(j); }
      tr8x4(r, fo[s]);
      PIN4(fo[s]);
      SBAR();
    }
  }
  if (has_ffn) {
    const char* pg = (n < 8 ? wg : wu) + (fs * 32 + 4 * (n & 7)) * 2;
#pragma unroll
    for (int s = 0; s < 2; ++s) {
      v2u r[8];
      const char* pr = pg + (size_t)slice_col(16 * g + 2 * w + s, 0) * (FF * 2);
#pragma unroll
      for (int j = 0; j < 8; ++j) { r[j] = (NOW & 2) ? v2u{0u, 0u} : ldnt<v2u>(pr + j * (4 * FF * 2)); PACE(j); }
      tr8x4(r, fg[s]);
      PIN4(fg[s]);
      SBAR();
    }
    v2u r[8];
    const __amdgpu_buffer_rsrc_t rd = __builtin_amdgcn_make_buffer_rsrc((void*)(wd + (size_t)fs * 32768), 0, (NOW & 2) ? 0 : 32768, 0x00020000);
    const int vd = 8 * g * 1024 + (64 * w + 4 * n) * 2;
#pragma unroll
    for (int j = 0; j < 8; ++j) { r[j] = __builtin_amdgcn_raw_buffer_load_b64(rd, vd + (j & 3) * 1024, (j >> 2) * 4096, NT); PACE(j); }
    tr8x4(r, fd);
    PIN4(fd);
    SBAR();
  } else {
    mod_tile<true>(cnd, wmod, rs, 16 + b * 8 + xcd, w, lane);
#pragma unroll
    for (int t = 0; t < 4; ++t) fg[0][t] = fg[1][t] = fd[t] = v4u{0u, 0u, 0u, 0u};
    nap<12000>();
  }
  {
    v2u sv, cv;
    const int so = mod_off(b, 64 * w + lane);
#pragma nounroll
    for (u32 spin = 0; spin < SPIN_CAP; ++spin) {
      CFENCE();
      sv = __builtin_amdgcn_raw_buffer_load_b64(rs, so, 3 * 8192, SC1);
      cv = __builtin_amdgcn_raw_buffer_load_b64(rs, so, 4 * 8192, SC1);
      if (__all(((sv.y ^ tag) | (cv.y ^ tag)) == 0)) break;
      __builtin_amdgcn_s_sleep(64);
    }
    *lp<u32>(L_SBCB + ((8 * w + 2 * (lane & 3) + (lane >> 5)) * 8 + ((lane >> 2) & 7)) * 4) = pk(half_of(sv.x, lane & 1), 1.f + half_of(cv.x, lane & 1));
  }
  if (blockIdx.x == 0 && w == 7) {
#pragma nounroll
    for (u32 spin = 0; spin < SPIN_CAP; ++spin) {
      CFENCE();
      u32 bad = 0;
#pragma nounroll
      for (int i = 0; i < 8; ++i) {
        const v4u t = __builtin_amdgcn_raw_buffer_load_b128(rs, W_ML + lane * 16, i * 1024, SC1);
        bad |= (t.y ^ tag) | (t.w ^ tag);
      }
      if (__all(bad == 0)) break;
      __builtin_amdgcn_s_sleep(64);
    }
    if (lane == 0) __builtin_amdgcn_raw_buffer_store_b32(tag, rs, W_EPOCH, 0, SC1);
  }
  if (w == 0) STAMP(13);
  const int mo = (b * 64 + 8 * w + (lane >> 3)) * 4096 + (lane & 7) * 8;
  const int gs = 16 * g + ((n + 2 * w) & 15);
  asm volatile("s_waitcnt vmcnt(0) lgkmcnt(0)" ::: "memory");
  __builtin_amdgcn_s_barrier();
  CFENCE();
  {
    v4u oa[4];
#pragma unroll
    for (int s = 0; s < 4; ++s) oa[s] = *lp<v4u>(L_OA + (n & 3) * 320 + s * 64 + g * 16);
    SBAR();
    f4 d[4] = {zero4, zero4, zero4, zero4};
#pragma unroll
    for (int s = 0; s < 4; ++s)
#pragma unroll
      for (int t = 0; t < 4; ++t) d[t] = mfma32(oa[s], fo[s][t], d[t]);
    float y[4];
#pragma unroll
    for (int t = 0; t < 4; ++t) y[t] = (d[t][0] + d[t][2]) + (d[t][1] + d[t][3]);
    const float y01 = (g & 1) ? y[1] : y[0], y23 = (g & 1) ? y[3] : y[2];
    __builtin_amdgcn_raw_buffer_store_b64(v2u{fu((g & 2) ? y23 : y01), tag}, rs, mo, W_IN1 + pid * 64, SC1);
  }
  if (w == 0) STAMP(14);
  float sb[8], cb[8];
  {
    const v4u s0 = *lp<v4u>(L_SBCB + gs * 32), s1 = *lp<v4u>(L_SBCB + gs * 32 + 16);
#pragma unroll
    for (int e = 0; e < 4; ++e) { sb[e] = blo(s0[e]); cb[e] = bhi(s0[e]); sb[4 + e] = blo(s1[e]); cb[4 + e] = bhi(s1[e]); }
    PIN4(sb); PIN4(sb + 4); PIN4(cb); PIN4(cb + 4);
  }
  __builtin_amdgcn_s_barrier();
  delay(D2);
  v4u m0, m1;
#pragma nounroll
  for (u32 spin = 0; spin < SPIN_CAP; ++spin) {
    CFENCE();
    m0 = __builtin_amdgcn_raw_buffer_load_b128(rs, gs * 32, W_X1 + (b * 8 + xcd) * 2048, SC1);
    m1 = __builtin_amdgcn_raw_buffer_load_b128(rs, gs * 32 + 16, W_X1 + (b * 8 + xcd) * 2048, SC1);
    if (__all(((m0.y ^ tag) | (m0.w ^ tag) | (m1.y ^ tag) | (m1.w ^ tag)) == 0)) break;
  }
  if (w == 0) STAMP(17);
  {
    const float v[8] = {blo(m0.x), bhi(m0.x), blo(m0.z), bhi(m0.z), blo(m1.x), bhi(m1.x), blo(m1.z), bhi(m1.z)};
    const float sh = uf((u32)__builtin_amdgcn_readlane((int)fu(v[0]), (16 - 2 * w) & 15));
    float d[8];
#pragma unroll
    for (int e = 0; e < 8; ++e) d[e] = v[e] - sh;
    float s1 = ((d[0] + d[1]) + (d[2] + d[3])) + ((d[4] + d[5]) + (d[6] + d[7]));
    float s2 = (fmaf(d[0], d[0], d[1] * d[1]) + fmaf(d[2], d[2], d[3] * d[3])) + (fmaf(d[4], d[4], d[5] * d[5]) + fmaf(d[6], d[6], d[7] * d[7]));
    s1 = wave_sum(s1) * (1.f / 512.f); s2 = wave_sum(s2) * (1.f / 512.f);
    const float mb = rbf(sh + s1), var = fmaxf(s2 - s1 * s1, 0.f);
    const float rsd = __builtin_amdgcn_rcpf(rbf(__builtin_amdgcn_sqrtf(rbf(rbf(var) + 1e-5f))));
    v4u ha;
#pragma unroll
    for (int e = 0; e < 4; ++e) ha[e] = pk(fmaf((v[2 * e] - mb) * rsd, cb[2 * e], sb[2 * e]), fmaf((v[2 * e + 1] - mb) * rsd, cb[2 * e + 1], sb[2 * e + 1]));
    f4 gp;
#pragma unroll
    for (int t = 0; t < 4; ++t) gp[t] = mfma32(ha, fg[0][t], zero4)[0] + mfma32(ha, fg[1][t], zero4)[1];
    if (g == 0) *lp<f4>(L_GURED + w * 256 + n * 16) = gp;
  }
  asm volatile("s_waitcnt lgkmcnt(0)" ::: "memory");
  __builtin_amdgcn_s_barrier();
  CFENCE();
  {
    float pt[8];
#pragma unroll
    for (int ww = 0; ww < 8; ++ww) pt[ww] = *lp<float>(L_GURED + ww * 256 + ((n >> 3) * 32 + 8 * g + (n & 7)) * 4);
    const float s = rbf(((pt[0] + pt[1]) + (pt[2] + pt[3])) + ((pt[4] + pt[5]) + (pt[6] + pt[7])));
    const float up = dppf<ROR8>(s);
    const float z = s * fmaf(s * s, 0.044715f * 0.7978845608028654f * 2.f * LOG2E, 0.7978845608028654f * 2.f * LOG2E);
    const float ge = rbf(s * __builtin_amdgcn_rcpf(1.f + __builtin_amdgcn_exp2f(-z)));
    const float act = ge * up;
    const u32 ap = pk(act, dppf<XOR1>(act));
    const v4u aa = {dppu<0x150>(ap), dppu<0x152>(ap), dppu<0x154>(ap), dppu<0x156>(ap)};
    float y[4];
#pragma unroll
    for (int t = 0; t < 4; ++t) y[t] = mfma32(aa, fd[t], zero4)[0];
    const float y01 = (g & 1) ? y[1] : y[0], y23 = (g & 1) ? y[3] : y[2];
    if (has_ffn) __builtin_amdgcn_raw_buffer_store_b64(v2u{fu((g & 2) ? y23 : y01), tag}, rs, mo, W_IN3 + fs * 64, SC1);
  }
  if (w == 0) STAMP(18);
}

}

extern "C" void kernel_launch(void* const* d_in, const int* in_sizes, int n_in, void* d_out, int out_size, void* d_ws, size_t ws_size, hipStream_t stream) {
  hipLaunchKernelGGL(blk::fused_block, dim3(256), dim3(blk::NTHR), 0, stream,
                     (const char*)d_in[0], (const char*)d_in[1], (const char*)d_in[2], (const char*)d_in[3], (const char*)d_in[4], (const char*)d_in[6],
                     (const char*)d_in[7], (const char*)d_in[8], (const char*)d_in[9], (const char*)d_in[10], (char*)d_out, (char*)d_ws);
}
```

```cpp
#include <hip/hip_runtime.h>
#pragma clang diagnostic ignored "-Wint-to-pointer-cast"

#define WLEVELS "17,18,19,20,21,23,25,28"
#define LVL0 2
#define U0 5
#define TAU 1400
#define T00 250
#define GAIN 8
#define CLOSED 1
#define RDLAG 24
#define NPRE 8
#define CSLEEP 64
#define HSLEEP 16
#define QSLEEP 16
#define HDELAY 0
#define QDELAY 2000
#define WQFLY 3
#define PACEN 1
#define NOW 0
#define D1 1200
#define D2 500
#define D3 1100
#define STAMPS 0

namespace blk {

typedef unsigned short u16;
typedef unsigned u32;
typedef unsigned long long u64;
typedef u32 v4u __attribute__((ext_vector_type(4)));
typedef u32 v3u __attribute__((ext_vector_type(3)));
typedef u32 v2u __attribute__((ext_vector_type(2)));
typedef float f4 __attribute__((ext_vector_type(4)));
typedef float f2 __attribute__((ext_vector_type(2)));
typedef __bf16 bf8 __attribute__((ext_vector_type(8)));
typedef __bf16 bf2 __attribute__((ext_vector_type(2)));
typedef short s4 __attribute__((ext_vector_type(4)));
#define GAS __attribute__((address_space(1)))
#define LAS __attribute__((address_space(3)))

constexpr int NTHR = 768;
constexpr int FF = 1792;
constexpr int L_SC = 131072;
constexpr int L_MODRED = L_SC + 16384;
constexpr int L_GURED = L_MODRED + 8192;
constexpr int L_SBCB = L_GURED + 2048;
constexpr int L_OA = L_SBCB + 2048;
constexpr int L_QV = L_OA + 1280;
constexpr int L_CTRL = L_QV + 256;
constexpr int C_CONS = L_CTRL, C_PULL = L_CTRL + 8, C_MODCNT = L_CTRL + 12, C_PRE = L_CTRL + 16, C_QGOT = L_CTRL + 20, C_TAG = L_CTRL + 24;
constexpr int C_CTL = L_CTRL + 32;
constexpr int C_LANDED = L_CTRL + 48;
constexpr int L_STAMP = C_LANDED + 256;
constexpr int LDS_BYTES = L_STAMP + (STAMPS ? 256 : 0);
constexpr int W_EPOCH = 0, W_PROG = 4096, W_ML = 16384;
constexpr int W_MOD = 65536;
constexpr int W_Q = 131072;
constexpr int W_X1 = 196608;
constexpr int W_IN1 = 262144, W_IN3 = W_IN1 + (1 << 20);
constexpr int W_END = W_IN3 + (1 << 20), W_DBG = W_END;
constexpr int SC1 = 16, NT = 2;
constexpr u32 SPIN_CAP = 1u << 18;

__device__ __forceinline__ float blo(u32 u) { return __builtin_bit_cast(float, u << 16); }
__device__ __forceinline__ float bhi(u32 u) { return __builtin_bit_cast(float, u & 0xffff0000u); }
__device__ __forceinline__ u32 pk(float a, float b) { f2 p = {a, b}; return __builtin_bit_cast(u32, __builtin_convertvector(p, bf2)); }
__device__ __forceinline__ float rbf(float a) { return blo(pk(a, a)); }
__device__ __forceinline__ u32 fu(float a) { return __builtin_bit_cast(u32, a); }
__device__ __forceinline__ float uf(u32 a) { return __builtin_bit_cast(float, a); }
__device__ __forceinline__ f4 mfma32(v4u a, v4u b, f4 c) { return __builtin_amdgcn_mfma_f32_16x16x32_bf16(__builtin_bit_cast(bf8, a), __builtin_bit_cast(bf8, b), c, 0, 0, 0); }
__device__ __forceinline__ f4 mfma16(v2u a, v2u b, f4 c) { return __builtin_amdgcn_mfma_f32_16x16x16bf16_1k(__builtin_bit_cast(s4, a), __builtin_bit_cast(s4, b), c, 0, 0, 0); }
template <int CTRL> __device__ __forceinline__ u32 dppu(u32 x) { return (u32)__builtin_amdgcn_mov_dpp((int)x, CTRL, 0xf, 0xf, true); }
template <int CTRL> __device__ __forceinline__ float dppf(float x) { return uf(dppu<CTRL>(fu(x))); }
constexpr int XOR1 = 0xB1, XOR2 = 0x4E, HMIR = 0x141, ROR4 = 0x124, ROR8 = 0x128;
__device__ __forceinline__ float xrow_sum(float x) {
  auto s = __builtin_amdgcn_permlane16_swap(fu(x), fu(x), false, false);
  x = uf(s[0]) + uf(s[1]);
  auto t = __builtin_amdgcn_permlane32_swap(fu(x), fu(x), false, false);
  return uf(t[0]) + uf(t[1]);
}
__device__ __forceinline__ float xrow_max(float x) {
  auto s = __builtin_amdgcn_permlane16_swap(fu(x), fu(x), false, false);
  x = fmaxf(uf(s[0]), uf(s[1]));
  auto t = __builtin_amdgcn_permlane32_swap(fu(x), fu(x), false, false);
  return fmaxf(uf(t[0]), uf(t[1]));
}
__device__ __forceinline__ float wave_sum(float x) { x += dppf<XOR1>(x); x += dppf<XOR2>(x); x += dppf<HMIR>(x); x += dppf<ROR8>(x); return xrow_sum(x); }
__device__ __forceinline__ float wave_max(float x) { x = fmaxf(x, dppf<XOR1>(x)); x = fmaxf(x, dppf<XOR2>(x)); x = fmaxf(x, dppf<HMIR>(x)); x = fmaxf(x, dppf<ROR8>(x)); return xrow_max(x); }
__device__ __forceinline__ u32 perm_lo(u32 r0, u32 r1) { return __builtin_amdgcn_perm(r1, r0, 0x05040100u); }
__device__ __forceinline__ u32 perm_hi(u32 r0, u32 r1) { return __builtin_amdgcn_perm(r1, r0, 0x07060302u); }
#define CFENCE() asm volatile("" ::: "memory")
#define SBAR() __builtin_amdgcn_sched_barrier(0)
#define PIN4(a) asm volatile("" : "+v"((a)[0]), "+v"((a)[1]), "+v"((a)[2]), "+v"((a)[3]))
template <typename T> __device__ __forceinline__ LAS T* lp(u32 off) { return (LAS T*)off; }
__device__ __forceinline__ u32 lds_poll(u32 off) { return (u32)__builtin_amdgcn_readfirstlane((int)*(volatile LAS u32*)off); }
__device__ __forceinline__ void lds_add(u32 off, u32 v) {
  asm volatile("s_mov_b64 exec, 1\n ds_add_u32 %0, %1\n s_mov_b64 exec, -1" :: "v"(off), "v"(v) : "memory");
}
template <typename T> __device__ __forceinline__ T ldnt(const void* p) { return __builtin_nontemporal_load((const GAS T*)p); }
template <typename T> __device__ __forceinline__ T ldg(const void* p) { return *(const GAS T*)p; }
__device__ __forceinline__ void tr8x4(const v2u (&r)[8], v4u (&f)[4]) {
#pragma unroll
  for (int q = 0; q < 4; ++q) {
    f[0][q] = perm_lo(r[2 * q].x, r[2 * q + 1].x); f[1][q] = perm_hi(r[2 * q].x, r[2 * q + 1].x);
    f[2][q] = perm_lo(r[2 * q].y, r[2 * q + 1].y); f[3][q] = perm_hi(r[2 * q].y, r[2 * q + 1].y);
  }
}
__device__ __forceinline__ int slice_col(int s, int pos) { return 64 * (s >> 3) + 32 * (s & 1) + 4 * pos + ((s >> 1) & 3); }

#define STAMP(i) do { } while (0)

__device__ __forceinline__ int mod_off(int b, int c) { return W_MOD + (c >> 6) * 1024 + b * 256 + ((c & 63) >> 1) * 8; }
__device__ __forceinline__ float half_of(u32 v, int odd) { return odd ? bhi(v) : blo(v); }
__device__ __forceinline__ void delay(int cyc) {
  const u64 t0 = __builtin_amdgcn_s_memtime();
#pragma nounroll
  while ((int)(__builtin_amdgcn_s_memtime() - t0) < cyc) { }
}
template <int CYC> __device__ __forceinline__ void nap() {
#pragma unroll
  for (int i = 0; i < CYC / 8128; ++i) __builtin_amdgcn_s_sleep(127);
  if (CYC % 8128 >= 64) __builtin_amdgcn_s_sleep((CYC % 8128) / 64);
}
__device__ __forceinline__ u32 get_tag() {
  v2u t;
#pragma nounroll
  for (u32 spin = 0; spin < SPIN_CAP * 8; ++spin) { t = *(volatile LAS v2u*)C_TAG; if (__builtin_amdgcn_readfirstlane((int)t.y)) break; }
  return (u32)__builtin_amdgcn_readfirstlane((int)t.x);
}
__device__ __forceinline__ u32 inbox_sum(const v4u (&m)[4]) {
  float y0 = (uf(m[0].x) + uf(m[1].x)) + (uf(m[2].x) + uf(m[3].x)), y1 = (uf(m[0].z) + uf(m[1].z)) + (uf(m[2].z) + uf(m[3].z));
  y0 += dppf<ROR4>(y0); y1 += dppf<ROR4>(y1); y0 += dppf<ROR8>(y0); y1 += dppf<ROR8>(y1);
  return pk(xrow_sum(y0), xrow_sum(y1));
}
#define PACE(j) do { if (((j) % PACEN) == PACEN - 1) { asm volatile("s_waitcnt vmcnt(0)" ::: "memory"); SBAR(); } } while (0)
constexpr float LOG2E = 1.4426950408889634f;

template <bool PACED>
__device__ __forceinline__ void mod_tile(const char* cnd, const char* wmod, __amdgpu_buffer_rsrc_t rs, int cb, int w, int lane) {
  const int g = lane >> 4, n = lane & 15;
  const f4 zero4 = {0.f, 0.f, 0.f, 0.f};
  const v2u cv = ldnt<v2u>(cnd + ((n >> 2) * 512 + 64 * w + 16 * g + 4 * (n & 3)) * 2);
  const __amdgpu_buffer_rsrc_t rm = __builtin_amdgcn_make_buffer_rsrc((void*)(wmod + (size_t)(64 * w) * 6144 + cb * 128), 0, 64 * 6144, 0x00020000);
  v2u r[2][8];
#pragma unroll
  for (int j = 0; j < 16; ++j) { r[j >> 3][j & 7] = __builtin_amdgcn_raw_buffer_load_b64(rm, 16 * g * 6144 + n * 8, j * 6144, NT); if (PACED) PACE(j); }
  SBAR();
  u32 sp[2];
#pragma unroll
  for (int e = 0; e < 2; ++e) {
    const float c0 = blo(cv[e]), c1 = bhi(cv[e]);
    sp[e] = pk(c0 * __builtin_amdgcn_rcpf(1.f + __builtin_amdgcn_exp2f(-c0 * LOG2E)), c1 * __builtin_amdgcn_rcpf(1.f + __builtin_amdgcn_exp2f(-c1 * LOG2E)));
  }
  const v4u a0 = {dppu<0x00>(sp[0]), dppu<0x00>(sp[1]), dppu<0x55>(sp[0]), dppu<0x55>(sp[1])};
  const v4u a1 = {dppu<0xAA>(sp[0]), dppu<0xAA>(sp[1]), dppu<0xFF>(sp[0]), dppu<0xFF>(sp[1])};
  v4u f0[4], f1[4];
  tr8x4(r[0], f0); tr8x4(r[1], f1);
  f4 pt;
#pragma unroll
  for (int t = 0; t < 4; ++t) pt[t] = mfma32(a1, f1[t], mfma32(a0, f0[t], zero4))[0];
  *lp<f4>(L_MODRED + w * 1024 + lane * 16) = pt;
  u32 old;
  asm volatile("s_mov_b64 exec, 1\n ds_add_rtn_u32 %0, %1, %2\n s_mov_b64 exec, -1\n s_waitcnt lgkmcnt(0)" : "=&v"(old) : "v"(C_MODCNT), "v"(1u) : "memory");
  if ((__builtin_amdgcn_readfirstlane((int)old) & 7) == 7) {
    f4 p[8];
#pragma unroll
    for (int ww = 0; ww < 8; ++ww) p[ww] = *lp<f4>(L_MODRED + ww * 1024 + lane * 16);
    const f4 v = ((p[0] + p[1]) + (p[2] + p[3])) + ((p[4] + p[5]) + (p[6] + p[7]));
    const u32 tag = get_tag();
    __builtin_amdgcn_raw_buffer_store_b128(v4u{pk(v[0], v[1]), tag, pk(v[2], v[3]), tag}, rs, lane * 16 + W_MOD + cb * 1024, 0, SC1);
    if (!PACED) STAMP(9);
  }
}

__global__ void __launch_bounds__(NTHR) fused_block(const char* x, const char* kc, const char* vc, const char* wq, const char* wo, const char* wg,
                                                    const char* wu, const char* wd, const char* cnd, const char* wmod, char* out, char* ws) {
  __shared__ __attribute__((aligned(1024))) char lds_mem[LDS_BYTES];
  const int lane = threadIdx.x & 63, wave = __builtin_amdgcn_readfirstlane(threadIdx.x >> 6);
  const int xcd = blockIdx.x & 7, idx = blockIdx.x >> 3;
  const int half = idx & 1, hh = (idx >> 1) & 3, b = idx >> 3;
  const int h = xcd + 8 * hh;
  const int pid = 8 * xcd + (idx & 7);
  const bool has_ffn = (idx & 7) < 7;
  const bool first_mod = !has_ffn && b < 2;
  const int g = lane >> 4, n = lane & 15;
  const f4 zero4 = {0.f, 0.f, 0.f, 0.f};
  if (threadIdx.x < (STAMPS ? 140 : 16)) *lp<u32>(L_CTRL + 4 * threadIdx.x) = threadIdx.x == 8 ? (32u << 12) | (LVL0 * 128) : 0u;
  asm volatile("s_waitcnt lgkmcnt(0)" :: "v"(lds_mem), "s"(kc), "s"(ws) : "memory");
  __builtin_amdgcn_s_barrier();

  if (wave == 0) {
    __builtin_amdgcn_s_setprio(3);
    constexpr u32 HOLES = 0xA000u;
    const size_t reg_off = ((size_t)(b * 32 + h) << 21) + half * 32768 - HOLES;
    u32 o0 = g * 256 + ((n ^ g) << 4), o1 = o0 ^ 64, o2 = (o0 ^ 128) + 6144, o3 = (o0 ^ 192) + 6144;
    const u32 va = C_LANDED + lane * 4;
    const u64 kb = (u64)(kc + reg_off), vb = (u64)(vc + reg_off);
    v4u rsrc = {(u32)kb, (u32)(kb >> 32) & 0xffffu, (2u << 20) + 0x2000u, 0x00020000u};
    STAMP(5);
    if (first_mod) __builtin_amdgcn_s_sleep(CSLEEP);
    int pub = -7;
    u32 ctl = (32u << 12) | (LVL0 * 128);
#pragma nounroll
    for (u32 end = 1u << 20; end <= (2u << 20); end += 1u << 20) {
      u32 off = end - (1u << 20), src = HOLES, spin = SPIN_CAP * 8, blk, tmp, pcl, pch;
#define LD(v, o) "s_waitcnt vmcnt(\\w)\n buffer_load_dwordx4 %[" #v "], %[rs], %[src] offen offset:" #o " nt lds\n"
      asm volatile(
        "s_getpc_b64 vcc\n"
        ".Lldr_pc:\n s_add_u32 %[pcl], vcc_lo, .Lldr_copies-.Lldr_pc\n s_addc_u32 %[pch], vcc_hi, 0\n"
        ".Lldr_next:\n s_waitcnt lgkmcnt(4)\n"
        ".Lldr_dec:\n v_readfirstlane_b32 %[tmp], %[ctl]\n ds_read_b32 %[ctl], %[ca]\n s_add_u32 %[blk], %[off], 0x4000\n"
        "s_cmp_gt_u32 %[blk], %[tmp]\n s_cbranch_scc1 .Lldr_stall\n"
        "s_and_b32 %[tmp], %[tmp], 0x380\n s_add_u32 vcc_lo, %[pcl], %[tmp]\n s_addc_u32 vcc_hi, %[pch], 0\n s_setpc_b64 vcc\n"
        ".Lldr_stall:\n s_waitcnt lgkmcnt(0)\n s_sub_u32 %[spin], %[spin], 1\n s_cbranch_scc0 .Lldr_dec\n s_branch .Lldr_exit\n"
        ".p2align 7\n.Lldr_copies:\n"
        ".irp w," WLEVELS "\n"
        "s_cbranch_execz 2f\n"
        "1:\n s_and_b32 m0, %[off], 0x1ffff\n" LD(v0, 0) "s_add_u32 %[off], %[off], 0x1000\n" LD(v1, 1024) LD(v2, 2048) LD(v3, 3072)
        "ds_write_b32 %[va], %[pub]\n s_add_u32 %[src], %[src], 0x800\n s_or_b32 %[src], %[src], 0xa000\n v_add_u32 %[pub], 1, %[pub]\n"
        "s_cmp_lt_u32 %[off], %[blk]\n s_cbranch_scc1 1b\n s_cmp_lt_u32 %[off], %[end]\n s_cbranch_scc1 .Lldr_next\n s_branch .Lldr_exit\n"
        ".p2align 7\n2:\n.endr\n"
        "s_branch .Lwarm_ret0\n"
        ".Lldr_exit:\n s_waitcnt lgkmcnt(0)"
        : [off] "+&s"(off), [src] "+&s"(src), [spin] "+&s"(spin), [blk] "=&s"(blk), [tmp] "=&s"(tmp), [pcl] "=&s"(pcl), [pch] "=&s"(pch), [pub] "+&v"(pub), [ctl] "+&v"(ctl)
        : [v0] "v"(o0), [v1] "v"(o1), [v2] "v"(o2), [v3] "v"(o3), [rs] "s"(rsrc), [end] "s"(end), [va] "v"(va), [ca] "v"(C_CTL) : "memory", "scc", "vcc");
      rsrc.x = (u32)vb; rsrc.y = (u32)(vb >> 32) & 0xffffu;
      o0 = o1 = lane * 16; o2 = o3 = lane * 16 + 6144;
    }
#pragma unroll
    for (int k = 6; k >= 0; --k) {
      const u32 pb = 512 - k;
      asm volatile("s_waitcnt vmcnt(%2)\n ds_write_b32 %0, %1" :: "v"(va), "v"(pb), "i"(4 * k) : "memory");
    }
    STAMP(1);
    return;
  }

  const __amdgpu_buffer_rsrc_t rs = __builtin_amdgcn_make_buffer_rsrc((void*)ws, 0, W_END, 0x00020000);

  if (wave <= 2) {
    const int a = wave - 1;
    u32 tag;
    if (a == 0) {
      const int beta = n >> 2, kq = 16 * g + 4 * (n & 3);
      v2u xr[8];
#pragma unroll
      for (int i = 0; i < 8; ++i) xr[i] = ldnt<v2u>(x + (beta * 512 + 64 * i + kq) * 2);
      SBAR();
      if (first_mod) {
#pragma nounroll
        for (u32 spin = 0; lds_poll(C_MODCNT) == 0u && spin < SPIN_CAP * 8; ++spin) __builtin_amdgcn_s_sleep(2);
      }
      {
        const char* wt = wq + (h * 128 + (2 * b + half) * 16) * 2;
        const u32 vo = (64 * (lane >> 5) + 16 * ((lane >> 3) & 3) + ((lane >> 1) & 3)) * 8192 + (lane & 1) * 16;
#pragma nounroll
        for (int p = 0; p < 16; ++p)
          asm volatile("s_mov_b32 m0, %2\n s_waitcnt vmcnt(%3)\n global_load_lds_dwordx4 %0, %1 nt"
                       :: "v"(vo), "s"(wt + (size_t)(128 * (p >> 2) + 4 * (p & 3)) * 8192), "s"(L_SC + p * 1024), "i"(WQFLY - 1) : "memory");
      }
      SBAR();
      {
        float s = 0.f;
#pragma unroll
        for (int i = 0; i < 8; ++i) s += (blo(xr[i].x) + bhi(xr[i].x)) + (blo(xr[i].y) + bhi(xr[i].y));
        s += dppf<XOR1>(s); s += dppf<XOR2>(s);
        const float mean = xrow_sum(s) * (1.f / 512.f);
        float q = 0.f;
#pragma unroll
        for (int i = 0; i < 8; ++i)
#pragma unroll
          for (int e = 0; e < 2; ++e) { const float d0 = blo(xr[i][e]) - mean, d1 = bhi(xr[i][e]) - mean; q = fmaf(d0, d0, q); q = fmaf(d1, d1, q); }
        q += dppf<XOR1>(q); q += dppf<XOR2>(q);
        const float mb = rbf(mean), rsd = __builtin_amdgcn_rcpf(rbf(__builtin_amdgcn_sqrtf(rbf(rbf(xrow_sum(q) * (1.f / 512.f)) + 1e-5f))));
#pragma unroll
        for (int i = 0; i < 8; ++i)
#pragma unroll
          for (int e = 0; e < 2; ++e) { const u32 d = pk(blo(xr[i][e]) - mb, bhi(xr[i][e]) - mb); xr[i][e] = pk(blo(d) * rsd, bhi(d) * rsd); }
        PIN4(xr); PIN4(xr + 4);
      }
      STAMP(10);
      tag = get_tag();
      nap<HDELAY>();
      u32 ha[8][2];
#pragma nounroll
      for (u32 spin = 0; spin < SPIN_CAP; ++spin) {
        CFENCE();
        bool go = spin == 0;
        if (!go) go = __all(__builtin_amdgcn_raw_buffer_load_b64(rs, W_MOD + n * 1024 + 1016, 0, SC1)[1] == tag);
        if (go) {
          u32 bad = 0;
#pragma unroll
          for (int hf = 0; hf < 2; ++hf) {
            v4u sa[4], ca[4];
#pragma unroll
            for (int i = 0; i < 4; ++i) {
              sa[i] = __builtin_amdgcn_raw_buffer_load_b128(rs, beta * 256 + kq * 4 + i * 1024, W_MOD + hf * 4096, SC1);
              ca[i] = __builtin_amdgcn_raw_buffer_load_b128(rs, beta * 256 + kq * 4 + i * 1024, W_MOD + 8192 + hf * 4096, SC1);
            }
#pragma unroll
            for (int i = 0; i < 4; ++i) {
              bad |= (sa[i].y ^ tag) | (sa[i].w ^ tag) | (ca[i].y ^ tag) | (ca[i].w ^ tag);
#pragma unroll
              for (int e = 0; e < 2; ++e) {
                const u32 c1 = pk(1.f + blo(ca[i][2 * e]), 1.f + bhi(ca[i][2 * e]));
                const u32 t = pk(blo(xr[4 * hf + i][e]) * blo(c1), bhi(xr[4 * hf + i][e]) * bhi(c1));
                ha[4 * hf + i][e] = pk(blo(t) + blo(sa[i][2 * e]), bhi(t) + bhi(sa[i][2 * e]));
              }
            }
          }
          if (__all(bad == 0)) break;
        }
        __builtin_amdgcn_s_sleep(HSLEEP);
      }
      asm volatile("s_waitcnt vmcnt(0)" ::: "memory");
      STAMP(11);
      f4 acc[4] = {zero4, zero4, zero4, zero4};
#pragma unroll
      for (int p = 0; p < 16; ++p) {
        const v2u lo = __builtin_bit_cast(v2u, __builtin_amdgcn_ds_read_tr16_b64_v4i16(lp<s4>(L_SC + p * 1024 + lane * 8)));
        const v2u hi = __builtin_bit_cast(v2u, __builtin_amdgcn_ds_read_tr16_b64_v4i16(lp<s4>(L_SC + p * 1024 + 512 + lane * 8)));
        const int sg = p >> 2;
        acc[p & 3] = mfma32(v4u{ha[2 * sg][0], ha[2 * sg][1], ha[2 * sg + 1][0], ha[2 * sg + 1][1]}, v4u{lo.x, lo.y, hi.x, hi.y}, acc[p & 3]);
      }
      const float qv = (acc[0][0] + acc[1][1]) + (acc[2][2] + acc[3][3]);
      const u32 pr = pk(qv, dppf<XOR1>(qv));
      if (!(n & 1)) __builtin_amdgcn_raw_buffer_store_b64(v2u{pr, tag}, rs, W_Q + (h * 4 + g) * 512 + (2 * b + half) * 64 + (n >> 1) * 8, 0, SC1);
      STAMP(12);
      nap<QDELAY>();
      v2u m;
#pragma nounroll
      for (u32 spin = 0; spin < SPIN_CAP; ++spin) {
        CFENCE();
        m = __builtin_amdgcn_raw_buffer_load_b64(rs, lane * 8, W_Q + (h * 4 + b) * 512, SC1);
        if (__all(m.y == tag)) break;
        __builtin_amdgcn_s_sleep(QSLEEP);
      }
      *lp<u32>(L_QV + lane * 4) = m.x;
      CFENCE();
      *(volatile LAS u32*)C_QGOT = 1u;
    } else {
#pragma nounroll
      for (u32 spin = 0; lds_poll(C_QGOT) == 0u && spin < SPIN_CAP * 8; ++spin) __builtin_amdgcn_s_sleep(4);
      tag = get_tag();
    }
    CFENCE();
    v4u qa[4];
#pragma unroll
    for (int s = 0; s < 4; ++s) qa[s] = *lp<v4u>(L_QV + 64 * s + 16 * g);
    if (a == 0) STAMP(2);
    const u32 sc = L_SC + a * 8192;
    int landed = 0;
    const u32 kofs = n * 256 + ((g ^ n) << 4);
    const u32 spos = sc + ((n & 3) * 4 + (n >> 2)) * 4;
#pragma nounroll
    for (int j = 4 * NPRE; j < 128; ++j) {
      const int u = 2 * j + a;
#pragma nounroll
      for (u32 spin = 0; landed <= u && spin < SPIN_CAP * 8; ++spin) landed = (int)lds_poll(C_LANDED);
      CFENCE();
      const u32 ad = ((u & 31) << 12) + kofs;
      v4u k0 = *lp<v4u>(ad), k1 = *lp<v4u>(ad ^ 64), k2 = *lp<v4u>(ad ^ 128), k3 = *lp<v4u>(ad ^ 192);
      asm volatile("" : "+v"(k0), "+v"(k1), "+v"(k2), "+v"(k3) :: "memory");
      *(volatile LAS u32*)(C_CONS + 4 * a) = j + 1;
      f4 d = mfma32(qa[0], k0, zero4);
      d = mfma32(qa[1], k1, d); d = mfma32(qa[2], k2, d); d = mfma32(qa[3], k3, d);
      *lp<float>(spos + j * 64) = d[0];
    }
#pragma nounroll
    for (u32 spin = 0; lds_poll(C_PRE) != 8u && spin < SPIN_CAP * 8; ++spin) { }
    CFENCE();
    if (a == 0) STAMP(3);
    float mw, lw;
    {
      f4 s[8];
      float mx = -INFINITY;
#pragma unroll
      for (int t = 0; t < 8; ++t) {
        s[t] = *lp<f4>(sc + lane * 16 + t * 1024);
        const u32 p0 = pk(s[t][0], s[t][1]), p1 = pk(s[t][2], s[t][3]);
        const u32 r0 = pk(blo(p0) * 0.08838834764831845f, bhi(p0) * 0.08838834764831845f), r1 = pk(blo(p1) * 0.08838834764831845f, bhi(p1) * 0.08838834764831845f);
        s[t] = f4{blo(r0), bhi(r0), blo(r1), bhi(r1)};
        mx = fmaxf(fmaxf(mx, fmaxf(s[t][0], s[t][1])), fmaxf(s[t][2], s[t][3]));
      }
      mw = wave_max(mx);
      float sum = 0.f;
#pragma unroll
      for (int t = 0; t < 8; ++t) {
        v4u pp;
#pragma unroll
        for (int e = 0; e < 4; ++e) { const float p = __builtin_amdgcn_exp2f((s[t][e] - mw) * LOG2E); sum += p; pp[e] = pk(p, p); }
        *lp<v4u>(sc + lane * 16 + t * 1024) = pp;
      }
      lw = wave_sum(sum);
    }
    const int mlbase = W_ML + (b * 32 + h) * 64;
    if (lane == 0) __builtin_amdgcn_raw_buffer_store_b128(v4u{fu(mw), tag, fu(lw), tag}, rs, mlbase + (half * 2 + a) * 16, 0, SC1);
    u32 msk[4];
#pragma unroll
    for (int q = 0; q < 4; ++q) msk[q] = ((n & 7) >> 1) == q ? ((n & 1) ? 0xffff0000u : 0x0000ffffu) : 0u;
    f4 acc = zero4;
    float scale = 0.f, ga0 = 0.f, ga1 = 0.f, gb0 = 0.f, gb1 = 0.f;
    u32 xo0 = 0, xo1 = 0;
    const int oc = slice_col(pid, 2 * (lane & 3));
#pragma nounroll
    for (int j = 0; j < 128; ++j) {
      const int u = 256 + 2 * j + a;
      if (j == 100) {
        v4u ml; v2u gm[4];
        const int go = mod_off(b, oc);
#pragma nounroll
        for (u32 spin = 0; spin < SPIN_CAP; ++spin) {
          CFENCE();
          ml = __builtin_amdgcn_raw_buffer_load_b128(rs, mlbase + (lane & 3) * 16, 0, SC1);
          u32 bad = (ml.y ^ tag) | (ml.w ^ tag);
#pragma unroll
          for (int i = 0; i < 4; ++i) {
            gm[i] = __builtin_amdgcn_raw_buffer_load_b64(rs, go + (i & 1) * 16, (i & 2) ? 5 * 8192 : 2 * 8192, SC1);
            bad |= gm[i].y ^ tag;
          }
          xo0 = ldnt<u16>(x + (b * 512 + oc) * 2); xo1 = ldnt<u16>(x + (b * 512 + oc + 4) * 2);
          if (__all(bad == 0)) break;
        }
        ga0 = half_of(gm[0].x, oc & 1); ga1 = half_of(gm[1].x, oc & 1); gb0 = half_of(gm[2].x, oc & 1); gb1 = half_of(gm[3].x, oc & 1);
        float M = uf(ml.x); M = fmaxf(M, dppf<XOR1>(M)); M = fmaxf(M, dppf<XOR2>(M));
        float L = uf(ml.z) * __builtin_amdgcn_exp2f((uf(ml.x) - M) * LOG2E);
        L += dppf<XOR1>(L); L += dppf<XOR2>(L);
        scale = __builtin_amdgcn_exp2f((mw - M) * LOG2E) / L;
        asm volatile("" : "+v"(xo0), "+v"(xo1));
      }
      const v4u p4 = *lp<v4u>(sc + j * 64 + g * 16);
      v4u bf[4];
#pragma unroll
      for (int i = 0; i < 4; ++i) bf[i] = v4u{p4[i] & msk[0], p4[i] & msk[1], p4[i] & msk[2], p4[i] & msk[3]};
      asm volatile("" : "+v"(bf[0]), "+v"(bf[1]), "+v"(bf[2]), "+v"(bf[3]) :: "memory");
#pragma nounroll
      for (u32 spin = 0; landed <= u && spin < SPIN_CAP * 8; ++spin) landed = (int)lds_poll(C_LANDED);
      CFENCE();
      const u32 ad = ((u & 31) << 12) + lane * 16;
      v4u v0 = *lp<v4u>(ad), v1 = *lp<v4u>(ad + 1024), v2 = *lp<v4u>(ad + 2048), v3 = *lp<v4u>(ad + 3072);
      SBAR();
      acc = mfma32(v0, bf[0], acc); acc = mfma32(v1, bf[1], acc); acc = mfma32(v2, bf[2], acc);
      asm volatile("" : "+v"(v3) :: "memory");
      *(volatile LAS u32*)(C_CONS + 4 * a) = 128 + j + 1;
      acc = mfma32(v3, bf[3], acc);
    }
    {
      const float o0 = acc[0] * scale, o1 = acc[1] * scale, o2 = acc[2] * scale, o3 = acc[3] * scale;
      const u32 h0 = pk(o0, o1), h1 = pk(o2, o3);
      const u32 l0 = pk(o0 - blo(h0), o1 - bhi(h0)), l1 = pk(o2 - blo(h1), o3 - bhi(h1));
      const bool lo = n >= 8;
      *lp<v2u>(L_OA + (2 * a + (n >> 3)) * 320 + (g * 8 + (n & 7)) * 8) = v2u{lo ? l0 : h0, lo ? l1 : h1};
    }
    asm volatile("s_waitcnt lgkmcnt(0)" ::: "memory");
    __builtin_amdgcn_s_barrier();
    if (a == 1) return;
    STAMP(4);
    v4u m[4];
    const int io = (b * 64 + pid) * 4096 + lane * 16;
    delay(D1);
#pragma nounroll
    for (u32 spin = 0; spin < SPIN_CAP; ++spin) {
      CFENCE();
      u32 bad = 0;
#pragma unroll
      for (int i = 0; i < 4; ++i) m[i] = __builtin_amdgcn_raw_buffer_load_b128(rs, io + i * 1024, W_IN1, SC1);
#pragma unroll
      for (int i = 0; i < 4; ++i) bad |= (m[i].y ^ tag) | (m[i].w ^ tag);
      if (__all(bad == 0)) break;
    }
    STAMP(15);
    u32 yb = inbox_sum(m);
    u32 t = pk(ga0 * blo(yb), ga1 * bhi(yb));
    const u32 x1 = pk(blo(xo0) + blo(t), blo(xo1) + bhi(t));
    if (lane < 32) __builtin_amdgcn_raw_buffer_store_b64(v2u{x1, tag}, rs, W_X1 + (b * 8 + (lane >> 2)) * 2048 + pid * 32 + (lane & 3) * 8, 0, SC1);
    __builtin_amdgcn_s_barrier();
    STAMP(16);
    __builtin_amdgcn_s_barrier();
    delay(D3);
#pragma nounroll
    for (u32 spin = 0; spin < SPIN_CAP; ++spin) {
      CFENCE();
      u32 bad = 0;
#pragma unroll
      for (int i = 0; i < 4; ++i) m[i] = __builtin_amdgcn_raw_buffer_load_b128(rs, io + (i == 3 && lane >= 32 ? 0 : i * 1024), W_IN3, SC1);
#pragma unroll
      for (int i = 0; i < 4; ++i) bad |= (m[i].y ^ tag) | (m[i].w ^ tag);
      if (__all(bad == 0)) break;
    }
    STAMP(19);
    if (lane >= 32) m[3] = v4u{0u, 0u, 0u, 0u};
    yb = inbox_sum(m);
    t = pk(gb0 * blo(yb), gb1 * bhi(yb));
    const u32 r = pk(blo(x1) + blo(t), bhi(x1) + bhi(t));
    if (lane < 8) *(GAS u16*)(out + (b * 512 + oc + (lane & 4)) * 2) = (u16)(lane < 4 ? r : r >> 16);
    STAMP(20);
    return;
  }

  if (wave == 3) {
    u32 tref = (u32)__builtin_amdgcn_s_memrealtime() + T00;
    const u32 tag = (u32)__builtin_amdgcn_readfirstlane((int)__builtin_amdgcn_raw_buffer_load_b32(rs, W_EPOCH, 0, SC1)) + 1u;
    *(volatile LAS v2u*)C_TAG = v2u{tag, 1u};
    asm volatile("s_mov_b64 exec, 0\n s_branch .Lldr_copies\n .Lwarm_ret0:\n s_mov_b64 exec, -1" ::: "memory");
    int uref = 0, tau = TAU * 64 / 100, kp = 1, kr = 1;
#pragma nounroll
    for (u32 spin = 0; spin < SPIN_CAP * 8; ++spin) {
      const v3u cc = *(volatile LAS v3u*)C_CONS;
      const int u = (int)lds_poll(C_LANDED);
      const u32 now = (u32)__builtin_amdgcn_s_memrealtime();
      const u32 c0 = __builtin_amdgcn_readfirstlane(cc.x), c1 = __builtin_amdgcn_readfirstlane(cc.y), pl = __builtin_amdgcn_readfirstlane(cc.z) ^ (0x88888888u >> (32 - 4 * NPRE));
      const u32 done = pl ? (__builtin_ctz(pl) & 28) * 2 : max(8u * NPRE, 2 * min(c0, c1));
      const int lag = (int)(now - tref) - (((u - uref) * tau) >> 6);
      const int lvl = min(max(LVL0 + ((lag * GAIN + 128) >> 8), (CLOSED && uref) ? 0 : LVL0), 7);
      *(volatile LAS u32*)C_CTL = ((done + 32) << 12) | (lvl << 7);
      if (u >= 496 && done >= 480) break;
      if (CLOSED) {
        if (u >= 64 * kp) {
          if ((idx & 3) == 0 && lane == 0) __builtin_amdgcn_raw_buffer_store_b64(v2u{now, tag}, rs, W_PROG + kp * 512 + ((idx >> 2) * 8 + xcd) * 8, 0, SC1);
          ++kp;
        }
        if (u >= 64 * kr + RDLAG) {
          const v2u pr = __builtin_amdgcn_raw_buffer_load_b64(rs, W_PROG + kr * 512 + lane * 8, 0, SC1);
          const float ago = wave_sum(pr.y == tag ? (float)(int)(now - pr.x) : 0.f);
          const u32 tk = now - (u32)__builtin_amdgcn_readfirstlane((int)(ago * (1.f / 64.f)));
          if (uref) tau = min(max((int)(tk - tref), 800), 1000);
          tref = tk; uref = 64 * kr; ++kr;
        }
      }
      __builtin_amdgcn_s_sleep(3);
    }
    return;
  }

  const int w = wave - 4;
  if (first_mod) mod_tile<false>(cnd, wmod, rs, b * 8 + xcd, w, lane);
  {
    v4u kr[NPRE][4];
    const u32 ad = w * 4096 + n * 256 + ((g ^ n) << 4);
#pragma unroll
    for (int r = 0; r < NPRE; ++r) {
#pragma nounroll
      for (u32 spin = 0; (int)lds_poll(C_LANDED) <= 8 * r + w && spin < SPIN_CAP * 8; ++spin) __builtin_amdgcn_s_sleep(8);
      CFENCE();
#pragma unroll
      for (int s = 0; s < 4; ++s) kr[r][s] = *lp<v4u>((ad ^ (s * 64)) + (r & 3) * 32768);
      asm volatile("" : "+v"(kr[r][0]), "+v"(kr[r][1]), "+v"(kr[r][2]), "+v"(kr[r][3]) :: "memory");
      lds_add(C_PULL, 1u << (4 * r));
    }
#pragma nounroll
    for (u32 spin = 0; lds_poll(C_QGOT) == 0u && spin < SPIN_CAP * 8; ++spin) __builtin_amdgcn_s_sleep(8);
    CFENCE();
    v4u qa[4];
#pragma unroll
    for (int s = 0; s < 4; ++s) qa[s] = *lp<v4u>(L_QV + 64 * s + 16 * g);
#pragma unroll
    for (int r = 0; r < NPRE; ++r) {
      f4 d = mfma32(qa[0], kr[r][0], zero4);
      d = mfma32(qa[1], kr[r][1], d); d = mfma32(qa[2], kr[r][2], d); d = mfma32(qa[3], kr[r][3], d);
      *lp<float>(L_SC + (w & 1) * 8192 + (4 * r + (w >> 1)) * 64 + ((n & 3) * 4 + (n >> 2)) * 4) = d[0];
    }
    lds_add(C_PRE, 1u);
  }
  const u32 tag = get_tag();
  const int fs = 7 * xcd + (pid & 7);
  v4u fo[4][4], fg[2][4], fd[4];
  {
    const __amdgpu_buffer_rsrc_t ro = __builtin_amdgcn_make_buffer_rsrc((void*)(wo + (size_t)h * 131072), 0, (NOW & 1) ? 0 : 131072, 0x00020000);
    const int vo = 2 * g * 1024 + (64 * w + 4 * n) * 2;
#pragma unroll
    for (int s = 0; s < 4; ++s) {
      v2u r[8];
#pragma unroll
      for (int j = 0; j < 8; ++j) { r[j] = __builtin_amdgcn_raw_buffer_load_b64(ro, vo + (j >> 2) * 1024, (32 * s + 8 * (j & 3)) * 1024, NT); PACE(j); }
      tr8x4(r, fo[s]);
      PIN4(fo[s]);
      SBAR();
    }
  }
  if (has_ffn) {
    const char* pg = (n < 8 ? wg : wu) + (fs * 32 + 4 * (n & 7)) * 2;
#pragma unroll
    for (int s = 0; s < 2; ++s) {
      v2u r[8];
      const char* pr = pg + (size_t)slice_col(16 * g + 2 * w + s, 0) * (FF * 2);
#pragma unroll
      for (int j = 0; j < 8; ++j) { r[j] = (NOW & 2) ? v2u{0u, 0u} : ldnt<v2u>(pr + j * (4 * FF * 2)); PACE(j); }
      tr8x4(r, fg[s]);
      PIN4(fg[s]);
      SBAR();
    }
    v2u r[8];
    const __amdgpu_buffer_rsrc_t rd = __builtin_amdgcn_make_buffer_rsrc((void*)(wd + (size_t)fs * 32768), 0, (NOW & 2) ? 0 : 32768, 0x00020000);
    const int vd = 8 * g * 1024 + (64 * w + 4 * n) * 2;
#pragma unroll
    for (int j = 0; j < 8; ++j) { r[j] = __builtin_amdgcn_raw_buffer_load_b64(rd, vd + (j & 3) * 1024, (j >> 2) * 4096, NT); PACE(j); }
    tr8x4(r, fd);
    PIN4(fd);
    SBAR();
  } else {
    mod_tile<true>(cnd, wmod, rs, 16 + b * 8 + xcd, w, lane);
#pragma unroll
    for (int t = 0; t < 4; ++t) fg[0][t] = fg[1][t] = fd[t] = v4u{0u, 0u, 0u, 0u};
    nap<12000>();
  }
  {
    v2u sv, cv;
    const int so = mod_off(b, 64 * w + lane);
#pragma nounroll
    for (u32 spin = 0; spin < SPIN_CAP; ++spin) {
      CFENCE();
      sv = __builtin_amdgcn_raw_buffer_load_b64(rs, so, 3 * 8192, SC1);
      cv = __builtin_amdgcn_raw_buffer_load_b64(rs, so, 4 * 8192, SC1);
      if (__all(((sv.y ^ tag) | (cv.y ^ tag)) == 0)) break;
      __builtin_amdgcn_s_sleep(64);
    }
    *lp<u32>(L_SBCB + ((8 * w + 2 * (lane & 3) + (lane >> 5)) * 8 + ((lane >> 2) & 7)) * 4) = pk(half_of(sv.x, lane & 1), 1.f + half_of(cv.x, lane & 1));
  }
  if (blockIdx.x == 0 && w == 7) {
#pragma nounroll
    for (u32 spin = 0; spin < SPIN_CAP; ++spin) {
      CFENCE();
      u32 bad = 0;
#pragma nounroll
      for (int i = 0; i < 8; ++i) {
        const v4u t = __builtin_amdgcn_raw_buffer_load_b128(rs, W_ML + lane * 16, i * 1024, SC1);
        bad |= (t.y ^ tag) | (t.w ^ tag);
      }
      if (__all(bad == 0)) break;
      __builtin_amdgcn_s_sleep(64);
    }
    if (lane == 0) __builtin_amdgcn_raw_buffer_store_b32(tag, rs, W_EPOCH, 0, SC1);
  }
  if (w == 0) STAMP(13);
  const int mo = (b * 64 + 8 * w + (lane >> 3)) * 4096 + (lane & 7) * 8;
  const int gs = 16 * g + ((n + 2 * w) & 15);
  asm volatile("s_waitcnt vmcnt(0) lgkmcnt(0)" ::: "memory");
  __builtin_amdgcn_s_barrier();
  CFENCE();
  {
    v4u oa[4];
#pragma unroll
    for (int s = 0; s < 4; ++s) oa[s] = *lp<v4u>(L_OA + (n & 3) * 320 + s * 64 + g * 16);
    SBAR();
    f4 d[4] = {zero4, zero4, zero4, zero4};
#pragma unroll
    for (int s = 0; s < 4; ++s)
#pragma unroll
      for (int t = 0; t < 4; ++t) d[t] = mfma32(oa[s], fo[s][t], d[t]);
    float y[4];
#pragma unroll
    for (int t = 0; t < 4; ++t) y[t] = (d[t][0] + d[t][2]) + (d[t][1] + d[t][3]);
    const float y01 = (g & 1) ? y[1] : y[0], y23 = (g & 1) ? y[3] : y[2];
    __builtin_amdgcn_raw_buffer_store_b64(v2u{fu((g & 2) ? y23 : y01), tag}, rs, mo, W_IN1 + pid * 64, SC1);
  }
  if (w == 0) STAMP(14);
  float sb[8], cb[8];
  {
    const v4u s0 = *lp<v4u>(L_SBCB + gs * 32), s1 = *lp<v4u>(L_SBCB + gs * 32 + 16);
#pragma unroll
    for (int e = 0; e < 4; ++e) { sb[e] = blo(s0[e]); cb[e] = bhi(s0[e]); sb[4 + e] = blo(s1[e]); cb[4 + e] = bhi(s1[e]); }
    PIN4(sb); PIN4(sb + 4); PIN4(cb); PIN4(cb + 4);
  }
  __builtin_amdgcn_s_barrier();
  delay(D2);
  v4u m0, m1;
#pragma nounroll
  for (u32 spin = 0; spin < SPIN_CAP; ++spin) {
    CFENCE();
    m0 = __builtin_amdgcn_raw_buffer_load_b128(rs, gs * 32, W_X1 + (b * 8 + xcd) * 2048, SC1);
    m1 = __builtin_amdgcn_raw_buffer_load_b128(rs, gs * 32 + 16, W_X1 + (b * 8 + xcd) * 2048, SC1);
    if (__all(((m0.y ^ tag) | (m0.w ^ tag) | (m1.y ^ tag) | (m1.w ^ tag)) == 0)) break;
  }
  if (w == 0) STAMP(17);
  {
    const float v[8] = {blo(m0.x), bhi(m0.x), blo(m0.z), bhi(m0.z), blo(m1.x), bhi(m1.x), blo(m1.z), bhi(m1.z)};
    const float sh = uf((u32)__builtin_amdgcn_readlane((int)fu(v[0]), (16 - 2 * w) & 15));
    float d[8];
#pragma unroll
    for (int e = 0; e < 8; ++e) d[e] = v[e] - sh;
    float s1 = ((d[0] + d[1]) + (d[2] + d[3])) + ((d[4] + d[5]) + (d[6] + d[7]));
    float s2 = (fmaf(d[0], d[0], d[1] * d[1]) + fmaf(d[2], d[2], d[3] * d[3])) + (fmaf(d[4], d[4], d[5] * d[5]) + fmaf(d[6], d[6], d[7] * d[7]));
    s1 = wave_sum(s1) * (1.f / 512.f); s2 = wave_sum(s2) * (1.f / 512.f);
    const float mb = rbf(sh + s1), var = fmaxf(s2 - s1 * s1, 0.f);
    const float rsd = __builtin_amdgcn_rcpf(rbf(__builtin_amdgcn_sqrtf(rbf(rbf(var) + 1e-5f))));
    v4u ha;
#pragma unroll
    for (int e = 0; e < 4; ++e) ha[e] = pk(fmaf((v[2 * e] - mb) * rsd, cb[2 * e], sb[2 * e]), fmaf((v[2 * e + 1] - mb) * rsd, cb[2 * e + 1], sb[2 * e + 1]));
    f4 gp;
#pragma unroll
    for (int t = 0; t < 4; ++t) gp[t] = mfma32(ha, fg[0][t], zero4)[0] + mfma32(ha, fg[1][t], zero4)[1];
    if (g == 0) *lp<f4>(L_GURED + w * 256 + n * 16) = gp;
  }
  asm volatile("s_waitcnt lgkmcnt(0)" ::: "memory");
  __builtin_amdgcn_s_barrier();
  CFENCE();
  {
    float pt[8];
#pragma unroll
    for (int ww = 0; ww < 8; ++ww) pt[ww] = *lp<float>(L_GURED + ww * 256 + ((n >> 3) * 32 + 8 * g + (n & 7)) * 4);
    const float s = rbf(((pt[0] + pt[1]) + (pt[2] + pt[3])) + ((pt[4] + pt[5]) + (pt[6] + pt[7])));
    const float up = dppf<ROR8>(s);
    const float z = s * fmaf(s * s, 0.044715f * 0.7978845608028654f * 2.f * LOG2E, 0.7978845608028654f * 2.f * LOG2E);
    const float ge = rbf(s * __builtin_amdgcn_rcpf(1.f + __builtin_amdgcn_exp2f(-z)));
    const float act = ge * up;
    const u32 ap = pk(act, dppf<XOR1>(act));
    const v4u aa = {dppu<0x150>(ap), dppu<0x152>(ap), dppu<0x154>(ap), dppu<0x156>(ap)};
    float y[4];
#pragma unroll
    for (int t = 0; t < 4; ++t) y[t] = mfma32(aa, fd[t], zero4)[0];
    const float y01 = (g & 1) ? y[1] : y[0], y23 = (g & 1) ? y[3] : y[2];
    if (has_ffn) __builtin_amdgcn_raw_buffer_store_b64(v2u{fu((g & 2) ? y23 : y01), tag}, rs, mo, W_IN3 + fs * 64, SC1);
  }
  if (w == 0) STAMP(18);
}

}

namespace diag {
typedef unsigned u32; typedef unsigned long long u64;
__global__ void k_t0(u64* p) { if (threadIdx.x == 0) *p = __builtin_amdgcn_s_memrealtime(); }
__device__ u32 lat(const char* p) {
  p = (const char*)((u64)p & ~2047ull); u32 best = 100000;
  for (int r = 0; r < 7; ++r) {
    u64 t0, t1; u32 v; const char* q = p + r * 256 + 128;
    asm volatile("s_memtime %0\n s_waitcnt lgkmcnt(0)\n global_load_dword %2, %3, off nt\n s_waitcnt vmcnt(0)\n s_memtime %1\n s_waitcnt lgkmcnt(0)" : "=&s"(t0), "=&s"(t1), "=&v"(v) : "v"(q) : "memory");
    u32 d = (u32)(t1 - t0); if (r > 0 && d > 420 && d < best) best = d;
  }
  return best;
}
__device__ int side(const char* p) { return lat(p) < lat(p + 8192); }
__global__ void k_diag(const char* K, const char* V, const char* ws, u64* t0p, int mode, u32* dbg) {
  if (threadIdx.x != 0) return;
  u32 val = 0;
  const size_t M2 = 2u << 20;
  if (mode == 0) val = ((u64)K >> 8) & 0x1fff;
  if (mode == 1) val = ((u64)V >> 8) & 0x1fff;
  if (mode == 2 || mode == 3) { const char* B = (mode == 2 ? K : V) + 65536; int prev = side(B), same = 0; for (int i = 1; i < 64; ++i) { int s = side(B + (size_t)i * M2); same += s == prev; prev = s; }
    int ch = 0; B += 3 * M2; prev = side(B); for (int j = 1; j < 64; ++j) { int s = side(B + (size_t)j * 32768 ); ch += s != prev; prev = s; } val = same * 64 + ch; }
  if (mode == 4) val = (((u64)K >> 21) & 0x3f) * 64 + (((u64)V >> 21) & 0x3f);
  if (mode == 5) val = ((u64)ws >> 8) & 0x1fff;
  if (dbg) *dbg = val;
  const u64 end = *t0p + 150000 + (u64)val * 400;
  while (__builtin_amdgcn_s_memrealtime() < end) __builtin_amdgcn_s_sleep(8);
}
}
extern "C" void kernel_launch(void* const* d_in, const int* in_sizes, int n_in, void* d_out, int out_size, void* d_ws, size_t ws_size, hipStream_t stream) {
  diag::k_t0<<<1, 64, 0, stream>>>((diag::u64*)((char*)d_ws + (64 << 20)));

  hipLaunchKernelGGL(blk::fused_block, dim3(256), dim3(blk::NTHR), 0, stream,
                     (const char*)d_in[0], (const char*)d_in[1], (const char*)d_in[2], (const char*)d_in[3], (const char*)d_in[4], (const char*)d_in[6],
                     (const char*)d_in[7], (const char*)d_in[8], (const char*)d_in[9], (const char*)d_in[10], (char*)d_out, (char*)d_ws);
  diag::k_diag<<<1, 64, 0, stream>>>((const char*)d_in[1], (const char*)d_in[2], (const char*)d_ws, (diag::u64*)((char*)d_ws + (64 << 20)), 0, (diag::u32*)((char*)d_ws + (64 << 20) + 64));
}
```

```cpp
#define STAMPS 1
#include <hip/hip_runtime.h>
#pragma clang diagnostic ignored "-Wint-to-pointer-cast"

#define WLEVELS "17,18,19,20,21,23,25,28"
#define LVL0 2
#define U0 5
#define TAU 1400
#define T00 250
#define GAIN 8
#define CLOSED 1
#define RDLAG 24
#define NPRE 8
#define CSLEEP 64
#define HSLEEP 16
#define QSLEEP 16
#define HDELAY 0
#define QDELAY 2000
#define WQFLY 3
#define PACEN 1
#define NOW 0
#define D1 1200
#define D2 500
#define D3 1100

namespace blk {

typedef unsigned short u16;
typedef unsigned u32;
typedef unsigned long long u64;
typedef u32 v4u __attribute__((ext_vector_type(4)));
typedef u32 v3u __attribute__((ext_vector_type(3)));
typedef u32 v2u __attribute__((ext_vector_type(2)));
typedef float f4 __attribute__((ext_vector_type(4)));
typedef float f2 __attribute__((ext_vector_type(2)));
typedef __bf16 bf8 __attribute__((ext_vector_type(8)));
typedef __bf16 bf2 __attribute__((ext_vector_type(2)));
typedef short s4 __attribute__((ext_vector_type(4)));
#define GAS __attribute__((address_space(1)))
#define LAS __attribute__((address_space(3)))

constexpr int NTHR = 768;
constexpr int FF = 1792;
constexpr int L_SC = 131072;
constexpr int L_MODRED = L_SC + 16384;
constexpr int L_GURED = L_MODRED + 8192;
constexpr int L_SBCB = L_GURED + 2048;
constexpr int L_OA = L_SBCB + 2048;
constexpr int L_QV = L_OA + 1280;
constexpr int L_CTRL = L_QV + 256;
constexpr int C_CONS = L_CTRL, C_PULL = L_CTRL + 8, C_MODCNT = L_CTRL + 12, C_PRE = L_CTRL + 16, C_QGOT = L_CTRL + 20, C_TAG = L_CTRL + 24;
constexpr int C_CTL = L_CTRL + 32;
constexpr int C_LANDED = L_CTRL + 48;
constexpr int L_STAMP = C_LANDED + 256;
constexpr int LDS_BYTES = L_STAMP + (STAMPS ? 256 : 0);
constexpr int W_EPOCH = 0, W_PROG = 4096, W_ML = 16384;
constexpr int W_MOD = 65536;
constexpr int W_Q = 131072;
constexpr int W_X1 = 196608;
constexpr int W_IN1 = 262144, W_IN3 = W_IN1 + (1 << 20);
constexpr int W_END = W_IN3 + (1 << 20), W_DBG = W_END;
constexpr int SC1 = 16, NT = 2;
constexpr u32 SPIN_CAP = 1u << 18;

__device__ __forceinline__ float blo(u32 u) { return __builtin_bit_cast(float, u << 16); }
__device__ __forceinline__ float bhi(u32 u) { return __builtin_bit_cast(float, u & 0xffff0000u); }
__device__ __forceinline__ u32 pk(float a, float b) { f2 p = {a, b}; return __builtin_bit_cast(u32, __builtin_convertvector(p, bf2)); }
__device__ __forceinline__ float rbf(float a) { return blo(pk(a, a)); }
__device__ __forceinline__ u32 fu(float a) { return __builtin_bit_cast(u32, a); }
__device__ __forceinline__ float uf(u32 a) { return __builtin_bit_cast(float, a); }
__device__ __forceinline__ f4 mfma32(v4u a, v4u b, f4 c) { return __builtin_amdgcn_mfma_f32_16x16x32_bf16(__builtin_bit_cast(bf8, a), __builtin_bit_cast(bf8, b), c, 0, 0, 0); }
__device__ __forceinline__ f4 mfma16(v2u a, v2u b, f4 c) { return __builtin_amdgcn_mfma_f32_16x16x16bf16_1k(__builtin_bit_cast(s4, a), __builtin_bit_cast(s4, b), c, 0, 0, 0); }
template <int CTRL> __device__ __forceinline__ u32 dppu(u32 x) { return (u32)__builtin_amdgcn_mov_dpp((int)x, CTRL, 0xf, 0xf, true); }
template <int CTRL> __device__ __forceinline__ float dppf(float x) { return uf(dppu<CTRL>(fu(x))); }
constexpr int XOR1 = 0xB1, XOR2 = 0x4E, HMIR = 0x141, ROR4 = 0x124, ROR8 = 0x128;
__device__ __forceinline__ float xrow_sum(float x) {
  auto s = __builtin_amdgcn_permlane16_swap(fu(x), fu(x), false, false);
  x = uf(s[0]) + uf(s[1]);
  auto t = __builtin_amdgcn_permlane32_swap(fu(x), fu(x), false, false);
  return uf(t[0]) + uf(t[1]);
}
__device__ __forceinline__ float xrow_max(float x) {
  auto s = __builtin_amdgcn_permlane16_swap(fu(x), fu(x), false, false);
  x = fmaxf(uf(s[0]), uf(s[1]));
  auto t = __builtin_amdgcn_permlane32_swap(fu(x), fu(x), false, false);
  return fmaxf(uf(t[0]), uf(t[1]));
}
__device__ __forceinline__ float wave_sum(float x) { x += dppf<XOR1>(x); x += dppf<XOR2>(x); x += dppf<HMIR>(x); x += dppf<ROR8>(x); return xrow_sum(x); }
__device__ __forceinline__ float wave_max(float x) { x = fmaxf(x, dppf<XOR1>(x)); x = fmaxf(x, dppf<XOR2>(x)); x = fmaxf(x, dppf<HMIR>(x)); x = fmaxf(x, dppf<ROR8>(x)); return xrow_max(x); }
__device__ __forceinline__ u32 perm_lo(u32 r0, u32 r1) { return __builtin_amdgcn_perm(r1, r0, 0x05040100u); }
__device__ __forceinline__ u32 perm_hi(u32 r0, u32 r1) { return __builtin_amdgcn_perm(r1, r0, 0x07060302u); }
#define CFENCE() asm volatile("" ::: "memory")
#define SBAR() __builtin_amdgcn_sched_barrier(0)
#define PIN4(a) asm volatile("" : "+v"((a)[0]), "+v"((a)[1]), "+v"((a)[2]), "+v"((a)[3]))
template <typename T> __device__ __forceinline__ LAS T* lp(u32 off) { return (LAS T*)off; }
__device__ __forceinline__ u32 lds_poll(u32 off) { return (u32)__builtin_amdgcn_readfirstlane((int)*(volatile LAS u32*)off); }
__device__ __forceinline__ void lds_add(u32 off, u32 v) {
  asm volatile("s_mov_b64 exec, 1\n ds_add_u32 %0, %1\n s_mov_b64 exec, -1" :: "v"(off), "v"(v) : "memory");
}
template <typename T> __device__ __forceinline__ T ldnt(const void* p) { return __builtin_nontemporal_load((const GAS T*)p); }
template <typename T> __device__ __forceinline__ T ldg(const void* p) { return *(const GAS T*)p; }
__device__ __forceinline__ void tr8x4(const v2u (&r)[8], v4u (&f)[4]) {
#pragma unroll
  for (int q = 0; q < 4; ++q) {
    f[0][q] = perm_lo(r[2 * q].x, r[2 * q + 1].x); f[1][q] = perm_hi(r[2 * q].x, r[2 * q + 1].x);
    f[2][q] = perm_lo(r[2 * q].y, r[2 * q + 1].y); f[3][q] = perm_hi(r[2 * q].y, r[2 * q + 1].y);
  }
}
__device__ __forceinline__ int slice_col(int s, int pos) { return 64 * (s >> 3) + 32 * (s & 1) + 4 * pos + ((s >> 1) & 3); }

#define STAMP(i) do { SBAR(); const u64 t_ = __builtin_amdgcn_s_memrealtime(); *(volatile LAS u64*)(L_STAMP + (i) * 8) = t_; SBAR(); } while (0)

__device__ __forceinline__ int mod_off(int b, int c) { return W_MOD + (c >> 6) * 1024 + b * 256 + ((c & 63) >> 1) * 8; }
__device__ __forceinline__ float half_of(u32 v, int odd) { return odd ? bhi(v) : blo(v); }
__device__ __forceinline__ void delay(int cyc) {
  const u64 t0 = __builtin_amdgcn_s_memtime();
#pragma nounroll
  while ((int)(__builtin_amdgcn_s_memtime() - t0) < cyc) { }
}
template <int CYC> __device__ __forceinline__ void nap() {
#pragma unroll
  for (int i = 0; i < CYC / 8128; ++i) __builtin_amdgcn_s_sleep(127);
  if (CYC % 8128 >= 64) __builtin_amdgcn_s_sleep((CYC % 8128) / 64);
}
__device__ __forceinline__ u32 get_tag() {
  v2u t;
#pragma nounroll
  for (u32 spin = 0; spin < SPIN_CAP * 8; ++spin) { t = *(volatile LAS v2u*)C_TAG; if (__builtin_amdgcn_readfirstlane((int)t.y)) break; }
  return (u32)__builtin_amdgcn_readfirstlane((int)t.x);
}
__device__ __forceinline__ u32 inbox_sum(const v4u (&m)[4]) {
  float y0 = (uf(m[0].x) + uf(m[1].x)) + (uf(m[2].x) + uf(m[3].x)), y1 = (uf(m[0].z) + uf(m[1].z)) + (uf(m[2].z) + uf(m[3].z));
  y0 += dppf<ROR4>(y0); y1 += dppf<ROR4>(y1); y0 += dppf<ROR8>(y0); y1 += dppf<ROR8>(y1);
  return pk(xrow_sum(y0), xrow_sum(y1));
}
#define PACE(j) do { if (((j) % PACEN) == PACEN - 1) { asm volatile("s_waitcnt vmcnt(0)" ::: "memory"); SBAR(); } } while (0)
constexpr float LOG2E = 1.4426950408889634f;

template <bool PACED>
__device__ __forceinline__ void mod_tile(const char* cnd, const char* wmod, __amdgpu_buffer_rsrc_t rs, int cb, int w, int lane) {
  const int g = lane >> 4, n = lane & 15;
  const f4 zero4 = {0.f, 0.f, 0.f, 0.f};
  const v2u cv = ldnt<v2u>(cnd + ((n >> 2) * 512 + 64 * w + 16 * g + 4 * (n & 3)) * 2);
  const __amdgpu_buffer_rsrc_t rm = __builtin_amdgcn_make_buffer_rsrc((void*)(wmod + (size_t)(64 * w) * 6144 + cb * 128), 0, 64 * 6144, 0x00020000);
  v2u r[2][8];
#pragma unroll
  for (int j = 0; j < 16; ++j) { r[j >> 3][j & 7] = __builtin_amdgcn_raw_buffer_load_b64(rm, 16 * g * 6144 + n * 8, j * 6144, NT); if (PACED) PACE(j); }
  SBAR();
  u32 sp[2];
#pragma unroll
  for (int e = 0; e < 2; ++e) {
    const float c0 = blo(cv[e]), c1 = bhi(cv[e]);
    sp[e] = pk(c0 * __builtin_amdgcn_rcpf(1.f + __builtin_amdgcn_exp2f(-c0 * LOG2E)), c1 * __builtin_amdgcn_rcpf(1.f + __builtin_amdgcn_exp2f(-c1 * LOG2E)));
  }
  const v4u a0 = {dppu<0x00>(sp[0]), dppu<0x00>(sp[1]), dppu<0x55>(sp[0]), dppu<0x55>(sp[1])};
  const v4u a1 = {dppu<0xAA>(sp[0]), dppu<0xAA>(sp[1]), dppu<0xFF>(sp[0]), dppu<0xFF>(sp[1])};
  v4u f0[4], f1[4];
  tr8x4(r[0], f0); tr8x4(r[1], f1);
  f4 pt;
#pragma unroll
  for (int t = 0; t < 4; ++t) pt[t] = mfma32(a1, f1[t], mfma32(a0, f0[t], zero4))[0];
  *lp<f4>(L_MODRED + w * 1024 + lane * 16) = pt;
  u32 old;
  asm volatile("s_mov_b64 exec, 1\n ds_add_rtn_u32 %0, %1, %2\n s_mov_b64 exec, -1\n s_waitcnt lgkmcnt(0)" : "=&v"(old) : "v"(C_MODCNT), "v"(1u) : "memory");
  if ((__builtin_amdgcn_readfirstlane((int)old) & 7) == 7) {
    f4 p[8];
#pragma unroll
    for (int ww = 0; ww < 8; ++ww) p[ww] = *lp<f4>(L_MODRED + ww * 1024 + lane * 16);
    const f4 v = ((p[0] + p[1]) + (p[2] + p[3])) + ((p[4] + p[5]) + (p[6] + p[7]));
    const u32 tag = get_tag();
    __builtin_amdgcn_raw_buffer_store_b128(v4u{pk(v[0], v[1]), tag, pk(v[2], v[3]), tag}, rs, lane * 16 + W_MOD + cb * 1024, 0, SC1);
    if (!PACED) STAMP(9);
  }
}

__global__ void __launch_bounds__(NTHR) fused_block(const char* x, const char* kc, const char* vc, const char* wq, const char* wo, const char* wg,
                                                    const char* wu, const char* wd, const char* cnd, const char* wmod, char* out, char* ws) {
  __shared__ __attribute__((aligned(1024))) char lds_mem[LDS_BYTES];
  const int lane = threadIdx.x & 63, wave = __builtin_amdgcn_readfirstlane(threadIdx.x >> 6);
  const int xcd = blockIdx.x & 7, idx = blockIdx.x >> 3;
  const int half = idx & 1, hh = (idx >> 1) & 3, b = idx >> 3;
  const int h = xcd + 8 * hh;
  const int pid = 8 * xcd + (idx & 7);
  const bool has_ffn = (idx & 7) < 7;
  const bool first_mod = !has_ffn && b < 2;
  const int g = lane >> 4, n = lane & 15;
  const f4 zero4 = {0.f, 0.f, 0.f, 0.f};
  const u64 t_start = __builtin_amdgcn_s_memrealtime();
  if (threadIdx.x < (STAMPS ? 140 : 16)) *lp<u32>(L_CTRL + 4 * threadIdx.x) = threadIdx.x == 8 ? (32u << 12) | (LVL0 * 128) : 0u;
  asm volatile("s_waitcnt lgkmcnt(0)" :: "v"(lds_mem), "s"(kc), "s"(ws) : "memory");
  __builtin_amdgcn_s_barrier();

  if (wave == 0) {
    __builtin_amdgcn_s_setprio(3);
    constexpr u32 HOLES = 0xA000u;
    const size_t reg_off = ((size_t)(b * 32 + h) << 21) + half * 32768 - HOLES;
    u32 o0 = g * 256 + ((n ^ g) << 4), o1 = o0 ^ 64, o2 = (o0 ^ 128) + 6144, o3 = (o0 ^ 192) + 6144;
    const u32 va = C_LANDED + lane * 4;
    const u64 kb = (u64)(kc + reg_off), vb = (u64)(vc + reg_off);
    v4u rsrc = {(u32)kb, (u32)(kb >> 32) & 0xffffu, (2u << 20) + 0x2000u, 0x00020000u};
    STAMP(5);
    if (first_mod) __builtin_amdgcn_s_sleep(CSLEEP);
    int pub = -7;
    u32 ctl = (32u << 12) | (LVL0 * 128);
#pragma nounroll
    for (u32 end = 1u << 20; end <= (2u << 20); end += 1u << 20) {
      u32 off = end - (1u << 20), src = HOLES, spin = SPIN_CAP * 8, blk, tmp, pcl, pch;
#define LD(v, o) "s_waitcnt vmcnt(\\w)\n buffer_load_dwordx4 %[" #v "], %[rs], %[src] offen offset:" #o " nt lds\n"
      asm volatile(
        "s_getpc_b64 vcc\n"
        ".Lldr_pc:\n s_add_u32 %[pcl], vcc_lo, .Lldr_copies-.Lldr_pc\n s_addc_u32 %[pch], vcc_hi, 0\n"
        ".Lldr_next:\n s_waitcnt lgkmcnt(4)\n"
        ".Lldr_dec:\n v_readfirstlane_b32 %[tmp], %[ctl]\n ds_read_b32 %[ctl], %[ca]\n s_add_u32 %[blk], %[off], 0x4000\n"
        "s_cmp_gt_u32 %[blk], %[tmp]\n s_cbranch_scc1 .Lldr_stall\n"
        "s_and_b32 %[tmp], %[tmp], 0x380\n s_add_u32 vcc_lo, %[pcl], %[tmp]\n s_addc_u32 vcc_hi, %[pch], 0\n s_setpc_b64 vcc\n"
        ".Lldr_stall:\n s_waitcnt lgkmcnt(0)\n s_sub_u32 %[spin], %[spin], 1\n s_cbranch_scc0 .Lldr_dec\n s_branch .Lldr_exit\n"
        ".p2align 7\n.Lldr_copies:\n"
        ".irp w," WLEVELS "\n"
        "s_cbranch_execz 2f\n"
        "1:\n s_and_b32 m0, %[off], 0x1ffff\n" LD(v0, 0) "s_add_u32 %[off], %[off], 0x1000\n" LD(v1, 1024) LD(v2, 2048) LD(v3, 3072)
        "ds_write_b32 %[va], %[pub]\n s_add_u32 %[src], %[src], 0x800\n s_or_b32 %[src], %[src], 0xa000\n v_add_u32 %[pub], 1, %[pub]\n"
        "s_cmp_lt_u32 %[off], %[blk]\n s_cbranch_scc1 1b\n s_cmp_lt_u32 %[off], %[end]\n s_cbranch_scc1 .Lldr_next\n s_branch .Lldr_exit\n"
        ".p2align 7\n2:\n.endr\n"
        "s_branch .Lwarm_ret0\n"
        ".Lldr_exit:\n s_waitcnt lgkmcnt(0)"
        : [off] "+&s"(off), [src] "+&s"(src), [spin] "+&s"(spin), [blk] "=&s"(blk), [tmp] "=&s"(tmp), [pcl] "=&s"(pcl), [pch] "=&s"(pch), [pub] "+&v"(pub), [ctl] "+&v"(ctl)
        : [v0] "v"(o0), [v1] "v"(o1), [v2] "v"(o2), [v3] "v"(o3), [rs] "s"(rsrc), [end] "s"(end), [va] "v"(va), [ca] "v"(C_CTL) : "memory", "scc", "vcc");
      rsrc.x = (u32)vb; rsrc.y = (u32)(vb >> 32) & 0xffffu;
      o0 = o1 = lane * 16; o2 = o3 = lane * 16 + 6144;
    }
#pragma unroll
    for (int k = 6; k >= 0; --k) {
      const u32 pb = 512 - k;
      asm volatile("s_waitcnt vmcnt(%2)\n ds_write_b32 %0, %1" :: "v"(va), "v"(pb), "i"(4 * k) : "memory");
    }
    *(volatile LAS u64*)L_STAMP = t_start;
    if (first_mod) STAMP(31);
    *(volatile LAS u64*)(L_STAMP + 24 * 8) = (u32)__builtin_amdgcn_s_getreg(20 | (3 << 11)) & 0xfu;
    STAMP(1);
    return;
  }

  const __amdgpu_buffer_rsrc_t rs = __builtin_amdgcn_make_buffer_rsrc((void*)ws, 0, W_END, 0x00020000);

  if (wave <= 2) {
    const int a = wave - 1;
    u32 tag;
    if (a == 0) {
      const int beta = n >> 2, kq = 16 * g + 4 * (n & 3);
      v2u xr[8];
#pragma unroll
      for (int i = 0; i < 8; ++i) xr[i] = ldnt<v2u>(x + (beta * 512 + 64 * i + kq) * 2);
      SBAR();
      if (first_mod) {
#pragma nounroll
        for (u32 spin = 0; lds_poll(C_MODCNT) == 0u && spin < SPIN_CAP * 8; ++spin) __builtin_amdgcn_s_sleep(2);
      }
      {
        const char* wt = wq + (h * 128 + (2 * b + half) * 16) * 2;
        const u32 vo = (64 * (lane >> 5) + 16 * ((lane >> 3) & 3) + ((lane >> 1) & 3)) * 8192 + (lane & 1) * 16;
#pragma nounroll
        for (int p = 0; p < 16; ++p)
          asm volatile("s_mov_b32 m0, %2\n s_waitcnt vmcnt(%3)\n global_load_lds_dwordx4 %0, %1 nt"
                       :: "v"(vo), "s"(wt + (size_t)(128 * (p >> 2) + 4 * (p & 3)) * 8192), "s"(L_SC + p * 1024), "i"(WQFLY - 1) : "memory");
      }
      SBAR();
      {
        float s = 0.f;
#pragma unroll
        for (int i = 0; i < 8; ++i) s += (blo(xr[i].x) + bhi(xr[i].x)) + (blo(xr[i].y) + bhi(xr[i].y));
        s += dppf<XOR1>(s); s += dppf<XOR2>(s);
        const float mean = xrow_sum(s) * (1.f / 512.f);
        float q = 0.f;
#pragma unroll
        for (int i = 0; i < 8; ++i)
#pragma unroll
          for (int e = 0; e < 2; ++e) { const float d0 = blo(xr[i][e]) - mean, d1 = bhi(xr[i][e]) - mean; q = fmaf(d0, d0, q); q = fmaf(d1, d1, q); }
        q += dppf<XOR1>(q); q += dppf<XOR2>(q);
        const float mb = rbf(mean), rsd = __builtin_amdgcn_rcpf(rbf(__builtin_amdgcn_sqrtf(rbf(rbf(xrow_sum(q) * (1.f / 512.f)) + 1e-5f))));
#pragma unroll
        for (int i = 0; i < 8; ++i)
#pragma unroll
          for (int e = 0; e < 2; ++e) { const u32 d = pk(blo(xr[i][e]) - mb, bhi(xr[i][e]) - mb); xr[i][e] = pk(blo(d) * rsd, bhi(d) * rsd); }
        PIN4(xr); PIN4(xr + 4);
      }
      STAMP(10);
      tag = get_tag();
      nap<HDELAY>();
      u32 ha[8][2];
#pragma nounroll
      for (u32 spin = 0; spin < SPIN_CAP; ++spin) {
        CFENCE();
        bool go = spin == 0;
        if (!go) go = __all(__builtin_amdgcn_raw_buffer_load_b64(rs, W_MOD + n * 1024 + 1016, 0, SC1)[1] == tag);
        if (go) {
          u32 bad = 0;
#pragma unroll
          for (int hf = 0; hf < 2; ++hf) {
            v4u sa[4], ca[4];
#pragma unroll
            for (int i = 0; i < 4; ++i) {
              sa[i] = __builtin_amdgcn_raw_buffer_load_b128(rs, beta * 256 + kq * 4 + i * 1024, W_MOD + hf * 4096, SC1);
              ca[i] = __builtin_amdgcn_raw_buffer_load_b128(rs, beta * 256 + kq * 4 + i * 1024, W_MOD + 8192 + hf * 4096, SC1);
            }
#pragma unroll
            for (int i = 0; i < 4; ++i) {
              bad |= (sa[i].y ^ tag) | (sa[i].w ^ tag) | (ca[i].y ^ tag) | (ca[i].w ^ tag);
#pragma unroll
              for (int e = 0; e < 2; ++e) {
                const u32 c1 = pk(1.f + blo(ca[i][2 * e]), 1.f + bhi(ca[i][2 * e]));
                const u32 t = pk(blo(xr[4 * hf + i][e]) * blo(c1), bhi(xr[4 * hf + i][e]) * bhi(c1));
                ha[4 * hf + i][e] = pk(blo(t) + blo(sa[i][2 * e]), bhi(t) + bhi(sa[i][2 * e]));
              }
            }
          }
          if (__all(bad == 0)) break;
        }
        __builtin_amdgcn_s_sleep(HSLEEP);
      }
      asm volatile("s_waitcnt vmcnt(0)" ::: "memory");
      STAMP(11);
      f4 acc[4] = {zero4, zero4, zero4, zero4};
#pragma unroll
      for (int p = 0; p < 16; ++p) {
        const v2u lo = __builtin_bit_cast(v2u, __builtin_amdgcn_ds_read_tr16_b64_v4i16(lp<s4>(L_SC + p * 1024 + lane * 8)));
        const v2u hi = __builtin_bit_cast(v2u, __builtin_amdgcn_ds_read_tr16_b64_v4i16(lp<s4>(L_SC + p * 1024 + 512 + lane * 8)));
        const int sg = p >> 2;
        acc[p & 3] = mfma32(v4u{ha[2 * sg][0], ha[2 * sg][1], ha[2 * sg + 1][0], ha[2 * sg + 1][1]}, v4u{lo.x, lo.y, hi.x, hi.y}, acc[p & 3]);
      }
      const float qv = (acc[0][0] + acc[1][1]) + (acc[2][2] + acc[3][3]);
      const u32 pr = pk(qv, dppf<XOR1>(qv));
      if (!(n & 1)) __builtin_amdgcn_raw_buffer_store_b64(v2u{pr, tag}, rs, W_Q + (h * 4 + g) * 512 + (2 * b + half) * 64 + (n >> 1) * 8, 0, SC1);
      STAMP(12);
      nap<QDELAY>();
      v2u m;
#pragma nounroll
      for (u32 spin = 0; spin < SPIN_CAP; ++spin) {
        CFENCE();
        m = __builtin_amdgcn_raw_buffer_load_b64(rs, lane * 8, W_Q + (h * 4 + b) * 512, SC1);
        if (__all(m.y == tag)) break;
        __builtin_amdgcn_s_sleep(QSLEEP);
      }
      *lp<u32>(L_QV + lane * 4) = m.x;
      CFENCE();
      *(volatile LAS u32*)C_QGOT = 1u;
    } else {
#pragma nounroll
      for (u32 spin = 0; lds_poll(C_QGOT) == 0u && spin < SPIN_CAP * 8; ++spin) __builtin_amdgcn_s_sleep(4);
      tag = get_tag();
    }
    CFENCE();
    v4u qa[4];
#pragma unroll
    for (int s = 0; s < 4; ++s) qa[s] = *lp<v4u>(L_QV + 64 * s + 16 * g);
    if (a == 0) STAMP(2);
    const u32 sc = L_SC + a * 8192;
    int landed = 0;
    const u32 kofs = n * 256 + ((g ^ n) << 4);
    const u32 spos = sc + ((n & 3) * 4 + (n >> 2)) * 4;
#pragma nounroll
    for (int j = 4 * NPRE; j < 128; ++j) {
      const int u = 2 * j + a;
#pragma nounroll
      for (u32 spin = 0; landed <= u && spin < SPIN_CAP * 8; ++spin) landed = (int)lds_poll(C_LANDED);
      CFENCE();
      const u32 ad = ((u & 31) << 12) + kofs;
      v4u k0 = *lp<v4u>(ad), k1 = *lp<v4u>(ad ^ 64), k2 = *lp<v4u>(ad ^ 128), k3 = *lp<v4u>(ad ^ 192);
      asm volatile("" : "+v"(k0), "+v"(k1), "+v"(k2), "+v"(k3) :: "memory");
      *(volatile LAS u32*)(C_CONS + 4 * a) = j + 1;
      f4 d = mfma32(qa[0], k0, zero4);
      d = mfma32(qa[1], k1, d); d = mfma32(qa[2], k2, d); d = mfma32(qa[3], k3, d);
      *lp<float>(spos + j * 64) = d[0];
    }
#pragma nounroll
    for (u32 spin = 0; lds_poll(C_PRE) != 8u && spin < SPIN_CAP * 8; ++spin) { }
    CFENCE();
    if (a == 0) STAMP(3);
    float mw, lw;
    {
      f4 s[8];
      float mx = -INFINITY;
#pragma unroll
      for (int t = 0; t < 8; ++t) {
        s[t] = *lp<f4>(sc + lane * 16 + t * 1024);
        const u32 p0 = pk(s[t][0], s[t][1]), p1 = pk(s[t][2], s[t][3]);
        const u32 r0 = pk(blo(p0) * 0.08838834764831845f, bhi(p0) * 0.08838834764831845f), r1 = pk(blo(p1) * 0.08838834764831845f, bhi(p1) * 0.08838834764831845f);
        s[t] = f4{blo(r0), bhi(r0), blo(r1), bhi(r1)};
        mx = fmaxf(fmaxf(mx, fmaxf(s[t][0], s[t][1])), fmaxf(s[t][2], s[t][3]));
      }
      mw = wave_max(mx);
      float sum = 0.f;
#pragma unroll
      for (int t = 0; t < 8; ++t) {
        v4u pp;
#pragma unroll
        for (int e = 0; e < 4; ++e) { const float p = __builtin_amdgcn_exp2f((s[t][e] - mw) * LOG2E); sum += p; pp[e] = pk(p, p); }
        *lp<v4u>(sc + lane * 16 + t * 1024) = pp;
      }
      lw = wave_sum(sum);
    }
    const int mlbase = W_ML + (b * 32 + h) * 64;
    if (lane == 0) __builtin_amdgcn_raw_buffer_store_b128(v4u{fu(mw), tag, fu(lw), tag}, rs, mlbase + (half * 2 + a) * 16, 0, SC1);
    u32 msk[4];
#pragma unroll
    for (int q = 0; q < 4; ++q) msk[q] = ((n & 7) >> 1) == q ? ((n & 1) ? 0xffff0000u : 0x0000ffffu) : 0u;
    f4 acc = zero4;
    float scale = 0.f, ga0 = 0.f, ga1 = 0.f, gb0 = 0.f, gb1 = 0.f;
    u32 xo0 = 0, xo1 = 0;
    const int oc = slice_col(pid, 2 * (lane & 3));
#pragma nounroll
    for (int j = 0; j < 128; ++j) {
      const int u = 256 + 2 * j + a;
      if (j == 100) {
        v4u ml; v2u gm[4];
        const int go = mod_off(b, oc);
#pragma nounroll
        for (u32 spin = 0; spin < SPIN_CAP; ++spin) {
          CFENCE();
          ml = __builtin_amdgcn_raw_buffer_load_b128(rs, mlbase + (lane & 3) * 16, 0, SC1);
          u32 bad = (ml.y ^ tag) | (ml.w ^ tag);
#pragma unroll
          for (int i = 0; i < 4; ++i) {
            gm[i] = __builtin_amdgcn_raw_buffer_load_b64(rs, go + (i & 1) * 16, (i & 2) ? 5 * 8192 : 2 * 8192, SC1);
            bad |= gm[i].y ^ tag;
          }
          xo0 = ldnt<u16>(x + (b * 512 + oc) * 2); xo1 = ldnt<u16>(x + (b * 512 + oc + 4) * 2);
          if (__all(bad == 0)) break;
        }
        ga0 = half_of(gm[0].x, oc & 1); ga1 = half_of(gm[1].x, oc & 1); gb0 = half_of(gm[2].x, oc & 1); gb1 = half_of(gm[3].x, oc & 1);
        float M = uf(ml.x); M = fmaxf(M, dppf<XOR1>(M)); M = fmaxf(M, dppf<XOR2>(M));
        float L = uf(ml.z) * __builtin_amdgcn_exp2f((uf(ml.x) - M) * LOG2E);
        L += dppf<XOR1>(L); L += dppf<XOR2>(L);
        scale = __builtin_amdgcn_exp2f((mw - M) * LOG2E) / L;
        asm volatile("" : "+v"(xo0), "+v"(xo1));
      }
      const v4u p4 = *lp<v4u>(sc + j * 64 + g * 16);
      v4u bf[4];
#pragma unroll
      for (int i = 0; i < 4; ++i) bf[i] = v4u{p4[i] & msk[0], p4[i] & msk[1], p4[i] & msk[2], p4[i] & msk[3]};
      asm volatile("" : "+v"(bf[0]), "+v"(bf[1]), "+v"(bf[2]), "+v"(bf[3]) :: "memory");
#pragma nounroll
      for (u32 spin = 0; landed <= u && spin < SPIN_CAP * 8; ++spin) landed = (int)lds_poll(C_LANDED);
      CFENCE();
      const u32 ad = ((u & 31) << 12) + lane * 16;
      v4u v0 = *lp<v4u>(ad), v1 = *lp<v4u>(ad + 1024), v2 = *lp<v4u>(ad + 2048), v3 = *lp<v4u>(ad + 3072);
      SBAR();
      acc = mfma32(v0, bf[0], acc); acc = mfma32(v1, bf[1], acc); acc = mfma32(v2, bf[2], acc);
      asm volatile("" : "+v"(v3) :: "memory");
      *(volatile LAS u32*)(C_CONS + 4 * a) = 128 + j + 1;
      acc = mfma32(v3, bf[3], acc);
    }
    {
      const float o0 = acc[0] * scale, o1 = acc[1] * scale, o2 = acc[2] * scale, o3 = acc[3] * scale;
      const u32 h0 = pk(o0, o1), h1 = pk(o2, o3);
      const u32 l0 = pk(o0 - blo(h0), o1 - bhi(h0)), l1 = pk(o2 - blo(h1), o3 - bhi(h1));
      const bool lo = n >= 8;
      *lp<v2u>(L_OA + (2 * a + (n >> 3)) * 320 + (g * 8 + (n & 7)) * 8) = v2u{lo ? l0 : h0, lo ? l1 : h1};
    }
    asm volatile("s_waitcnt lgkmcnt(0)" ::: "memory");
    __builtin_amdgcn_s_barrier();
    if (a == 1) return;
    STAMP(4);
    v4u m[4];
    const int io = (b * 64 + pid) * 4096 + lane * 16;
    delay(D1);
#pragma nounroll
    for (u32 spin = 0; spin < SPIN_CAP; ++spin) {
      CFENCE();
      u32 bad = 0;
#pragma unroll
      for (int i = 0; i < 4; ++i) m[i] = __builtin_amdgcn_raw_buffer_load_b128(rs, io + i * 1024, W_IN1, SC1);
#pragma unroll
      for (int i = 0; i < 4; ++i) bad |= (m[i].y ^ tag) | (m[i].w ^ tag);
      if (__all(bad == 0)) break;
    }
    STAMP(15);
    u32 yb = inbox_sum(m);
    u32 t = pk(ga0 * blo(yb), ga1 * bhi(yb));
    const u32 x1 = pk(blo(xo0) + blo(t), blo(xo1) + bhi(t));
    if (lane < 32) __builtin_amdgcn_raw_buffer_store_b64(v2u{x1, tag}, rs, W_X1 + (b * 8 + (lane >> 2)) * 2048 + pid * 32 + (lane & 3) * 8, 0, SC1);
    __builtin_amdgcn_s_barrier();
    STAMP(16);
    __builtin_amdgcn_s_barrier();
    delay(D3);
#pragma nounroll
    for (u32 spin = 0; spin < SPIN_CAP; ++spin) {
      CFENCE();
      u32 bad = 0;
#pragma unroll
      for (int i = 0; i < 4; ++i) m[i] = __builtin_amdgcn_raw_buffer_load_b128(rs, io + (i == 3 && lane >= 32 ? 0 : i * 1024), W_IN3, SC1);
#pragma unroll
      for (int i = 0; i < 4; ++i) bad |= (m[i].y ^ tag) | (m[i].w ^ tag);
      if (__all(bad == 0)) break;
    }
    STAMP(19);
    if (lane >= 32) m[3] = v4u{0u, 0u, 0u, 0u};
    yb = inbox_sum(m);
    t = pk(gb0 * blo(yb), gb1 * bhi(yb));
    const u32 r = pk(blo(x1) + blo(t), bhi(x1) + bhi(t));
    if (lane < 8) *(GAS u16*)(out + (b * 512 + oc + (lane & 4)) * 2) = (u16)(lane < 4 ? r : r >> 16);
    STAMP(20);
    if (lane < 32) *(GAS u64*)(ws + W_DBG + (blockIdx.x * 32 + lane) * 8) = *(volatile LAS u64*)(L_STAMP + lane * 8);
    return;
  }

  if (wave == 3) {
    u32 tref = (u32)__builtin_amdgcn_s_memrealtime() + T00;
    const u32 tag = (u32)__builtin_amdgcn_readfirstlane((int)__builtin_amdgcn_raw_buffer_load_b32(rs, W_EPOCH, 0, SC1)) + 1u;
    *(volatile LAS v2u*)C_TAG = v2u{tag, 1u};
    asm volatile("s_mov_b64 exec, 0\n s_branch .Lldr_copies\n .Lwarm_ret0:\n s_mov_b64 exec, -1" ::: "memory");
    int uref = 0, tau = TAU * 64 / 100, kp = 1, kr = 1;
    u32 lsum = 0, lcnt = 0, st = 0;
#pragma nounroll
    for (u32 spin = 0; spin < SPIN_CAP * 8; ++spin) {
      const v3u cc = *(volatile LAS v3u*)C_CONS;
      const int u = (int)lds_poll(C_LANDED);
      const u32 now = (u32)__builtin_amdgcn_s_memrealtime();
      const u32 c0 = __builtin_amdgcn_readfirstlane(cc.x), c1 = __builtin_amdgcn_readfirstlane(cc.y), pl = __builtin_amdgcn_readfirstlane(cc.z) ^ (0x88888888u >> (32 - 4 * NPRE));
      const u32 done = pl ? (__builtin_ctz(pl) & 28) * 2 : max(8u * NPRE, 2 * min(c0, c1));
      const int lag = (int)(now - tref) - (((u - uref) * tau) >> 6);
      const int lvl = min(max(LVL0 + ((lag * GAIN + 128) >> 8), (CLOSED && uref) ? 0 : LVL0), 7);
      *(volatile LAS u32*)C_CTL = ((done + 32) << 12) | (lvl << 7);
      lsum += lvl; ++lcnt;
      if (u >= 32 && st == 0) { STAMP(25); st = 1; }
      if (u >= 80 && st == 1) { STAMP(23); st = 2; }
      if (u >= 128 && st == 2) { STAMP(26); st = 3; }
      if (u >= 256 && st == 3) { STAMP(28); st = 4; }
      if (u >= 384 && st == 4) { STAMP(27); st = 5; }
      if (u >= 496) { *(volatile LAS u64*)(L_STAMP + 21 * 8) = t_start + lsum * 100 / lcnt; *(volatile LAS u64*)(L_STAMP + 22 * 8) = t_start + lvl * 100; }
      if (u >= 496 && done >= 480) break;
      if (CLOSED) {
        if (u >= 64 * kp) {
          if ((idx & 3) == 0 && lane == 0) __builtin_amdgcn_raw_buffer_store_b64(v2u{now, tag}, rs, W_PROG + kp * 512 + ((idx >> 2) * 8 + xcd) * 8, 0, SC1);
          ++kp;
        }
        if (u >= 64 * kr + RDLAG) {
          const v2u pr = __builtin_amdgcn_raw_buffer_load_b64(rs, W_PROG + kr * 512 + lane * 8, 0, SC1);
          const float ago = wave_sum(pr.y == tag ? (float)(int)(now - pr.x) : 0.f);
          const u32 tk = now - (u32)__builtin_amdgcn_readfirstlane((int)(ago * (1.f / 64.f)));
          if (uref) tau = min(max((int)(tk - tref), 800), 1000);
          tref = tk; uref = 64 * kr; ++kr;
        }
      }
      __builtin_amdgcn_s_sleep(3);
    }
    return;
  }

  const int w = wave - 4;
  if (first_mod) mod_tile<false>(cnd, wmod, rs, b * 8 + xcd, w, lane);
  {
    v4u kr[NPRE][4];
    const u32 ad = w * 4096 + n * 256 + ((g ^ n) << 4);
#pragma unroll
    for (int r = 0; r < NPRE; ++r) {
#pragma nounroll
      for (u32 spin = 0; (int)lds_poll(C_LANDED) <= 8 * r + w && spin < SPIN_CAP * 8; ++spin) __builtin_amdgcn_s_sleep(8);
      CFENCE();
#pragma unroll
      for (int s = 0; s < 4; ++s) kr[r][s] = *lp<v4u>((ad ^ (s * 64)) + (r & 3) * 32768);
      asm volatile("" : "+v"(kr[r][0]), "+v"(kr[r][1]), "+v"(kr[r][2]), "+v"(kr[r][3]) :: "memory");
      lds_add(C_PULL, 1u << (4 * r));
    }
#pragma nounroll
    for (u32 spin = 0; lds_poll(C_QGOT) == 0u && spin < SPIN_CAP * 8; ++spin) __builtin_amdgcn_s_sleep(8);
    CFENCE();
    v4u qa[4];
#pragma unroll
    for (int s = 0; s < 4; ++s) qa[s] = *lp<v4u>(L_QV + 64 * s + 16 * g);
#pragma unroll
    for (int r = 0; r < NPRE; ++r) {
      f4 d = mfma32(qa[0], kr[r][0], zero4);
      d = mfma32(qa[1], kr[r][1], d); d = mfma32(qa[2], kr[r][2], d); d = mfma32(qa[3], kr[r][3], d);
      *lp<float>(L_SC + (w & 1) * 8192 + (4 * r + (w >> 1)) * 64 + ((n & 3) * 4 + (n >> 2)) * 4) = d[0];
    }
    lds_add(C_PRE, 1u);
  }
  const u32 tag = get_tag();
  const int fs = 7 * xcd + (pid & 7);
  v4u fo[4][4], fg[2][4], fd[4];
  {
    const __amdgpu_buffer_rsrc_t ro = __builtin_amdgcn_make_buffer_rsrc((void*)(wo + (size_t)h * 131072), 0, (NOW & 1) ? 0 : 131072, 0x00020000);
    const int vo = 2 * g * 1024 + (64 * w + 4 * n) * 2;
#pragma unroll
    for (int s = 0; s < 4; ++s) {
      v2u r[8];
#pragma unroll
      for (int j = 0; j < 8; ++j) { r[j] = __builtin_amdgcn_raw_buffer_load_b64(ro, vo + (j >> 2) * 1024, (32 * s + 8 * (j & 3)) * 1024, NT); PACE(j); }
      tr8x4(r, fo[s]);
      PIN4(fo[s]);
      SBAR();
    }
  }
  if (has_ffn) {
    const char* pg = (n < 8 ? wg : wu) + (fs * 32 + 4 * (n & 7)) * 2;
#pragma unroll
    for (int s = 0; s < 2; ++s) {
      v2u r[8];
      const char* pr = pg + (size_t)slice_col(16 * g + 2 * w + s, 0) * (FF * 2);
#pragma unroll
      for (int j = 0; j < 8; ++j) { r[j] = (NOW & 2) ? v2u{0u, 0u} : ldnt<v2u>(pr + j * (4 * FF * 2)); PACE(j); }
      tr8x4(r, fg[s]);
      PIN4(fg[s]);
      SBAR();
    }
    v2u r[8];
    const __amdgpu_buffer_rsrc_t rd = __builtin_amdgcn_make_buffer_rsrc((void*)(wd + (size_t)fs * 32768), 0, (NOW & 2) ? 0 : 32768, 0x00020000);
    const int vd = 8 * g * 1024 + (64 * w + 4 * n) * 2;
#pragma unroll
    for (int j = 0; j < 8; ++j) { r[j] = __builtin_amdgcn_raw_buffer_load_b64(rd, vd + (j & 3) * 1024, (j >> 2) * 4096, NT); PACE(j); }
    tr8x4(r, fd);
    PIN4(fd);
    SBAR();
  } else {
    mod_tile<true>(cnd, wmod, rs, 16 + b * 8 + xcd, w, lane);
#pragma unroll
    for (int t = 0; t < 4; ++t) fg[0][t] = fg[1][t] = fd[t] = v4u{0u, 0u, 0u, 0u};
    nap<12000>();
  }
  {
    v2u sv, cv;
    const int so = mod_off(b, 64 * w + lane);
#pragma nounroll
    for (u32 spin = 0; spin < SPIN_CAP; ++spin) {
      CFENCE();
      sv = __builtin_amdgcn_raw_buffer_load_b64(rs, so, 3 * 8192, SC1);
      cv = __builtin_amdgcn_raw_buffer_load_b64(rs, so, 4 * 8192, SC1);
      if (__all(((sv.y ^ tag) | (cv.y ^ tag)) == 0)) break;
      __builtin_amdgcn_s_sleep(64);
    }
    *lp<u32>(L_SBCB + ((8 * w + 2 * (lane & 3) + (lane >> 5)) * 8 + ((lane >> 2) & 7)) * 4) = pk(half_of(sv.x, lane & 1), 1.f + half_of(cv.x, lane & 1));
  }
  if (blockIdx.x == 0 && w == 7) {
#pragma nounroll
    for (u32 spin = 0; spin < SPIN_CAP; ++spin) {
      CFENCE();
      u32 bad = 0;
#pragma nounroll
      for (int i = 0; i < 8; ++i) {
        const v4u t = __builtin_amdgcn_raw_buffer_load_b128(rs, W_ML + lane * 16, i * 1024, SC1);
        bad |= (t.y ^ tag) | (t.w ^ tag);
      }
      if (__all(bad == 0)) break;
      __builtin_amdgcn_s_sleep(64);
    }
    if (lane == 0) __builtin_amdgcn_raw_buffer_store_b32(tag, rs, W_EPOCH, 0, SC1);
  }
  if (w == 0) STAMP(13);
  const int mo = (b * 64 + 8 * w + (lane >> 3)) * 4096 + (lane & 7) * 8;
  const int gs = 16 * g + ((n + 2 * w) & 15);
  asm volatile("s_waitcnt vmcnt(0) lgkmcnt(0)" ::: "memory");
  __builtin_amdgcn_s_barrier();
  CFENCE();
  {
    v4u oa[4];
#pragma unroll
    for (int s = 0; s < 4; ++s) oa[s] = *lp<v4u>(L_OA + (n & 3) * 320 + s * 64 + g * 16);
    SBAR();
    f4 d[4] = {zero4, zero4, zero4, zero4};
#pragma unroll
    for (int s = 0; s < 4; ++s)
#pragma unroll
      for (int t = 0; t < 4; ++t) d[t] = mfma32(oa[s], fo[s][t], d[t]);
    float y[4];
#pragma unroll
    for (int t = 0; t < 4; ++t) y[t] = (d[t][0] + d[t][2]) + (d[t][1] + d[t][3]);
    const float y01 = (g & 1) ? y[1] : y[0], y23 = (g & 1) ? y[3] : y[2];
    __builtin_amdgcn_raw_buffer_store_b64(v2u{fu((g & 2) ? y23 : y01), tag}, rs, mo, W_IN1 + pid * 64, SC1);
  }
  if (w == 0) STAMP(14);
  float sb[8], cb[8];
  {
    const v4u s0 = *lp<v4u>(L_SBCB + gs * 32), s1 = *lp<v4u>(L_SBCB + gs * 32 + 16);
#pragma unroll
    for (int e = 0; e < 4; ++e) { sb[e] = blo(s0[e]); cb[e] = bhi(s0[e]); sb[4 + e] = blo(s1[e]); cb[4 + e] = bhi(s1[e]); }
    PIN4(sb); PIN4(sb + 4); PIN4(cb); PIN4(cb + 4);
  }
  __builtin_amdgcn_s_barrier();
  delay(D2);
  v4u m0, m1;
#pragma nounroll
  for (u32 spin = 0; spin < SPIN_CAP; ++spin) {
    CFENCE();
    m0 = __builtin_amdgcn_raw_buffer_load_b128(rs, gs * 32, W_X1 + (b * 8 + xcd) * 2048, SC1);
    m1 = __builtin_amdgcn_raw_buffer_load_b128(rs, gs * 32 + 16, W_X1 + (b * 8 + xcd) * 2048, SC1);
    if (__all(((m0.y ^ tag) | (m0.w ^ tag) | (m1.y ^ tag) | (m1.w ^ tag)) == 0)) break;
  }
  if (w == 0) STAMP(17);
  {
    const float v[8] = {blo(m0.x), bhi(m0.x), blo(m0.z), bhi(m0.z), blo(m1.x), bhi(m1.x), blo(m1.z), bhi(m1.z)};
    const float sh = uf((u32)__builtin_amdgcn_readlane((int)fu(v[0]), (16 - 2 * w) & 15));
    float d[8];
#pragma unroll
    for (int e = 0; e < 8; ++e) d[e] = v[e] - sh;
    float s1 = ((d[0] + d[1]) + (d[2] + d[3])) + ((d[4] + d[5]) + (d[6] + d[7]));
    float s2 = (fmaf(d[0], d[0], d[1] * d[1]) + fmaf(d[2], d[2], d[3] * d[3])) + (fmaf(d[4], d[4], d[5] * d[5]) + fmaf(d[6], d[6], d[7] * d[7]));
    s1 = wave_sum(s1) * (1.f / 512.f); s2 = wave_sum(s2) * (1.f / 512.f);
    const float mb = rbf(sh + s1), var = fmaxf(s2 - s1 * s1, 0.f);
    const float rsd = __builtin_amdgcn_rcpf(rbf(__builtin_amdgcn_sqrtf(rbf(rbf(var) + 1e-5f))));
    v4u ha;
#pragma unroll
    for (int e = 0; e < 4; ++e) ha[e] = pk(fmaf((v[2 * e] - mb) * rsd, cb[2 * e], sb[2 * e]), fmaf((v[2 * e + 1] - mb) * rsd, cb[2 * e + 1], sb[2 * e + 1]));
    f4 gp;
#pragma unroll
    for (int t = 0; t < 4; ++t) gp[t] = mfma32(ha, fg[0][t], zero4)[0] + mfma32(ha, fg[1][t], zero4)[1];
    if (g == 0) *lp<f4>(L_GURED + w * 256 + n * 16) = gp;
  }
  asm volatile("s_waitcnt lgkmcnt(0)" ::: "memory");
  __builtin_amdgcn_s_barrier();
  CFENCE();
  {
    float pt[8];
#pragma unroll
    for (int ww = 0; ww < 8; ++ww) pt[ww] = *lp<float>(L_GURED + ww * 256 + ((n >> 3) * 32 + 8 * g + (n & 7)) * 4);
    const float s = rbf(((pt[0] + pt[1]) + (pt[2] + pt[3])) + ((pt[4] + pt[5]) + (pt[6] + pt[7])));
    const float up = dppf<ROR8>(s);
    const float z = s * fmaf(s * s, 0.044715f * 0.7978845608028654f * 2.f * LOG2E, 0.7978845608028654f * 2.f * LOG2E);
    const float ge = rbf(s * __builtin_amdgcn_rcpf(1.f + __builtin_amdgcn_exp2f(-z)));
    const float act = ge * up;
    const u32 ap = pk(act, dppf<XOR1>(act));
    const v4u aa = {dppu<0x150>(ap), dppu<0x152>(ap), dppu<0x154>(ap), dppu<0x156>(ap)};
    float y[4];
#pragma unroll
    for (int t = 0; t < 4; ++t) y[t] = mfma32(aa, fd[t], zero4)[0];
    const float y01 = (g & 1) ? y[1] : y[0], y23 = (g & 1) ? y[3] : y[2];
    if (has_ffn) __builtin_amdgcn_raw_buffer_store_b64(v2u{fu((g & 2) ? y23 : y01), tag}, rs, mo, W_IN3 + fs * 64, SC1);
  }
  if (w == 0) STAMP(18);
}

}

namespace diag {
typedef unsigned u32; typedef unsigned long long u64;
__global__ void k_diag(const u64* h, int stat, int st) {
  if (threadIdx.x != 0) return;
  u64 t0 = ~0ull; for (int w = 0; w < 256; ++w) t0 = h[w * 32] < t0 ? h[w * 32] : t0;
  u64 mn = ~0ull, mx = 0, sum = 0; int n = 0;
  for (int w = 0; w < 256; ++w) { u64 v = h[w * 32 + st]; if (v < t0 || v - t0 > 100000) continue; v -= t0; mn = v < mn ? v : mn; mx = v > mx ? v : mx; sum += v; ++n; }
  u64 val = stat == 0 ? mn : stat == 1 ? (n ? sum / n : 0) : stat == 2 ? mx : 0;
  if (stat == 3) { val = h[st] * 100; for (int w = 0; w < 256; ++w) if (h[w * 32 + st] != ((h[st] + w) & 7)) val += 1000; if (val > 50000) val = 50000; }
  const u64 end = t0 + 30000 + val * 40;
  while (__builtin_amdgcn_s_memrealtime() < end) __builtin_amdgcn_s_sleep(4);
}
}
extern "C" void kernel_launch(void* const* d_in, const int* in_sizes, int n_in, void* d_out, int out_size, void* d_ws, size_t ws_size, hipStream_t stream) {
  hipLaunchKernelGGL(blk::fused_block, dim3(256), dim3(blk::NTHR), 0, stream,
                     (const char*)d_in[0], (const char*)d_in[1], (const char*)d_in[2], (const char*)d_in[3], (const char*)d_in[4], (const char*)d_in[6],
                     (const char*)d_in[7], (const char*)d_in[8], (const char*)d_in[9], (const char*)d_in[10], (char*)d_out, (char*)d_ws);
  diag::k_diag<<<1, 64, 0, stream>>>((const diag::u64*)((char*)d_ws + blk::W_DBG), 3, 24);
}
```

```cpp
#include <hip/hip_runtime.h>
#pragma clang diagnostic ignored "-Wint-to-pointer-cast"

#define WLEVELS "17,18,19,20,21,23,25,28"
#define LVL0 2
#define U0 5
#define TAU 1400
#define T00 250
#define GAIN 8
#define CLOSED 1
#define RDLAG 24
#define NPRE 8
#define CSLEEP 64
#define HSLEEP 16
#define QSLEEP 16
#define HDELAY 0
#define QDELAY 2000
#define WQFLY 3
#define PACEN 1
#define NOW 0
#define D1 1200
#define D2 500
#define D3 1100
#define STAMPS 0

namespace blk {

typedef unsigned short u16;
typedef unsigned u32;
typedef unsigned long long u64;
typedef u32 v4u __attribute__((ext_vector_type(4)));
typedef u32 v3u __attribute__((ext_vector_type(3)));
typedef u32 v2u __attribute__((ext_vector_type(2)));
typedef float f4 __attribute__((ext_vector_type(4)));
typedef float f2 __attribute__((ext_vector_type(2)));
typedef __bf16 bf8 __attribute__((ext_vector_type(8)));
typedef __bf16 bf2 __attribute__((ext_vector_type(2)));
typedef short s4 __attribute__((ext_vector_type(4)));
#define GAS __attribute__((address_space(1)))
#define LAS __attribute__((address_space(3)))

constexpr int NTHR = 768;
constexpr int FF = 1792;
constexpr int L_SC = 131072;
constexpr int L_MODRED = L_SC + 16384;
constexpr int L_GURED = L_MODRED + 8192;
constexpr int L_SBCB = L_GURED + 2048;
constexpr int L_OA = L_SBCB + 2048;
constexpr int L_QV = L_OA + 1280;
constexpr int L_CTRL = L_QV + 256;
constexpr int C_CONS = L_CTRL, C_PULL = L_CTRL + 8, C_MODCNT = L_CTRL + 12, C_PRE = L_CTRL + 16, C_QGOT = L_CTRL + 20, C_TAG = L_CTRL + 24;
constexpr int C_CTL = L_CTRL + 32;
constexpr int C_LANDED = L_CTRL + 48;
constexpr int L_STAMP = C_LANDED + 256;
constexpr int LDS_BYTES = L_STAMP + (STAMPS ? 256 : 0);
constexpr int W_EPOCH = 0, W_PROG = 4096, W_ML = 16384;
constexpr int W_MOD = 65536;
constexpr int W_Q = 131072;
constexpr int W_X1 = 196608;
constexpr int W_IN1 = 262144, W_IN3 = W_IN1 + (1 << 20);
constexpr int W_END = W_IN3 + (1 << 20), W_DBG = W_END;
constexpr int SC1 = 16, NT = 2;
constexpr u32 SPIN_CAP = 1u << 18;

__device__ __forceinline__ float blo(u32 u) { return __builtin_bit_cast(float, u << 16); }
__device__ __forceinline__ float bhi(u32 u) { return __builtin_bit_cast(float, u & 0xffff0000u); }
__device__ __forceinline__ u32 pk(float a, float b) { f2 p = {a, b}; return __builtin_bit_cast(u32, __builtin_convertvector(p, bf2)); }
__device__ __forceinline__ float rbf(float a) { return blo(pk(a, a)); }
__device__ __forceinline__ u32 fu(float a) { return __builtin_bit_cast(u32, a); }
__device__ __forceinline__ float uf(u32 a) { return __builtin_bit_cast(float, a); }
__device__ __forceinline__ f4 mfma32(v4u a, v4u b, f4 c) { return __builtin_amdgcn_mfma_f32_16x16x32_bf16(__builtin_bit_cast(bf8, a), __builtin_bit_cast(bf8, b), c, 0, 0, 0); }
__device__ __forceinline__ f4 mfma16(v2u a, v2u b, f4 c) { return __builtin_amdgcn_mfma_f32_16x16x16bf16_1k(__builtin_bit_cast(s4, a), __builtin_bit_cast(s4, b), c, 0, 0, 0); }
template <int CTRL> __device__ __forceinline__ u32 dppu(u32 x) { return (u32)__builtin_amdgcn_mov_dpp((int)x, CTRL, 0xf, 0xf, true); }
template <int CTRL> __device__ __forceinline__ float dppf(float x) { return uf(dppu<CTRL>(fu(x))); }
constexpr int XOR1 = 0xB1, XOR2 = 0x4E, HMIR = 0x141, ROR4 = 0x124, ROR8 = 0x128;
__device__ __forceinline__ float xrow_sum(float x) {
  auto s = __builtin_amdgcn_permlane16_swap(fu(x), fu(x), false, false);
  x = uf(s[0]) + uf(s[1]);
  auto t = __builtin_amdgcn_permlane32_swap(fu(x), fu(x), false, false);
  return uf(t[0]) + uf(t[1]);
}
__device__ __forceinline__ float xrow_max(float x) {
  auto s = __builtin_amdgcn_permlane16_swap(fu(x), fu(x), false, false);
  x = fmaxf(uf(s[0]), uf(s[1]));
  auto t = __builtin_amdgcn_permlane32_swap(fu(x), fu(x), false, false);
  return fmaxf(uf(t[0]), uf(t[1]));
}
__device__ __forceinline__ float wave_sum(float x) { x += dppf<XOR1>(x); x += dppf<XOR2>(x); x += dppf<HMIR>(x); x += dppf<ROR8>(x); return xrow_sum(x); }
__device__ __forceinline__ float wave_max(float x) { x = fmaxf(x, dppf<XOR1>(x)); x = fmaxf(x, dppf<XOR2>(x)); x = fmaxf(x, dppf<HMIR>(x)); x = fmaxf(x, dppf<ROR8>(x)); return xrow_max(x); }
__device__ __forceinline__ u32 perm_lo(u32 r0, u32 r1) { return __builtin_amdgcn_perm(r1, r0, 0x05040100u); }
__device__ __forceinline__ u32 perm_hi(u32 r0, u32 r1) { return __builtin_amdgcn_perm(r1, r0, 0x07060302u); }
#define CFENCE() asm volatile("" ::: "memory")
#define SBAR() __builtin_amdgcn_sched_barrier(0)
#define PIN4(a) asm volatile("" : "+v"((a)[0]), "+v"((a)[1]), "+v"((a)[2]), "+v"((a)[3]))
template <typename T> __device__ __forceinline__ LAS T* lp(u32 off) { return (LAS T*)off; }
__device__ __forceinline__ u32 lds_poll(u32 off) { return (u32)__builtin_amdgcn_readfirstlane((int)*(volatile LAS u32*)off); }
__device__ __forceinline__ void lds_add(u32 off, u32 v) {
  asm volatile("s_mov_b64 exec, 1\n ds_add_u32 %0, %1\n s_mov_b64 exec, -1" :: "v"(off), "v"(v) : "memory");
}
template <typename T> __device__ __forceinline__ T ldnt(const void* p) { return __builtin_nontemporal_load((const GAS T*)p); }
template <typename T> __device__ __forceinline__ T ldg(const void* p) { return *(const GAS T*)p; }
__device__ __forceinline__ void tr8x4(const v2u (&r)[8], v4u (&f)[4]) {
#pragma unroll
  for (int q = 0; q < 4; ++q) {
    f[0][q] = perm_lo(r[2 * q].x, r[2 * q + 1].x); f[1][q] = perm_hi(r[2 * q].x, r[2 * q + 1].x);
    f[2][q] = perm_lo(r[2 * q].y, r[2 * q + 1].y); f[3][q] = perm_hi(r[2 * q].y, r[2 * q + 1].y);
  }
}
__device__ __forceinline__ int slice_col(int s, int pos) { return 64 * (s >> 3) + 32 * (s & 1) + 4 * pos + ((s >> 1) & 3); }

#define STAMP(i) do { } while (0)

__device__ __forceinline__ int mod_off(int b, int c) { return W_MOD + (c >> 6) * 1024 + b * 256 + ((c & 63) >> 1) * 8; }
__device__ __forceinline__ float half_of(u32 v, int odd) { return odd ? bhi(v) : blo(v); }
__device__ __forceinline__ void delay(int cyc) {
  const u64 t0 = __builtin_amdgcn_s_memtime();
#pragma nounroll
  while ((int)(__builtin_amdgcn_s_memtime() - t0) < cyc) { }
}
template <int CYC> __device__ __forceinline__ void nap() {
#pragma unroll
  for (int i = 0; i < CYC / 8128; ++i) __builtin_amdgcn_s_sleep(127);
  if (CYC % 8128 >= 64) __builtin_amdgcn_s_sleep((CYC % 8128) / 64);
}
__device__ __forceinline__ u32 get_tag() {
  v2u t;
#pragma nounroll
  for (u32 spin = 0; spin < SPIN_CAP * 8; ++spin) { t = *(volatile LAS v2u*)C_TAG; if (__builtin_amdgcn_readfirstlane((int)t.y)) break; }
  return (u32)__builtin_amdgcn_readfirstlane((int)t.x);
}
__device__ __forceinline__ u32 inbox_sum(const v4u (&m)[4]) {
  float y0 = (uf(m[0].x) + uf(m[1].x)) + (uf(m[2].x) + uf(m[3].x)), y1 = (uf(m[0].z) + uf(m[1].z)) + (uf(m[2].z) + uf(m[3].z));
  y0 += dppf<ROR4>(y0); y1 += dppf<ROR4>(y1); y0 += dppf<ROR8>(y0); y1 += dppf<ROR8>(y1);
  return pk(xrow_sum(y0), xrow_sum(y1));
}
#define PACE(j) do { if (((j) % PACEN) == PACEN - 1) { asm volatile("s_waitcnt vmcnt(0)" ::: "memory"); SBAR(); } } while (0)
constexpr float LOG2E = 1.4426950408889634f;

template <bool PACED>
__device__ __forceinline__ void mod_tile(const char* cnd, const char* wmod, __amdgpu_buffer_rsrc_t rs, int cb, int w, int lane) {
  const int g = lane >> 4, n = lane & 15;
  const f4 zero4 = {0.f, 0.f, 0.f, 0.f};
  const v2u cv = ldnt<v2u>(cnd + ((n >> 2) * 512 + 64 * w + 16 * g + 4 * (n & 3)) * 2);
  const __amdgpu_buffer_rsrc_t rm = __builtin_amdgcn_make_buffer_rsrc((void*)(wmod + (size_t)(64 * w) * 6144 + cb * 128), 0, 64 * 6144, 0x00020000);
  v2u r[2][8];
#pragma unroll
  for (int j = 0; j < 16; ++j) { r[j >> 3][j & 7] = __builtin_amdgcn_raw_buffer_load_b64(rm, 16 * g * 6144 + n * 8, j * 6144, NT); if (PACED) PACE(j); }
  SBAR();
  u32 sp[2];
#pragma unroll
  for (int e = 0; e < 2; ++e) {
    const float c0 = blo(cv[e]), c1 = bhi(cv[e]);
    sp[e] = pk(c0 * __builtin_amdgcn_rcpf(1.f + __builtin_amdgcn_exp2f(-c0 * LOG2E)), c1 * __builtin_amdgcn_rcpf(1.f + __builtin_amdgcn_exp2f(-c1 * LOG2E)));
  }
  const v4u a0 = {dppu<0x00>(sp[0]), dppu<0x00>(sp[1]), dppu<0x55>(sp[0]), dppu<0x55>(sp[1])};
  const v4u a1 = {dppu<0xAA>(sp[0]), dppu<0xAA>(sp[1]), dppu<0xFF>(sp[0]), dppu<0xFF>(sp[1])};
  v4u f0[4], f1[4];
  tr8x4(r[0], f0); tr8x4(r[1], f1);
  f4 pt;
#pragma unroll
  for (int t = 0; t < 4; ++t) pt[t] = mfma32(a1, f1[t], mfma32(a0, f0[t], zero4))[0];
  *lp<f4>(L_MODRED + w * 1024 + lane * 16) = pt;
  u32 old;
  asm volatile("s_mov_b64 exec, 1\n ds_add_rtn_u32 %0, %1, %2\n s_mov_b64 exec, -1\n s_waitcnt lgkmcnt(0)" : "=&v"(old) : "v"(C_MODCNT), "v"(1u) : "memory");
  if ((__builtin_amdgcn_readfirstlane((int)old) & 7) == 7) {
    f4 p[8];
#pragma unroll
    for (int ww = 0; ww < 8; ++ww) p[ww] = *lp<f4>(L_MODRED + ww * 1024 + lane * 16);
    const f4 v = ((p[0] + p[1]) + (p[2] + p[3])) + ((p[4] + p[5]) + (p[6] + p[7]));
    const u32 tag = get_tag();
    __builtin_amdgcn_raw_buffer_store_b128(v4u{pk(v[0], v[1]), tag, pk(v[2], v[3]), tag}, rs, lane * 16 + W_MOD + cb * 1024, 0, SC1);
    if (!PACED) STAMP(9);
  }
}

__global__ void __launch_bounds__(NTHR) fused_block(const char* x, const char* kc, const char* vc, const char* wq, const char* wo, const char* wg,
                                                    const char* wu, const char* wd, const char* cnd, const char* wmod, char* out, char* ws) {
  __shared__ __attribute__((aligned(1024))) char lds_mem[LDS_BYTES];
  const int lane = threadIdx.x & 63, wave = __builtin_amdgcn_readfirstlane(threadIdx.x >> 6);
  const int xcd = blockIdx.x & 7, idx = blockIdx.x >> 3;
  const int half = idx & 1, hh = (idx >> 1) & 3, b = idx >> 3;
  const int h = xcd + 8 * hh;
  const int pid = 8 * xcd + (idx & 7);
  const bool has_ffn = (idx & 7) < 7;
  const bool first_mod = !has_ffn && b < 2;
  const int g = lane >> 4, n = lane & 15;
  const f4 zero4 = {0.f, 0.f, 0.f, 0.f};
  const u64 t_fix = __builtin_amdgcn_s_memrealtime();
  if (threadIdx.x < (STAMPS ? 140 : 16)) *lp<u32>(L_CTRL + 4 * threadIdx.x) = threadIdx.x == 8 ? (32u << 12) | (LVL0 * 128) : 0u;
  asm volatile("s_waitcnt lgkmcnt(0)" :: "v"(lds_mem), "s"(kc), "s"(ws) : "memory");
  __builtin_amdgcn_s_barrier();

  if (wave == 0) {
    __builtin_amdgcn_s_setprio(3);
    constexpr u32 HOLES = 0xA000u;
    const size_t reg_off = ((size_t)(b * 32 + h) << 21) + half * 32768 - HOLES;
    u32 o0 = g * 256 + ((n ^ g) << 4), o1 = o0 ^ 64, o2 = (o0 ^ 128) + 6144, o3 = (o0 ^ 192) + 6144;
    const u32 va = C_LANDED + lane * 4;
    const u64 kb = (u64)(kc + reg_off), vb = (u64)(vc + reg_off);
    v4u rsrc = {(u32)kb, (u32)(kb >> 32) & 0xffffu, (2u << 20) + 0x2000u, 0x00020000u};
    STAMP(5);
    if (first_mod) __builtin_amdgcn_s_sleep(CSLEEP);
    int pub = -7;
    u32 ctl = (32u << 12) | (LVL0 * 128);
#pragma nounroll
    for (u32 end = 1u << 20; end <= (2u << 20); end += 1u << 20) {
      u32 off = end - (1u << 20), src = HOLES, spin = SPIN_CAP * 8, blk, tmp, pcl, pch;
#define LD(v, o) "s_waitcnt vmcnt(\\w)\n buffer_load_dwordx4 %[" #v "], %[rs], %[src] offen offset:" #o " nt lds\n"
      asm volatile(
        "s_getpc_b64 vcc\n"
        ".Lldr_pc:\n s_add_u32 %[pcl], vcc_lo, .Lldr_copies-.Lldr_pc\n s_addc_u32 %[pch], vcc_hi, 0\n"
        ".Lldr_next:\n s_waitcnt lgkmcnt(4)\n"
        ".Lldr_dec:\n v_readfirstlane_b32 %[tmp], %[ctl]\n ds_read_b32 %[ctl], %[ca]\n s_add_u32 %[blk], %[off], 0x4000\n"
        "s_cmp_gt_u32 %[blk], %[tmp]\n s_cbranch_scc1 .Lldr_stall\n"
        "s_and_b32 %[tmp], %[tmp], 0x380\n s_add_u32 vcc_lo, %[pcl], %[tmp]\n s_addc_u32 vcc_hi, %[pch], 0\n s_setpc_b64 vcc\n"
        ".Lldr_stall:\n s_waitcnt lgkmcnt(0)\n s_sub_u32 %[spin], %[spin], 1\n s_cbranch_scc0 .Lldr_dec\n s_branch .Lldr_exit\n"
        ".p2align 7\n.Lldr_copies:\n"
        ".irp w," WLEVELS "\n"
        "s_cbranch_execz 2f\n"
        "1:\n s_and_b32 m0, %[off], 0x1ffff\n" LD(v0, 0) "s_add_u32 %[off], %[off], 0x1000\n" LD(v1, 1024) LD(v2, 2048) LD(v3, 3072)
        "ds_write_b32 %[va], %[pub]\n s_add_u32 %[src], %[src], 0x800\n s_or_b32 %[src], %[src], 0xa000\n v_add_u32 %[pub], 1, %[pub]\n"
        "s_cmp_lt_u32 %[off], %[blk]\n s_cbranch_scc1 1b\n s_cmp_lt_u32 %[off], %[end]\n s_cbranch_scc1 .Lldr_next\n s_branch .Lldr_exit\n"
        ".p2align 7\n2:\n.endr\n"
        "s_branch .Lwarm_ret0\n"
        ".Lldr_exit:\n s_waitcnt lgkmcnt(0)"
        : [off] "+&s"(off), [src] "+&s"(src), [spin] "+&s"(spin), [blk] "=&s"(blk), [tmp] "=&s"(tmp), [pcl] "=&s"(pcl), [pch] "=&s"(pch), [pub] "+&v"(pub), [ctl] "+&v"(ctl)
        : [v0] "v"(o0), [v1] "v"(o1), [v2] "v"(o2), [v3] "v"(o3), [rs] "s"(rsrc), [end] "s"(end), [va] "v"(va), [ca] "v"(C_CTL) : "memory", "scc", "vcc");
      rsrc.x = (u32)vb; rsrc.y = (u32)(vb >> 32) & 0xffffu;
      o0 = o1 = lane * 16; o2 = o3 = lane * 16 + 6144;
    }
#pragma unroll
    for (int k = 6; k >= 0; --k) {
      const u32 pb = 512 - k;
      asm volatile("s_waitcnt vmcnt(%2)\n ds_write_b32 %0, %1" :: "v"(va), "v"(pb), "i"(4 * k) : "memory");
    }
    STAMP(1);
    return;
  }

  const __amdgpu_buffer_rsrc_t rs = __builtin_amdgcn_make_buffer_rsrc((void*)ws, 0, W_END, 0x00020000);

  if (wave <= 2) {
    const int a = wave - 1;
    u32 tag;
    if (a == 0) {
      const int beta = n >> 2, kq = 16 * g + 4 * (n & 3);
      v2u xr[8];
#pragma unroll
      for (int i = 0; i < 8; ++i) xr[i] = ldnt<v2u>(x + (beta * 512 + 64 * i + kq) * 2);
      SBAR();
      if (first_mod) {
#pragma nounroll
        for (u32 spin = 0; lds_poll(C_MODCNT) == 0u && spin < SPIN_CAP * 8; ++spin) __builtin_amdgcn_s_sleep(2);
      }
      {
        const char* wt = wq + (h * 128 + (2 * b + half) * 16) * 2;
        const u32 vo = (64 * (lane >> 5) + 16 * ((lane >> 3) & 3) + ((lane >> 1) & 3)) * 8192 + (lane & 1) * 16;
#pragma nounroll
        for (int p = 0; p < 16; ++p)
          asm volatile("s_mov_b32 m0, %2\n s_waitcnt vmcnt(%3)\n global_load_lds_dwordx4 %0, %1 nt"
                       :: "v"(vo), "s"(wt + (size_t)(128 * (p >> 2) + 4 * (p & 3)) * 8192), "s"(L_SC + p * 1024), "i"(WQFLY - 1) : "memory");
      }
      SBAR();
      {
        float s = 0.f;
#pragma unroll
        for (int i = 0; i < 8; ++i) s += (blo(xr[i].x) + bhi(xr[i].x)) + (blo(xr[i].y) + bhi(xr[i].y));
        s += dppf<XOR1>(s); s += dppf<XOR2>(s);
        const float mean = xrow_sum(s) * (1.f / 512.f);
        float q = 0.f;
#pragma unroll
        for (int i = 0; i < 8; ++i)
#pragma unroll
          for (int e = 0; e < 2; ++e) { const float d0 = blo(xr[i][e]) - mean, d1 = bhi(xr[i][e]) - mean; q = fmaf(d0, d0, q); q = fmaf(d1, d1, q); }
        q += dppf<XOR1>(q); q += dppf<XOR2>(q);
        const float mb = rbf(mean), rsd = __builtin_amdgcn_rcpf(rbf(__builtin_amdgcn_sqrtf(rbf(rbf(xrow_sum(q) * (1.f / 512.f)) + 1e-5f))));
#pragma unroll
        for (int i = 0; i < 8; ++i)
#pragma unroll
          for (int e = 0; e < 2; ++e) { const u32 d = pk(blo(xr[i][e]) - mb, bhi(xr[i][e]) - mb); xr[i][e] = pk(blo(d) * rsd, bhi(d) * rsd); }
        PIN4(xr); PIN4(xr + 4);
      }
      STAMP(10);
      tag = get_tag();
      nap<HDELAY>();
      u32 ha[8][2];
#pragma nounroll
      for (u32 spin = 0; spin < SPIN_CAP; ++spin) {
        CFENCE();
        bool go = spin == 0;
        if (!go) go = __all(__builtin_amdgcn_raw_buffer_load_b64(rs, W_MOD + n * 1024 + 1016, 0, SC1)[1] == tag);
        if (go) {
          u32 bad = 0;
#pragma unroll
          for (int hf = 0; hf < 2; ++hf) {
            v4u sa[4], ca[4];
#pragma unroll
            for (int i = 0; i < 4; ++i) {
              sa[i] = __builtin_amdgcn_raw_buffer_load_b128(rs, beta * 256 + kq * 4 + i * 1024, W_MOD + hf * 4096, SC1);
              ca[i] = __builtin_amdgcn_raw_buffer_load_b128(rs, beta * 256 + kq * 4 + i * 1024, W_MOD + 8192 + hf * 4096, SC1);
            }
#pragma unroll
            for (int i = 0; i < 4; ++i) {
              bad |= (sa[i].y ^ tag) | (sa[i].w ^ tag) | (ca[i].y ^ tag) | (ca[i].w ^ tag);
#pragma unroll
              for (int e = 0; e < 2; ++e) {
                const u32 c1 = pk(1.f + blo(ca[i][2 * e]), 1.f + bhi(ca[i][2 * e]));
                const u32 t = pk(blo(xr[4 * hf + i][e]) * blo(c1), bhi(xr[4 * hf + i][e]) * bhi(c1));
                ha[4 * hf + i][e] = pk(blo(t) + blo(sa[i][2 * e]), bhi(t) + bhi(sa[i][2 * e]));
              }
            }
          }
          if (__all(bad == 0)) break;
        }
        __builtin_amdgcn_s_sleep(HSLEEP);
      }
      asm volatile("s_waitcnt vmcnt(0)" ::: "memory");
      STAMP(11);
      f4 acc[4] = {zero4, zero4, zero4, zero4};
#pragma unroll
      for (int p = 0; p < 16; ++p) {
        const v2u lo = __builtin_bit_cast(v2u, __builtin_amdgcn_ds_read_tr16_b64_v4i16(lp<s4>(L_SC + p * 1024 + lane * 8)));
        const v2u hi = __builtin_bit_cast(v2u, __builtin_amdgcn_ds_read_tr16_b64_v4i16(lp<s4>(L_SC + p * 1024 + 512 + lane * 8)));
        const int sg = p >> 2;
        acc[p & 3] = mfma32(v4u{ha[2 * sg][0], ha[2 * sg][1], ha[2 * sg + 1][0], ha[2 * sg + 1][1]}, v4u{lo.x, lo.y, hi.x, hi.y}, acc[p & 3]);
      }
      const float qv = (acc[0][0] + acc[1][1]) + (acc[2][2] + acc[3][3]);
      const u32 pr = pk(qv, dppf<XOR1>(qv));
      if (!(n & 1)) __builtin_amdgcn_raw_buffer_store_b64(v2u{pr, tag}, rs, W_Q + (h * 4 + g) * 512 + (2 * b + half) * 64 + (n >> 1) * 8, 0, SC1);
      STAMP(12);
      nap<QDELAY>();
      v2u m;
#pragma nounroll
      for (u32 spin = 0; spin < SPIN_CAP; ++spin) {
        CFENCE();
        m = __builtin_amdgcn_raw_buffer_load_b64(rs, lane * 8, W_Q + (h * 4 + b) * 512, SC1);
        if (__all(m.y == tag)) break;
        __builtin_amdgcn_s_sleep(QSLEEP);
      }
      *lp<u32>(L_QV + lane * 4) = m.x;
      CFENCE();
      *(volatile LAS u32*)C_QGOT = 1u;
    } else {
#pragma nounroll
      for (u32 spin = 0; lds_poll(C_QGOT) == 0u && spin < SPIN_CAP * 8; ++spin) __builtin_amdgcn_s_sleep(4);
      tag = get_tag();
    }
    CFENCE();
    v4u qa[4];
#pragma unroll
    for (int s = 0; s < 4; ++s) qa[s] = *lp<v4u>(L_QV + 64 * s + 16 * g);
    if (a == 0) STAMP(2);
    const u32 sc = L_SC + a * 8192;
    int landed = 0;
    const u32 kofs = n * 256 + ((g ^ n) << 4);
    const u32 spos = sc + ((n & 3) * 4 + (n >> 2)) * 4;
#pragma nounroll
    for (int j = 4 * NPRE; j < 128; ++j) {
      const int u = 2 * j + a;
#pragma nounroll
      for (u32 spin = 0; landed <= u && spin < SPIN_CAP * 8; ++spin) landed = (int)lds_poll(C_LANDED);
      CFENCE();
      const u32 ad = ((u & 31) << 12) + kofs;
      v4u k0 = *lp<v4u>(ad), k1 = *lp<v4u>(ad ^ 64), k2 = *lp<v4u>(ad ^ 128), k3 = *lp<v4u>(ad ^ 192);
      asm volatile("" : "+v"(k0), "+v"(k1), "+v"(k2), "+v"(k3) :: "memory");
      *(volatile LAS u32*)(C_CONS + 4 * a) = j + 1;
      f4 d = mfma32(qa[0], k0, zero4);
      d = mfma32(qa[1], k1, d); d = mfma32(qa[2], k2, d); d = mfma32(qa[3], k3, d);
      *lp<float>(spos + j * 64) = d[0];
    }
#pragma nounroll
    for (u32 spin = 0; lds_poll(C_PRE) != 8u && spin < SPIN_CAP * 8; ++spin) { }
    CFENCE();
    if (a == 0) STAMP(3);
    float mw, lw;
    {
      f4 s[8];
      float mx = -INFINITY;
#pragma unroll
      for (int t = 0; t < 8; ++t) {
        s[t] = *lp<f4>(sc + lane * 16 + t * 1024);
        const u32 p0 = pk(s[t][0], s[t][1]), p1 = pk(s[t][2], s[t][3]);
        const u32 r0 = pk(blo(p0) * 0.08838834764831845f, bhi(p0) * 0.08838834764831845f), r1 = pk(blo(p1) * 0.08838834764831845f, bhi(p1) * 0.08838834764831845f);
        s[t] = f4{blo(r0), bhi(r0), blo(r1), bhi(r1)};
        mx = fmaxf(fmaxf(mx, fmaxf(s[t][0], s[t][1])), fmaxf(s[t][2], s[t][3]));
      }
      mw = wave_max(mx);
      float sum = 0.f;
#pragma unroll
      for (int t = 0; t < 8; ++t) {
        v4u pp;
#pragma unroll
        for (int e = 0; e < 4; ++e) { const float p = __builtin_amdgcn_exp2f((s[t][e] - mw) * LOG2E); sum += p; pp[e] = pk(p, p); }
        *lp<v4u>(sc + lane * 16 + t * 1024) = pp;
      }
      lw = wave_sum(sum);
    }
    const int mlbase = W_ML + (b * 32 + h) * 64;
    if (lane == 0) __builtin_amdgcn_raw_buffer_store_b128(v4u{fu(mw), tag, fu(lw), tag}, rs, mlbase + (half * 2 + a) * 16, 0, SC1);
    u32 msk[4];
#pragma unroll
    for (int q = 0; q < 4; ++q) msk[q] = ((n & 7) >> 1) == q ? ((n & 1) ? 0xffff0000u : 0x0000ffffu) : 0u;
    f4 acc = zero4;
    float scale = 0.f, ga0 = 0.f, ga1 = 0.f, gb0 = 0.f, gb1 = 0.f;
    u32 xo0 = 0, xo1 = 0;
    const int oc = slice_col(pid, 2 * (lane & 3));
#pragma nounroll
    for (int j = 0; j < 128; ++j) {
      const int u = 256 + 2 * j + a;
      if (j == 100) {
        v4u ml; v2u gm[4];
        const int go = mod_off(b, oc);
#pragma nounroll
        for (u32 spin = 0; spin < SPIN_CAP; ++spin) {
          CFENCE();
          ml = __builtin_amdgcn_raw_buffer_load_b128(rs, mlbase + (lane & 3) * 16, 0, SC1);
          u32 bad = (ml.y ^ tag) | (ml.w ^ tag);
#pragma unroll
          for (int i = 0; i < 4; ++i) {
            gm[i] = __builtin_amdgcn_raw_buffer_load_b64(rs, go + (i & 1) * 16, (i & 2) ? 5 * 8192 : 2 * 8192, SC1);
            bad |= gm[i].y ^ tag;
          }
          xo0 = ldnt<u16>(x + (b * 512 + oc) * 2); xo1 = ldnt<u16>(x + (b * 512 + oc + 4) * 2);
          if (__all(bad == 0)) break;
        }
        ga0 = half_of(gm[0].x, oc & 1); ga1 = half_of(gm[1].x, oc & 1); gb0 = half_of(gm[2].x, oc & 1); gb1 = half_of(gm[3].x, oc & 1);
        float M = uf(ml.x); M = fmaxf(M, dppf<XOR1>(M)); M = fmaxf(M, dppf<XOR2>(M));
        float L = uf(ml.z) * __builtin_amdgcn_exp2f((uf(ml.x) - M) * LOG2E);
        L += dppf<XOR1>(L); L += dppf<XOR2>(L);
        scale = __builtin_amdgcn_exp2f((mw - M) * LOG2E) / L;
        asm volatile("" : "+v"(xo0), "+v"(xo1));
      }
      const v4u p4 = *lp<v4u>(sc + j * 64 + g * 16);
      v4u bf[4];
#pragma unroll
      for (int i = 0; i < 4; ++i) bf[i] = v4u{p4[i] & msk[0], p4[i] & msk[1], p4[i] & msk[2], p4[i] & msk[3]};
      asm volatile("" : "+v"(bf[0]), "+v"(bf[1]), "+v"(bf[2]), "+v"(bf[3]) :: "memory");
#pragma nounroll
      for (u32 spin = 0; landed <= u && spin < SPIN_CAP * 8; ++spin) landed = (int)lds_poll(C_LANDED);
      CFENCE();
      const u32 ad = ((u & 31) << 12) + lane * 16;
      v4u v0 = *lp<v4u>(ad), v1 = *lp<v4u>(ad + 1024), v2 = *lp<v4u>(ad + 2048), v3 = *lp<v4u>(ad + 3072);
      SBAR();
      acc = mfma32(v0, bf[0], acc); acc = mfma32(v1, bf[1], acc); acc = mfma32(v2, bf[2], acc);
      asm volatile("" : "+v"(v3) :: "memory");
      *(volatile LAS u32*)(C_CONS + 4 * a) = 128 + j + 1;
      acc = mfma32(v3, bf[3], acc);
    }
    {
      const float o0 = acc[0] * scale, o1 = acc[1] * scale, o2 = acc[2] * scale, o3 = acc[3] * scale;
      const u32 h0 = pk(o0, o1), h1 = pk(o2, o3);
      const u32 l0 = pk(o0 - blo(h0), o1 - bhi(h0)), l1 = pk(o2 - blo(h1), o3 - bhi(h1));
      const bool lo = n >= 8;
      *lp<v2u>(L_OA + (2 * a + (n >> 3)) * 320 + (g * 8 + (n & 7)) * 8) = v2u{lo ? l0 : h0, lo ? l1 : h1};
    }
    asm volatile("s_waitcnt lgkmcnt(0)" ::: "memory");
    __builtin_amdgcn_s_barrier();
    if (a == 1) return;
    STAMP(4);
    v4u m[4];
    const int io = (b * 64 + pid) * 4096 + lane * 16;
    delay(D1);
#pragma nounroll
    for (u32 spin = 0; spin < SPIN_CAP; ++spin) {
      CFENCE();
      u32 bad = 0;
#pragma unroll
      for (int i = 0; i < 4; ++i) m[i] = __builtin_amdgcn_raw_buffer_load_b128(rs, io + i * 1024, W_IN1, SC1);
#pragma unroll
      for (int i = 0; i < 4; ++i) bad |= (m[i].y ^ tag) | (m[i].w ^ tag);
      if (__all(bad == 0)) break;
    }
    STAMP(15);
    u32 yb = inbox_sum(m);
    u32 t = pk(ga0 * blo(yb), ga1 * bhi(yb));
    const u32 x1 = pk(blo(xo0) + blo(t), blo(xo1) + bhi(t));
    if (lane < 32) __builtin_amdgcn_raw_buffer_store_b64(v2u{x1, tag}, rs, W_X1 + (b * 8 + (lane >> 2)) * 2048 + pid * 32 + (lane & 3) * 8, 0, SC1);
    __builtin_amdgcn_s_barrier();
    STAMP(16);
    __builtin_amdgcn_s_barrier();
    delay(D3);
#pragma nounroll
    for (u32 spin = 0; spin < SPIN_CAP; ++spin) {
      CFENCE();
      u32 bad = 0;
#pragma unroll
      for (int i = 0; i < 4; ++i) m[i] = __builtin_amdgcn_raw_buffer_load_b128(rs, io + (i == 3 && lane >= 32 ? 0 : i * 1024), W_IN3, SC1);
#pragma unroll
      for (int i = 0; i < 4; ++i) bad |= (m[i].y ^ tag) | (m[i].w ^ tag);
      if (__all(bad == 0)) break;
    }
    STAMP(19);
    if (lane >= 32) m[3] = v4u{0u, 0u, 0u, 0u};
    yb = inbox_sum(m);
    t = pk(gb0 * blo(yb), gb1 * bhi(yb));
    const u32 r = pk(blo(x1) + blo(t), bhi(x1) + bhi(t));
    if (lane < 8) __hip_atomic_store((GAS u16*)(out + (b * 512 + oc + (lane & 4)) * 2), (u16)(lane < 4 ? r : r >> 16), __ATOMIC_RELAXED, __HIP_MEMORY_SCOPE_AGENT);
    if (blockIdx.x == 0) while (__builtin_amdgcn_s_memrealtime() < t_fix + 20000) __builtin_amdgcn_s_sleep(1);
    STAMP(20);
    return;
  }

  if (wave == 3) {
    u32 tref = (u32)__builtin_amdgcn_s_memrealtime() + T00;
    const u32 tag = (u32)__builtin_amdgcn_readfirstlane((int)__builtin_amdgcn_raw_buffer_load_b32(rs, W_EPOCH, 0, SC1)) + 1u;
    *(volatile LAS v2u*)C_TAG = v2u{tag, 1u};
    asm volatile("s_mov_b64 exec, 0\n s_branch .Lldr_copies\n .Lwarm_ret0:\n s_mov_b64 exec, -1" ::: "memory");
    int uref = 0, tau = TAU * 64 / 100, kp = 1, kr = 1;
#pragma nounroll
    for (u32 spin = 0; spin < SPIN_CAP * 8; ++spin) {
      const v3u cc = *(volatile LAS v3u*)C_CONS;
      const int u = (int)lds_poll(C_LANDED);
      const u32 now = (u32)__builtin_amdgcn_s_memrealtime();
      const u32 c0 = __builtin_amdgcn_readfirstlane(cc.x), c1 = __builtin_amdgcn_readfirstlane(cc.y), pl = __builtin_amdgcn_readfirstlane(cc.z) ^ (0x88888888u >> (32 - 4 * NPRE));
      const u32 done = pl ? (__builtin_ctz(pl) & 28) * 2 : max(8u * NPRE, 2 * min(c0, c1));
      const int lag = (int)(now - tref) - (((u - uref) * tau) >> 6);
      const int lvl = min(max(LVL0 + ((lag * GAIN + 128) >> 8), (CLOSED && uref) ? 0 : LVL0), 7);
      *(volatile LAS u32*)C_CTL = ((done + 32) << 12) | (lvl << 7);
      if (u >= 496 && done >= 480) break;
      if (CLOSED) {
        if (u >= 64 * kp) {
          if ((idx & 3) == 0 && lane == 0) __builtin_amdgcn_raw_buffer_store_b64(v2u{now, tag}, rs, W_PROG + kp * 512 + ((idx >> 2) * 8 + xcd) * 8, 0, SC1);
          ++kp;
        }
        if (u >= 64 * kr + RDLAG) {
          const v2u pr = __builtin_amdgcn_raw_buffer_load_b64(rs, W_PROG + kr * 512 + lane * 8, 0, SC1);
          const float ago = wave_sum(pr.y == tag ? (float)(int)(now - pr.x) : 0.f);
          const u32 tk = now - (u32)__builtin_amdgcn_readfirstlane((int)(ago * (1.f / 64.f)));
          if (uref) tau = min(max((int)(tk - tref), 800), 1000);
          tref = tk; uref = 64 * kr; ++kr;
        }
      }
      __builtin_amdgcn_s_sleep(3);
    }
    return;
  }

  const int w = wave - 4;
  if (first_mod) mod_tile<false>(cnd, wmod, rs, b * 8 + xcd, w, lane);
  {
    v4u kr[NPRE][4];
    const u32 ad = w * 4096 + n * 256 + ((g ^ n) << 4);
#pragma unroll
    for (int r = 0; r < NPRE; ++r) {
#pragma nounroll
      for (u32 spin = 0; (int)lds_poll(C_LANDED) <= 8 * r + w && spin < SPIN_CAP * 8; ++spin) __builtin_amdgcn_s_sleep(8);
      CFENCE();
#pragma unroll
      for (int s = 0; s < 4; ++s) kr[r][s] = *lp<v4u>((ad ^ (s * 64)) + (r & 3) * 32768);
      asm volatile("" : "+v"(kr[r][0]), "+v"(kr[r][1]), "+v"(kr[r][2]), "+v"(kr[r][3]) :: "memory");
      lds_add(C_PULL, 1u << (4 * r));
    }
#pragma nounroll
    for (u32 spin = 0; lds_poll(C_QGOT) == 0u && spin < SPIN_CAP * 8; ++spin) __builtin_amdgcn_s_sleep(8);
    CFENCE();
    v4u qa[4];
#pragma unroll
    for (int s = 0; s < 4; ++s) qa[s] = *lp<v4u>(L_QV + 64 * s + 16 * g);
#pragma unroll
    for (int r = 0; r < NPRE; ++r) {
      f4 d = mfma32(qa[0], kr[r][0], zero4);
      d = mfma32(qa[1], kr[r][1], d); d = mfma32(qa[2], kr[r][2], d); d = mfma32(qa[3], kr[r][3], d);
      *lp<float>(L_SC + (w & 1) * 8192 + (4 * r + (w >> 1)) * 64 + ((n & 3) * 4 + (n >> 2)) * 4) = d[0];
    }
    lds_add(C_PRE, 1u);
  }
  const u32 tag = get_tag();
  const int fs = 7 * xcd + (pid & 7);
  v4u fo[4][4], fg[2][4], fd[4];
  {
    const __amdgpu_buffer_rsrc_t ro = __builtin_amdgcn_make_buffer_rsrc((void*)(wo + (size_t)h * 131072), 0, (NOW & 1) ? 0 : 131072, 0x00020000);
    const int vo = 2 * g * 1024 + (64 * w + 4 * n) * 2;
#pragma unroll
    for (int s = 0; s < 4; ++s) {
      v2u r[8];
#pragma unroll
      for (int j = 0; j < 8; ++j) { r[j] = __builtin_amdgcn_raw_buffer_load_b64(ro, vo + (j >> 2) * 1024, (32 * s + 8 * (j & 3)) * 1024, NT); PACE(j); }
      tr8x4(r, fo[s]);
      PIN4(fo[s]);
      SBAR();
    }
  }
  if (has_ffn) {
    const char* pg = (n < 8 ? wg : wu) + (fs * 32 + 4 * (n & 7)) * 2;
#pragma unroll
    for (int s = 0; s < 2; ++s) {
      v2u r[8];
      const char* pr = pg + (size_t)slice_col(16 * g + 2 * w + s, 0) * (FF * 2);
#pragma unroll
      for (int j = 0; j < 8; ++j) { r[j] = (NOW & 2) ? v2u{0u, 0u} : ldnt<v2u>(pr + j * (4 * FF * 2)); PACE(j); }
      tr8x4(r, fg[s]);
      PIN4(fg[s]);
      SBAR();
    }
    v2u r[8];
    const __amdgpu_buffer_rsrc_t rd = __builtin_amdgcn_make_buffer_rsrc((void*)(wd + (size_t)fs * 32768), 0, (NOW & 2) ? 0 : 32768, 0x00020000);
    const int vd = 8 * g * 1024 + (64 * w + 4 * n) * 2;
#pragma unroll
    for (int j = 0; j < 8; ++j) { r[j] = __builtin_amdgcn_raw_buffer_load_b64(rd, vd + (j & 3) * 1024, (j >> 2) * 4096, NT); PACE(j); }
    tr8x4(r, fd);
    PIN4(fd);
    SBAR();
  } else {
    mod_tile<true>(cnd, wmod, rs, 16 + b * 8 + xcd, w, lane);
#pragma unroll
    for (int t = 0; t < 4; ++t) fg[0][t] = fg[1][t] = fd[t] = v4u{0u, 0u, 0u, 0u};
    nap<12000>();
  }
  {
    v2u sv, cv;
    const int so = mod_off(b, 64 * w + lane);
#pragma nounroll
    for (u32 spin = 0; spin < SPIN_CAP; ++spin) {
      CFENCE();
      sv = __builtin_amdgcn_raw_buffer_load_b64(rs, so, 3 * 8192, SC1);
      cv = __builtin_amdgcn_raw_buffer_load_b64(rs, so, 4 * 8192, SC1);
      if (__all(((sv.y ^ tag) | (cv.y ^ tag)) == 0)) break;
      __builtin_amdgcn_s_sleep(64);
    }
    *lp<u32>(L_SBCB + ((8 * w + 2 * (lane & 3) + (lane >> 5)) * 8 + ((lane >> 2) & 7)) * 4) = pk(half_of(sv.x, lane & 1), 1.f + half_of(cv.x, lane & 1));
  }
  if (blockIdx.x == 0 && w == 7) {
#pragma nounroll
    for (u32 spin = 0; spin < SPIN_CAP; ++spin) {
      CFENCE();
      u32 bad = 0;
#pragma nounroll
      for (int i = 0; i < 8; ++i) {
        const v4u t = __builtin_amdgcn_raw_buffer_load_b128(rs, W_ML + lane * 16, i * 1024, SC1);
        bad |= (t.y ^ tag) | (t.w ^ tag);
      }
      if (__all(bad == 0)) break;
      __builtin_amdgcn_s_sleep(64);
    }
    if (lane == 0) __builtin_amdgcn_raw_buffer_store_b32(tag, rs, W_EPOCH, 0, SC1);
  }
  if (w == 0) STAMP(13);
  const int mo = (b * 64 + 8 * w + (lane >> 3)) * 4096 + (lane & 7) * 8;
  const int gs = 16 * g + ((n + 2 * w) & 15);
  asm volatile("s_waitcnt vmcnt(0) lgkmcnt(0)" ::: "memory");
  __builtin_amdgcn_s_barrier();
  CFENCE();
  {
    v4u oa[4];
#pragma unroll
    for (int s = 0; s < 4; ++s) oa[s] = *lp<v4u>(L_OA + (n & 3) * 320 + s * 64 + g * 16);
    SBAR();
    f4 d[4] = {zero4, zero4, zero4, zero4};
#pragma unroll
    for (int s = 0; s < 4; ++s)
#pragma unroll
      for (int t = 0; t < 4; ++t) d[t] = mfma32(oa[s], fo[s][t], d[t]);
    float y[4];
#pragma unroll
    for (int t = 0; t < 4; ++t) y[t] = (d[t][0] + d[t][2]) + (d[t][1] + d[t][3]);
    const float y01 = (g & 1) ? y[1] : y[0], y23 = (g & 1) ? y[3] : y[2];
    __builtin_amdgcn_raw_buffer_store_b64(v2u{fu((g & 2) ? y23 : y01), tag}, rs, mo, W_IN1 + pid * 64, SC1);
  }
  if (w == 0) STAMP(14);
  float sb[8], cb[8];
  {
    const v4u s0 = *lp<v4u>(L_SBCB + gs * 32), s1 = *lp<v4u>(L_SBCB + gs * 32 + 16);
#pragma unroll
    for (int e = 0; e < 4; ++e) { sb[e] = blo(s0[e]); cb[e] = bhi(s0[e]); sb[4 + e] = blo(s1[e]); cb[4 + e] = bhi(s1[e]); }
    PIN4(sb); PIN4(sb + 4); PIN4(cb); PIN4(cb + 4);
  }
  __builtin_amdgcn_s_barrier();
  delay(D2);
  v4u m0, m1;
#pragma nounroll
  for (u32 spin = 0; spin < SPIN_CAP; ++spin) {
    CFENCE();
    m0 = __builtin_amdgcn_raw_buffer_load_b128(rs, gs * 32, W_X1 + (b * 8 + xcd) * 2048, SC1);
    m1 = __builtin_amdgcn_raw_buffer_load_b128(rs, gs * 32 + 16, W_X1 + (b * 8 + xcd) * 2048, SC1);
    if (__all(((m0.y ^ tag) | (m0.w ^ tag) | (m1.y ^ tag) | (m1.w ^ tag)) == 0)) break;
  }
  if (w == 0) STAMP(17);
  {
    const float v[8] = {blo(m0.x), bhi(m0.x), blo(m0.z), bhi(m0.z), blo(m1.x), bhi(m1.x), blo(m1.z), bhi(m1.z)};
    const float sh = uf((u32)__builtin_amdgcn_readlane((int)fu(v[0]), (16 - 2 * w) & 15));
    float d[8];
#pragma unroll
    for (int e = 0; e < 8; ++e) d[e] = v[e] - sh;
    float s1 = ((d[0] + d[1]) + (d[2] + d[3])) + ((d[4] + d[5]) + (d[6] + d[7]));
    float s2 = (fmaf(d[0], d[0], d[1] * d[1]) + fmaf(d[2], d[2], d[3] * d[3])) + (fmaf(d[4], d[4], d[5] * d[5]) + fmaf(d[6], d[6], d[7] * d[7]));
    s1 = wave_sum(s1) * (1.f / 512.f); s2 = wave_sum(s2) * (1.f / 512.f);
    const float mb = rbf(sh + s1), var = fmaxf(s2 - s1 * s1, 0.f);
    const float rsd = __builtin_amdgcn_rcpf(rbf(__builtin_amdgcn_sqrtf(rbf(rbf(var) + 1e-5f))));
    v4u ha;
#pragma unroll
    for (int e = 0; e < 4; ++e) ha[e] = pk(fmaf((v[2 * e] - mb) * rsd, cb[2 * e], sb[2 * e]), fmaf((v[2 * e + 1] - mb) * rsd, cb[2 * e + 1], sb[2 * e + 1]));
    f4 gp;
#pragma unroll
    for (int t = 0; t < 4; ++t) gp[t] = mfma32(ha, fg[0][t], zero4)[0] + mfma32(ha, fg[1][t], zero4)[1];
    if (g == 0) *lp<f4>(L_GURED + w * 256 + n * 16) = gp;
  }
  asm volatile("s_waitcnt lgkmcnt(0)" ::: "memory");
  __builtin_amdgcn_s_barrier();
  CFENCE();
  {
    float pt[8];
#pragma unroll
    for (int ww = 0; ww < 8; ++ww) pt[ww] = *lp<float>(L_GURED + ww * 256 + ((n >> 3) * 32 + 8 * g + (n & 7)) * 4);
    const float s = rbf(((pt[0] + pt[1]) + (pt[2] + pt[3])) + ((pt[4] + pt[5]) + (pt[6] + pt[7])));
    const float up = dppf<ROR8>(s);
    const float z = s * fmaf(s * s, 0.044715f * 0.7978845608028654f * 2.f * LOG2E, 0.7978845608028654f * 2.f * LOG2E);
    const float ge = rbf(s * __builtin_amdgcn_rcpf(1.f + __builtin_amdgcn_exp2f(-z)));
    const float act = ge * up;
    const u32 ap = pk(act, dppf<XOR1>(act));
    const v4u aa = {dppu<0x150>(ap), dppu<0x152>(ap), dppu<0x154>(ap), dppu<0x156>(ap)};
    float y[4];
#pragma unroll
    for (int t = 0; t < 4; ++t) y[t] = mfma32(aa, fd[t], zero4)[0];
    const float y01 = (g & 1) ? y[1] : y[0], y23 = (g & 1) ? y[3] : y[2];
    if (has_ffn) __builtin_amdgcn_raw_buffer_store_b64(v2u{fu((g & 2) ? y23 : y01), tag}, rs, mo, W_IN3 + fs * 64, SC1);
  }
  if (w == 0) STAMP(18);
}

}

extern "C" void kernel_launch(void* const* d_in, const int* in_sizes, int n_in, void* d_out, int out_size, void* d_ws, size_t ws_size, hipStream_t stream) {
  hipLaunchKernelGGL(blk::fused_block, dim3(256), dim3(blk::NTHR), 0, stream,
                     (const char*)d_in[0], (const char*)d_in[1], (const char*)d_in[2], (const char*)d_in[3], (const char*)d_in[4], (const char*)d_in[6],
                     (const char*)d_in[7], (const char*)d_in[8], (const char*)d_in[9], (const char*)d_in[10], (char*)d_out, (char*)d_ws);
}
```

```cpp
#define STAMPS 1
#include <hip/hip_runtime.h>
#pragma clang diagnostic ignored "-Wint-to-pointer-cast"

#define WLEVELS "17,18,19,20,21,23,25,28"
#define LVL0 2
#define U0 4
#define TAU 1400
#define T00 250
#define GAIN 8
#define CLOSED 1
#define RDLAG 24
#define WSTART 104
#define WSTEP 48
#define WARM 1
#define NPRE 8
#define CMOD 1u
#define HSLEEP 16
#define QSLEEP 4
#define HTIME 550
#define QDELAY 1500
#define WQFLY 3
#define PACEN 1
#define NOW 0
#define T1 90
#define T2 170
#define T3 280
#define D3 40
#define HINTK 6
#define CW 0
#define LASTW 5
#define EXPA 0
#define EXPB 0
#define EXPC 0
#define NUMA 0

namespace blk {

typedef unsigned short u16;
typedef unsigned u32;
typedef unsigned long long u64;
typedef u32 v4u __attribute__((ext_vector_type(4)));
typedef u32 v8u __attribute__((ext_vector_type(8)));
typedef __bf16 bf16x16 __attribute__((ext_vector_type(16)));
typedef u32 v3u __attribute__((ext_vector_type(3)));
typedef u32 v2u __attribute__((ext_vector_type(2)));
typedef float f4 __attribute__((ext_vector_type(4)));
typedef float f2 __attribute__((ext_vector_type(2)));
typedef __bf16 bf8 __attribute__((ext_vector_type(8)));
typedef __bf16 bf2 __attribute__((ext_vector_type(2)));
typedef short s4 __attribute__((ext_vector_type(4)));
#define GAS __attribute__((address_space(1)))
#define LAS __attribute__((address_space(3)))

constexpr int NTHR = 768;
constexpr int FF = 1792;
constexpr int L_SC = 131072;
constexpr int L_MODRED = L_SC + 16384;
constexpr int L_GURED = L_MODRED + 8192;
constexpr int L_OA = L_GURED + 4096;
constexpr int L_C12 = L_MODRED;
constexpr int L_HA = L_GURED;
constexpr int L_QV = L_OA + 960;
constexpr int L_CTRL = L_QV + 256;
constexpr int C_CONS = L_CTRL, C_PULL = L_CTRL + 8, C_MODCNT = L_CTRL + 12, C_PRE = L_CTRL + 16, C_QGOT = L_CTRL + 20, C_TAG = L_CTRL + 24;
constexpr int C_HGOT = L_CTRL + 36, C_ISSUED = L_CTRL + 40, C_SCNT = L_CTRL + 44, C_SCALE = L_CTRL + 48, C_LN = L_CTRL + 56;
constexpr int C_CTL = L_CTRL + 32;
constexpr int C_LANDED = L_CTRL + 64;
constexpr int L_STAMP = C_LANDED + 256;
constexpr int LDS_BYTES = L_STAMP + (STAMPS ? 256 : 0);
constexpr int W_EPOCH = 0, W_PROG = 4096, W_ML = 16384;
constexpr int W_MOD = 65536;
constexpr int W_Q = 131072;
constexpr int W_LATE = 262144, W_END = W_LATE + 72 * 16384, W_DBG = W_END;
__device__ __forceinline__ int late(u32 sides, int x, int blk) { return W_LATE + ((x & 3) * 18 + blk) * 16384 + (int)((((u32)x >> 2) ^ (sides >> (x & 3))) & 1u) * 8192; }
constexpr int SC1 = 16, NT = 2;
constexpr u32 SPIN_CAP = 1u << 18;

__device__ __forceinline__ float blo(u32 u) { return __builtin_bit_cast(float, u << 16); }
__device__ __forceinline__ float bhi(u32 u) { return __builtin_bit_cast(float, u & 0xffff0000u); }
__device__ __forceinline__ u32 pk(float a, float b) { f2 p = {a, b}; return __builtin_bit_cast(u32, __builtin_convertvector(p, bf2)); }
__device__ __forceinline__ float rbf(float a) { return blo(pk(a, a)); }
__device__ __forceinline__ u32 fu(float a) { return __builtin_bit_cast(u32, a); }
__device__ __forceinline__ float uf(u32 a) { return __builtin_bit_cast(float, a); }
__device__ __forceinline__ f4 mfma32(v4u a, v4u b, f4 c) { return __builtin_amdgcn_mfma_f32_16x16x32_bf16(__builtin_bit_cast(bf8, a), __builtin_bit_cast(bf8, b), c, 0, 0, 0); }
__device__ __forceinline__ f4 mfma16(v2u a, v2u b, f4 c) { return __builtin_amdgcn_mfma_f32_16x16x16bf16_1k(__builtin_bit_cast(s4, a), __builtin_bit_cast(s4, b), c, 0, 0, 0); }
__device__ __forceinline__ float dot2(u32 a, u32 b, float c) { return __builtin_amdgcn_fdot2_f32_bf16(__builtin_bit_cast(bf2, a), __builtin_bit_cast(bf2, b), c, false); }
__device__ __forceinline__ f4 smfmac(v4u a, v8u b, f4 c, u32 idx) { return __builtin_amdgcn_smfmac_f32_16x16x64_bf16(__builtin_bit_cast(bf8, a), __builtin_bit_cast(bf16x16, b), c, (int)idx, 0, 0); }
template <int CTRL> __device__ __forceinline__ u32 dppu(u32 x) { return (u32)__builtin_amdgcn_mov_dpp((int)x, CTRL, 0xf, 0xf, true); }
template <int CTRL> __device__ __forceinline__ float dppf(float x) { return uf(dppu<CTRL>(fu(x))); }
constexpr int XOR1 = 0xB1, XOR2 = 0x4E, HMIR = 0x141, ROR4 = 0x124, ROR8 = 0x128;
__device__ __forceinline__ float xrow_sum(float x) {
  auto s = __builtin_amdgcn_permlane16_swap(fu(x), fu(x), false, false);
  x = uf(s[0]) + uf(s[1]);
  auto t = __builtin_amdgcn_permlane32_swap(fu(x), fu(x), false, false);
  return uf(t[0]) + uf(t[1]);
}
__device__ __forceinline__ float xrow_max(float x) {
  auto s = __builtin_amdgcn_permlane16_swap(fu(x), fu(x), false, false);
  x = fmaxf(uf(s[0]), uf(s[1]));
  auto t = __builtin_amdgcn_permlane32_swap(fu(x), fu(x), false, false);
  return fmaxf(uf(t[0]), uf(t[1]));
}
__device__ __forceinline__ float wave_sum(float x) { x += dppf<XOR1>(x); x += dppf<XOR2>(x); x += dppf<HMIR>(x); x += dppf<ROR8>(x); return xrow_sum(x); }
__device__ __forceinline__ float wave_max(float x) { x = fmaxf(x, dppf<XOR1>(x)); x = fmaxf(x, dppf<XOR2>(x)); x = fmaxf(x, dppf<HMIR>(x)); x = fmaxf(x, dppf<ROR8>(x)); return xrow_max(x); }
__device__ __forceinline__ u32 perm_lo(u32 r0, u32 r1) { return __builtin_amdgcn_perm(r1, r0, 0x05040100u); }
__device__ __forceinline__ u32 perm_hi(u32 r0, u32 r1) { return __builtin_amdgcn_perm(r1, r0, 0x07060302u); }
#define CFENCE() asm volatile("" ::: "memory")
#define SBAR() __builtin_amdgcn_sched_barrier(0)
#define PIN4(a) asm volatile("" : "+v"((a)[0]), "+v"((a)[1]), "+v"((a)[2]), "+v"((a)[3]))
template <typename T> __device__ __forceinline__ LAS T* lp(u32 off) { return (LAS T*)off; }
__device__ __forceinline__ u32 lds_poll(u32 off) { return (u32)__builtin_amdgcn_readfirstlane((int)*(volatile LAS u32*)off); }
__device__ __forceinline__ void lds_add(u32 off, u32 v) {
  asm volatile("s_mov_b64 exec, 1\n ds_add_u32 %0, %1\n s_mov_b64 exec, -1" :: "v"(off), "v"(v) : "memory");
}
template <typename T> __device__ __forceinline__ T ldnt(const void* p) { return __builtin_nontemporal_load((const GAS T*)p); }
template <typename T> __device__ __forceinline__ T ldg(const void* p) { return *(const GAS T*)p; }
__device__ __forceinline__ void tr8x4(const v2u (&r)[8], v4u (&f)[4]) {
#pragma unroll
  for (int q = 0; q < 4; ++q) {
    f[0][q] = perm_lo(r[2 * q].x, r[2 * q + 1].x); f[1][q] = perm_hi(r[2 * q].x, r[2 * q + 1].x);
    f[2][q] = perm_lo(r[2 * q].y, r[2 * q + 1].y); f[3][q] = perm_hi(r[2 * q].y, r[2 * q + 1].y);
  }
}
template <int HALF> __device__ __forceinline__ void tr8x4(const v2u (&r)[8], v8u (&f)[4]) {
#pragma unroll
  for (int q = 0; q < 4; ++q) {
    f[0][4 * HALF + q] = perm_lo(r[2 * q].x, r[2 * q + 1].x); f[1][4 * HALF + q] = perm_hi(r[2 * q].x, r[2 * q + 1].x);
    f[2][4 * HALF + q] = perm_lo(r[2 * q].y, r[2 * q + 1].y); f[3][4 * HALF + q] = perm_hi(r[2 * q].y, r[2 * q + 1].y);
  }
}
#define WARM_AT(k, nxt) do { SBAR(); asm volatile(".Lwarm_" #k ":\n s_cbranch_execz .Lwarm_" #nxt); SBAR(); } while (0)

#define CYC(i) do { } while (0)
#define STAMP(i) do { SBAR(); const u64 t_ = __builtin_amdgcn_s_memrealtime(); *(volatile LAS u64*)(L_STAMP + (i) * 8) = t_; SBAR(); } while (0)

__device__ __forceinline__ int mod_off(int b, int c) { return W_MOD + (c >> 6) * 1024 + b * 256 + ((c & 63) >> 1) * 8; }
__device__ __forceinline__ float half_of(u32 v, int odd) { return odd ? bhi(v) : blo(v); }
__device__ __forceinline__ void delay(int cyc) {
  const u64 t0 = __builtin_amdgcn_s_memtime();
#pragma nounroll
  while ((int)(__builtin_amdgcn_s_memtime() - t0) < cyc) { }
}
template <int CYC> __device__ __forceinline__ void nap() {
#pragma unroll
  for (int i = 0; i < CYC / 8128; ++i) __builtin_amdgcn_s_sleep(127);
  if (CYC % 8128 >= 64) __builtin_amdgcn_s_sleep((CYC % 8128) / 64);
}
__device__ __forceinline__ u32 get_tag() {
  v2u t;
#pragma nounroll
  for (u32 spin = 0; spin < SPIN_CAP * 8; ++spin) { t = *(volatile LAS v2u*)C_TAG; if (__builtin_amdgcn_readfirstlane((int)t.y)) break; }
  return (u32)__builtin_amdgcn_readfirstlane((int)t.x);
}
__device__ __forceinline__ u32 now32() { return (u32)__builtin_amdgcn_s_memrealtime(); }
__device__ __forceinline__ void wait_until(u32 t) {
#pragma nounroll
  while ((int)(now32() - t) < 0) { }
}
__device__ __forceinline__ float swap32_sum(float a, float b) {
  auto t = __builtin_amdgcn_permlane32_swap(fu(a), fu(b), false, false);
  return uf(t[0]) + uf(t[1]);
}
__device__ __forceinline__ float swap16_sum(float a, float b) {
  auto t = __builtin_amdgcn_permlane16_swap(fu(a), fu(b), false, false);
  return uf(t[0]) + uf(t[1]);
}
__device__ __forceinline__ void inbox_sum1(const v4u (&m)[4], float& y0, float& y1) {
  const float a0 = (blo(m[0].x) + blo(m[1].x)) + (blo(m[2].x) + blo(m[3].x)), a1 = (bhi(m[0].x) + bhi(m[1].x)) + (bhi(m[2].x) + bhi(m[3].x));
  const float b0 = (blo(m[0].z) + blo(m[1].z)) + (blo(m[2].z) + blo(m[3].z)), b1 = (bhi(m[0].z) + bhi(m[1].z)) + (bhi(m[2].z) + bhi(m[3].z));
  y0 = swap32_sum(a0, b0); y1 = swap32_sum(a1, b1);
}
__device__ __forceinline__ u32 inbox_sum2(float y0, float y1) {
  float y = swap16_sum(y0, y1);
  y += dppf<ROR4>(y); y += dppf<ROR8>(y);
  auto t = __builtin_amdgcn_permlane16_swap(fu(y), fu(y), false, false);
  return pk(uf(t[0]), uf(t[1]));
}
__device__ __forceinline__ bool tags_ok(const v4u (&m)[4], u32 tag, u32 tag3) {
  u32 bad = ((m[0].y ^ tag) | (m[0].w ^ tag)) | ((m[1].y ^ tag) | (m[1].w ^ tag));
  asm("" : "+v"(bad));
  bad |= (m[2].y ^ tag) | (m[2].w ^ tag);
  asm("" : "+v"(bad));
  bad |= (m[3].y ^ tag3) | (m[3].w ^ tag3);
  asm("" : "+v"(bad));
  return __builtin_amdgcn_ballot_w64(bad != 0) == 0;
}
__device__ __forceinline__ bool tags_ok(const v4u (&m)[4], u32 tag) { return tags_ok(m, tag, tag); }
__device__ __forceinline__ void gu_mfma(u32 p, u32 idx, const v8u (&f)[4], f4 (&d)[4]) {
  const v4u a = {dppu<0x00>(p), dppu<0x55>(p), dppu<0xAA>(p), dppu<0xFF>(p)};
#pragma unroll
  for (int t = 0; t < 4; ++t) d[t] = smfmac(a, f[t], d[t], idx);
}
__device__ __forceinline__ float gu_fold(const f4 (&d)[4]) { return swap16_sum(swap32_sum(d[0][0], d[2][0]), swap32_sum(d[1][0], d[3][0])); }
__device__ __forceinline__ float gu_dot(u32 p, u32 idx, const v8u (&f)[4]) { f4 d[4] = {}; gu_mfma(p, idx, f, d); return gu_fold(d); }
__device__ __forceinline__ float sum8(u32 addr) {
  float p[8];
#pragma unroll
  for (int i = 0; i < 8; ++i) p[i] = *lp<float>(addr + i * 256);
  return ((p[0] + p[1]) + (p[2] + p[3])) + ((p[4] + p[5]) + (p[6] + p[7]));
}
__device__ __forceinline__ void wait_pos(int u) {
#pragma nounroll
  for (u32 spin = 0; (int)lds_poll(C_LANDED) < u && spin < SPIN_CAP * 8; ++spin) __builtin_amdgcn_s_sleep(4);
}
#define PACE(j) do { if (((j) % PACEN) == PACEN - 1) { asm volatile("s_waitcnt vmcnt(0)" ::: "memory"); SBAR(); } } while (0)
constexpr float LOG2E = 1.4426950408889634f;

template <bool PACED>
__device__ __forceinline__ void mod_tile(const char* cnd, const char* wmod, __amdgpu_buffer_rsrc_t rs, int cb, int w, int lane) {
  const int g = lane >> 4, n = lane & 15;
  const f4 zero4 = {0.f, 0.f, 0.f, 0.f};
  const v2u cv = ldnt<v2u>(cnd + ((n >> 2) * 512 + 64 * w + 16 * g + 4 * (n & 3)) * 2);
  const __amdgpu_buffer_rsrc_t rm = __builtin_amdgcn_make_buffer_rsrc((void*)(wmod + (size_t)(64 * w) * 6144 + cb * 128), 0, 64 * 6144, 0x00020000);
  v2u r[2][8];
#pragma unroll
  for (int j = 0; j < 16; ++j) { r[j >> 3][j & 7] = __builtin_amdgcn_raw_buffer_load_b64(rm, 16 * g * 6144 + n * 8, j * 6144, NT); if (PACED) PACE(j); }
  SBAR();
  u32 sp[2];
#pragma unroll
  for (int e = 0; e < 2; ++e) {
    const float c0 = blo(cv[e]), c1 = bhi(cv[e]);
    sp[e] = pk(c0 * __builtin_amdgcn_rcpf(1.f + __builtin_amdgcn_exp2f(-c0 * LOG2E)), c1 * __builtin_amdgcn_rcpf(1.f + __builtin_amdgcn_exp2f(-c1 * LOG2E)));
  }
  const v4u a0 = {dppu<0x00>(sp[0]), dppu<0x00>(sp[1]), dppu<0x55>(sp[0]), dppu<0x55>(sp[1])};
  const v4u a1 = {dppu<0xAA>(sp[0]), dppu<0xAA>(sp[1]), dppu<0xFF>(sp[0]), dppu<0xFF>(sp[1])};
  v4u f0[4], f1[4];
  tr8x4(r[0], f0); tr8x4(r[1], f1);
  f4 pt;
#pragma unroll
  for (int t = 0; t < 4; ++t) pt[t] = mfma32(a1, f1[t], mfma32(a0, f0[t], zero4))[0];
  *lp<f4>(L_MODRED + w * 1024 + lane * 16) = pt;
  u32 old;
  asm volatile("s_mov_b64 exec, 1\n ds_add_rtn_u32 %0, %1, %2\n s_mov_b64 exec, -1\n s_waitcnt lgkmcnt(0)" : "=&v"(old) : "v"(C_MODCNT), "v"(1u) : "memory");
  if ((__builtin_amdgcn_readfirstlane((int)old) & 7) == 7) {
    f4 p[8];
#pragma unroll
    for (int ww = 0; ww < 8; ++ww) p[ww] = *lp<f4>(L_MODRED + ww * 1024 + lane * 16);
    const f4 v = ((p[0] + p[1]) + (p[2] + p[3])) + ((p[4] + p[5]) + (p[6] + p[7]));
    const u32 tag = get_tag();
    __builtin_amdgcn_raw_buffer_store_b128(v4u{pk(v[0], v[1]), tag, pk(v[2], v[3]), tag}, rs, lane * 16 + W_MOD + cb * 1024, 0, SC1);
    if (!PACED) STAMP(9);
  }
}

__global__ void __launch_bounds__(NTHR) fused_block(const char* x, const char* kc, const char* vc, const char* wq, const char* wo, const char* wg,
                                                    const char* wu, const char* wd, const char* cnd, const char* wmod, char* out, char* ws) {
  __shared__ __attribute__((aligned(1024))) char lds_mem[LDS_BYTES];
  const int lane = threadIdx.x & 63, wave = __builtin_amdgcn_readfirstlane(threadIdx.x >> 6);
  const int xcd = blockIdx.x & 7, idx = blockIdx.x >> 3;
  const int half = idx & 1, hh = (idx >> 1) & 3, b = idx >> 3;
  const int h = xcd + 8 * hh;
  const int pid = 8 * xcd + (idx & 7);
  const bool has_ffn = (idx & 7) < 7;
  const bool first_mod = (idx & 0x17) == 7;
  const int g = lane >> 4, n = lane & 15;
  const f4 zero4 = {0.f, 0.f, 0.f, 0.f};
  const u64 t_start = __builtin_amdgcn_s_memrealtime();
  constexpr u32 HOLES = 0xA000u;
  const size_t reg_off = ((size_t)(b * 32 + h) << 21) + half * 32768 - HOLES;
  const u64 kb = (u64)(kc + reg_off);
  v4u rsrc = {(u32)kb, (u32)(kb >> 32) & 0xffffu, (2u << 20) + 0x2000u, 0x00020000u};
  u32 o0 = g * 256 + ((n ^ g) << 4), o1 = o0 ^ 64, o2 = (o0 ^ 128) + 6144, o3 = (o0 ^ 192) + 6144;
  static_assert(U0 == 4, "");
#define LD0(v, o) "buffer_load_dwordx4 %[" #v "], %[rs], %[src] offen offset:" #o " nt lds\n"
  {
    u32 src = HOLES;
    asm volatile("s_cmp_lg_u32 %[wv], 0\n s_cbranch_scc1 2f\n s_cmp_eq_u32 %[fm], 7\n s_cbranch_scc1 2f\n s_mov_b32 m0, 0\n"
                 "1:\n s_nop 0\n" LD0(v0, 0) LD0(v1, 1024) LD0(v2, 2048) LD0(v3, 3072)
                 "s_addk_i32 %[src], 0x800\n s_add_u32 m0, m0, 0x1000\n s_cmp_lt_u32 m0, 0x4000\n s_cbranch_scc1 1b\n2:"
                 : [src] "+&s"(src) : [wv] "s"(wave), [fm] "s"(idx & 0x17), [v0] "v"(o0), [v1] "v"(o1), [v2] "v"(o2), [v3] "v"(o3), [rs] "s"(rsrc), "s"(ws) : "memory", "scc");
  }
  if (threadIdx.x < (STAMPS ? 144 : 20)) *lp<u32>(L_CTRL + 4 * threadIdx.x) = threadIdx.x == 8 ? (32u << 12) | (LVL0 * 128) : 0u;
  asm volatile("s_waitcnt lgkmcnt(0)" :: "v"(lds_mem) : "memory");
  __builtin_amdgcn_s_barrier();

  if (wave == 0) {
    __builtin_amdgcn_s_setprio(3);
    const u32 va = C_LANDED + lane * 4;
    const u64 vb = (u64)(vc + reg_off);
    STAMP(5);
    if (first_mod) {
#pragma nounroll
      for (u32 spin = 0; lds_poll(C_MODCNT) < CMOD && spin < SPIN_CAP * 8; ++spin) __builtin_amdgcn_s_sleep(1);
    }
    u32 off = first_mod ? 0u : 0x4000u, src = first_mod ? HOLES : HOLES | 0x4000u;
    u32 ctl = (32u << 12) | (LVL0 * 128);
#pragma nounroll
    for (u32 end = 1u << 20; end < (2u << 20); end += (1u << 20) - 0x4000u) {
      u32 cur = off, spin = SPIN_CAP * 8, blk, tmp, pcl, pch, pub;
#define LD(v, o) "s_waitcnt vmcnt(\\w)\n" LD0(v, o)
      asm volatile(
        "s_getpc_b64 vcc\n"
        ".Lldr_pc:\n s_add_u32 %[pcl], vcc_lo, .Lldr_copies-.Lldr_pc\n s_addc_u32 %[pch], vcc_hi, 0\n"
        ".Lldr_next:\n s_waitcnt lgkmcnt(4)\n"
        ".Lldr_dec:\n v_readfirstlane_b32 %[tmp], %[ctl]\n ds_read_b32 %[ctl], %[ca]\n s_add_u32 %[blk], %[off], 0x4000\n"
        "s_cmp_gt_u32 %[blk], %[tmp]\n s_cbranch_scc1 .Lldr_stall\n"
        "s_and_b32 %[tmp], %[tmp], 0x380\n s_add_u32 vcc_lo, %[pcl], %[tmp]\n s_addc_u32 vcc_hi, %[pch], 0\n s_lshr_b32 %[tmp], %[off], 12\n s_setpc_b64 vcc\n"
        ".Lldr_stall:\n s_waitcnt lgkmcnt(0)\n s_sub_u32 %[spin], %[spin], 1\n s_cbranch_scc0 .Lldr_dec\n s_branch .Lldr_exit\n"
        ".p2align 7\n.Lldr_copies:\n"
        ".irp w," WLEVELS "\n"
        "s_cbranch_execz 2f\n"
        "v_add_u32_e64 %[pub], %[tmp], -(\\w/4)\n"
        "1:\n s_and_b32 m0, %[off], 0x1ffff\n" LD(v0, 0) "s_addk_i32 %[off], 0x1000\n" LD(v1, 1024) LD(v2, 2048) LD(v3, 3072)
        "ds_write_b32 %[va], %[pub]\n s_addk_i32 %[src], 0x800\n s_or_b32 %[src], %[src], 0xa000\n v_add_u32 %[pub], 1, %[pub]\n"
        "s_cmp_lt_u32 %[off], %[blk]\n s_cbranch_scc1 1b\n s_cmp_lt_u32 %[off], %[end]\n s_cbranch_scc1 .Lldr_next\n s_branch .Lldr_exit\n"
        ".p2align 7\n2:\n.endr\n"
        "s_branch .Lwarm_ret0\n"
        ".Lldr_exit:\n s_waitcnt lgkmcnt(0)"
        : [off] "+&s"(cur), [src] "+&s"(src), [spin] "+&s"(spin), [blk] "=&s"(blk), [tmp] "=&s"(tmp), [pcl] "=&s"(pcl), [pch] "=&s"(pch), [pub] "=&v"(pub), [ctl] "+&v"(ctl)
        : [v0] "v"(o0), [v1] "v"(o1), [v2] "v"(o2), [v3] "v"(o3), [rs] "s"(rsrc), [end] "s"(end), [va] "v"(va), [ca] "v"(C_CTL) : "memory", "scc", "vcc");
      rsrc.x = (u32)vb; rsrc.y = (u32)(vb >> 32) & 0xffffu;
      o0 = o1 = lane * 16; o2 = o3 = lane * 16 + 6144;
      off = end; src = HOLES;
    }
    *(volatile LAS u32*)C_ISSUED = 1u;
#pragma unroll
    for (int k = 6; k >= 0; --k) {
      const u32 pb = 508 - k;
      asm volatile("s_waitcnt vmcnt(%2)\n ds_write_b32 %0, %1" :: "v"(va), "v"(pb), "i"(4 * k) : "memory");
    }
    *(volatile LAS u64*)L_STAMP = t_start;
    *(volatile LAS u64*)(L_STAMP + 24 * 8) = (u32)__builtin_amdgcn_s_getreg(20 | (3 << 11)) & 0xfu;
    return;
  }

  const __amdgpu_buffer_rsrc_t rs = __builtin_amdgcn_make_buffer_rsrc((void*)ws, 0, W_END, 0x00020000);
  const u32 sides = NUMA;

  if (wave <= 2) {
    const int a = wave - 1;
    u32 tag;
    if (a == 0) {
      if (first_mod) {
#pragma nounroll
        for (u32 spin = 0; lds_poll(C_MODCNT) < CMOD && spin < SPIN_CAP * 8; ++spin) __builtin_amdgcn_s_sleep(1);
      }
      const char* wt = wq + (h * 128 + (2 * b + half) * 16) * 2;
      const u32 vo = (64 * (lane >> 5) + 16 * ((lane >> 3) & 3) + ((lane >> 1) & 3)) * 8192 + (lane & 1) * 16;
#pragma nounroll
      for (int p = 0; p < 16; ++p)
        asm volatile("s_mov_b32 m0, %2\n s_waitcnt vmcnt(%3)\n global_load_lds_dwordx4 %0, %1 nt"
                     :: "v"(vo), "s"(wt + (size_t)(128 * (p >> 2) + 4 * (p & 3)) * 8192), "s"(L_SC + p * 1024), "i"(WQFLY - 1) : "memory");
      asm volatile("s_waitcnt vmcnt(0)" ::: "memory");
      v4u fq[16];
#pragma unroll
      for (int p = 0; p < 16; ++p) {
        const v2u lo = __builtin_bit_cast(v2u, __builtin_amdgcn_ds_read_tr16_b64_v4i16(lp<s4>(L_SC + p * 1024 + lane * 8)));
        const v2u hi = __builtin_bit_cast(v2u, __builtin_amdgcn_ds_read_tr16_b64_v4i16(lp<s4>(L_SC + p * 1024 + 512 + lane * 8)));
        fq[p] = v4u{lo.x, lo.y, hi.x, hi.y};
      }
      PIN4(fq); PIN4(fq + 4); PIN4(fq + 8); PIN4(fq + 12);
      STAMP(10);
      tag = get_tag();
#pragma nounroll
      for (u32 spin = 0; lds_poll(C_HGOT) == 0u && spin < SPIN_CAP * 8; ++spin) __builtin_amdgcn_s_sleep(1);
      CFENCE();
      f4 acc[4] = {zero4, zero4, zero4, zero4};
#pragma unroll
      for (int sg = 0; sg < 4; ++sg) {
        const v4u ha = *lp<v4u>(L_HA + sg * 1024 + lane * 16);
#pragma unroll
        for (int e = 0; e < 4; ++e) acc[e] = mfma32(ha, fq[4 * sg + e], acc[e]);
      }
      const float qv = (acc[0][0] + acc[1][1]) + (acc[2][2] + acc[3][3]);
      const u32 pr = pk(qv, dppf<XOR1>(qv));
      if (!(n & 1)) __builtin_amdgcn_raw_buffer_store_b64(v2u{pr, tag}, rs, W_Q + (h * 4 + g) * 512 + (2 * b + half) * 64 + (n >> 1) * 8, 0, SC1);
      STAMP(12);
      nap<QDELAY>();
      v2u m;
#pragma nounroll
      for (u32 spin = 0; spin < SPIN_CAP; ++spin) {
        CFENCE();
        m = __builtin_amdgcn_raw_buffer_load_b64(rs, lane * 8, W_Q + (h * 4 + b) * 512, SC1);
        if (__all(m.y == tag)) break;
        __builtin_amdgcn_s_sleep(QSLEEP);
      }
      *lp<u32>(L_QV + lane * 4) = m.x;
      CFENCE();
      *(volatile LAS u32*)C_QGOT = 1u;
    } else {
      const u32 t1 = (u32)__builtin_amdgcn_s_memrealtime() + HTIME;
      const int beta = n >> 2, kq = 16 * g + 4 * (n & 3);
      v2u xr[8];
#pragma unroll
      for (int i = 0; i < 8; ++i) xr[i] = ldnt<v2u>(x + (beta * 512 + 64 * i + kq) * 2);
      float s = 0.f;
#pragma unroll
      for (int i = 0; i < 8; ++i) s += (blo(xr[i].x) + bhi(xr[i].x)) + (blo(xr[i].y) + bhi(xr[i].y));
      s += dppf<XOR1>(s); s += dppf<XOR2>(s);
      const float mean = xrow_sum(s) * (1.f / 512.f);
      float q = 0.f;
#pragma unroll
      for (int i = 0; i < 8; ++i)
#pragma unroll
        for (int e = 0; e < 2; ++e) { const float d0 = blo(xr[i][e]) - mean, d1 = bhi(xr[i][e]) - mean; q = fmaf(d0, d0, q); q = fmaf(d1, d1, q); }
      q += dppf<XOR1>(q); q += dppf<XOR2>(q);
      const float mb = rbf(mean), rsd = __builtin_amdgcn_rcpf(rbf(__builtin_amdgcn_sqrtf(rbf(rbf(xrow_sum(q) * (1.f / 512.f)) + 1e-5f))));
#pragma unroll
      for (int i = 0; i < 8; ++i)
#pragma unroll
        for (int e = 0; e < 2; ++e) { const u32 d = pk(blo(xr[i][e]) - mb, bhi(xr[i][e]) - mb); xr[i][e] = pk(blo(d) * rsd, bhi(d) * rsd); }
      PIN4(xr); PIN4(xr + 4);
      tag = get_tag();
#pragma nounroll
      while ((int)((u32)__builtin_amdgcn_s_memrealtime() - t1) < 0) __builtin_amdgcn_s_sleep(2);
      v4u sa[8], ca[8];
#pragma nounroll
      for (u32 spin = 0; spin < SPIN_CAP; ++spin) {
        CFENCE();
        {
          u32 bad = 0;
#pragma unroll
          for (int i = 0; i < 8; ++i) {
            sa[i] = __builtin_amdgcn_raw_buffer_load_b128(rs, beta * 256 + kq * 4 + (i & 3) * 1024, W_MOD + (i >> 2) * 4096, SC1);
            ca[i] = __builtin_amdgcn_raw_buffer_load_b128(rs, beta * 256 + kq * 4 + (i & 3) * 1024, W_MOD + 8192 + (i >> 2) * 4096, SC1);
          }
          SBAR();
#pragma unroll
          for (int i = 0; i < 8; ++i) bad |= (sa[i].y ^ tag) | (sa[i].w ^ tag) | (ca[i].y ^ tag) | (ca[i].w ^ tag);
          if (__all(bad == 0)) break;
        }
        __builtin_amdgcn_s_sleep(HSLEEP);
      }
#pragma unroll
      for (int sg = 0; sg < 4; ++sg) {
        v4u ha;
#pragma unroll
        for (int r = 0; r < 4; ++r) {
          const int i = 2 * sg + (r >> 1), e = r & 1;
          const u32 c1 = pk(1.f + blo(ca[i][2 * e]), 1.f + bhi(ca[i][2 * e]));
          const u32 t = pk(blo(xr[i][e]) * blo(c1), bhi(xr[i][e]) * bhi(c1));
          ha[r] = pk(blo(t) + blo(sa[i][2 * e]), bhi(t) + bhi(sa[i][2 * e]));
        }
        *lp<v4u>(L_HA + sg * 1024 + lane * 16) = ha;
      }
      CFENCE();
      *(volatile LAS u32*)C_HGOT = 1u;
      STAMP(11);
#pragma nounroll
      for (u32 spin = 0; lds_poll(C_QGOT) == 0u && spin < SPIN_CAP * 8; ++spin) __builtin_amdgcn_s_sleep(4);
    }
    CFENCE();
    v4u qa[4];
#pragma unroll
    for (int s = 0; s < 4; ++s) qa[s] = *lp<v4u>(L_QV + 64 * s + 16 * g);
    if (a == 0) STAMP(2);
    const u32 sc = L_SC + a * 8192;
    int landed = 0;
    const u32 kofs = n * 256 + ((g ^ n) << 4);
    const u32 spos = sc + ((n & 3) * 4 + (n >> 2)) * 4;
#pragma nounroll
    for (int j = 4 * NPRE; j < 128; ++j) {
      const int u = 2 * j + a;
#pragma nounroll
      for (u32 spin = 0; landed <= u && spin < SPIN_CAP * 8; ++spin) landed = (int)lds_poll(C_LANDED);
      CFENCE();
      const u32 ad = ((u & 31) << 12) + kofs;
      v4u k0 = *lp<v4u>(ad), k1 = *lp<v4u>(ad ^ 64), k2 = *lp<v4u>(ad ^ 128), k3 = *lp<v4u>(ad ^ 192);
      asm volatile("" : "+v"(k0), "+v"(k1), "+v"(k2), "+v"(k3) :: "memory");
      *(volatile LAS u32*)(C_CONS + 4 * a) = j + 1;
      f4 d = mfma32(qa[0], k0, zero4);
      d = mfma32(qa[1], k1, d); d = mfma32(qa[2], k2, d); d = mfma32(qa[3], k3, d);
      *lp<float>(spos + j * 64) = d[0];
    }
#pragma nounroll
    for (u32 spin = 0; lds_poll(C_PRE) != 8u && spin < SPIN_CAP * 8; ++spin) { }
    CFENCE();
    if (a == 0) STAMP(3);
    float mw, lw;
    {
      f4 s[8];
      float mx = -INFINITY;
#pragma unroll
      for (int t = 0; t < 8; ++t) {
        s[t] = *lp<f4>(sc + lane * 16 + t * 1024);
        const u32 p0 = pk(s[t][0], s[t][1]), p1 = pk(s[t][2], s[t][3]);
        const u32 r0 = pk(blo(p0) * 0.08838834764831845f, bhi(p0) * 0.08838834764831845f), r1 = pk(blo(p1) * 0.08838834764831845f, bhi(p1) * 0.08838834764831845f);
        s[t] = f4{blo(r0), bhi(r0), blo(r1), bhi(r1)};
        mx = fmaxf(fmaxf(mx, fmaxf(s[t][0], s[t][1])), fmaxf(s[t][2], s[t][3]));
      }
      mw = wave_max(mx);
      float sum = 0.f;
#pragma unroll
      for (int t = 0; t < 8; ++t) {
        v4u pp;
#pragma unroll
        for (int e = 0; e < 4; ++e) { const float p = __builtin_amdgcn_exp2f((s[t][e] - mw) * LOG2E); sum += p; pp[e] = pk(p, p); }
        *lp<v4u>(sc + lane * 16 + t * 1024) = pp;
      }
      lw = wave_sum(sum);
    }
    const int mlbase = W_ML + (b * 32 + h) * 64;
    if (lane == 0) __builtin_amdgcn_raw_buffer_store_b128(v4u{fu(mw), tag, fu(lw), tag}, rs, mlbase + (half * 2 + a) * 16, 0, SC1);
    u32 msk[4];
#pragma unroll
    for (int q = 0; q < 4; ++q) msk[q] = ((n & 7) >> 1) == q ? ((n & 1) ? 0xffff0000u : 0x0000ffffu) : 0u;
    f4 acc = zero4;
    float scale = 0.f;
    u32 gap = 0, gbp = 0, xop = 0;
    const int ow = pid >> 1;
    const int oc = 64 * (ow >> 2) + 32 * (ow & 1) + 8 * (lane & 3) + 4 * (lane >> 5) + (ow & 2);
#pragma nounroll
    for (int j = 0; j < 126; ++j) {
      const int u = 256 + 2 * j + a;
      if (j == 108) {
        v4u ml;
        const int go = mod_off(b, oc);
#pragma nounroll
        for (u32 spin = 0; spin < SPIN_CAP; ++spin) {
          CFENCE();
          ml = __builtin_amdgcn_raw_buffer_load_b128(rs, mlbase + (lane & 3) * 16, 0, SC1);
          const v2u ga = __builtin_amdgcn_raw_buffer_load_b64(rs, go, 2 * 8192, SC1), gb = __builtin_amdgcn_raw_buffer_load_b64(rs, go, 5 * 8192, SC1);
          xop = ldnt<u32>(x + (b * 512 + oc) * 2);
          gap = ga.x; gbp = gb.x;
          if (__all(((ml.y ^ tag) | (ml.w ^ tag) | (ga.y ^ tag) | (gb.y ^ tag)) == 0)) break;
        }
        float M = uf(ml.x); M = fmaxf(M, dppf<XOR1>(M)); M = fmaxf(M, dppf<XOR2>(M));
        float L = uf(ml.z) * __builtin_amdgcn_exp2f((uf(ml.x) - M) * LOG2E);
        L += dppf<XOR1>(L); L += dppf<XOR2>(L);
        scale = __builtin_amdgcn_exp2f((mw - M) * LOG2E) / L;
        *(volatile LAS float*)(C_SCALE + 4 * a) = uf((u32)__builtin_amdgcn_readfirstlane((int)fu(scale)));
        lds_add(C_SCNT, 1u);
        asm volatile("" : "+v"(xop));
      }
      const v4u p4 = *lp<v4u>(sc + j * 64 + g * 16);
      v4u bf[4];
#pragma unroll
      for (int i = 0; i < 4; ++i) bf[i] = v4u{p4[i] & msk[0], p4[i] & msk[1], p4[i] & msk[2], p4[i] & msk[3]};
      asm volatile("" : "+v"(bf[0]), "+v"(bf[1]), "+v"(bf[2]), "+v"(bf[3]) :: "memory");
#pragma nounroll
      for (u32 spin = 0; landed <= u && spin < SPIN_CAP * 8; ++spin) landed = (int)lds_poll(C_LANDED);
      CFENCE();
      const u32 ad = ((u & 31) << 12) + lane * 16;
      v4u v0 = *lp<v4u>(ad), v1 = *lp<v4u>(ad + 1024), v2 = *lp<v4u>(ad + 2048), v3 = *lp<v4u>(ad + 3072);
      SBAR();
      acc = mfma32(v0, bf[0], acc); acc = mfma32(v1, bf[1], acc); acc = mfma32(v2, bf[2], acc);
      asm volatile("" : "+v"(v3) :: "memory");
      *(volatile LAS u32*)(C_CONS + 4 * a) = 128 + j + 1;
      acc = mfma32(v3, bf[3], acc);
    }
    WARM_AT(1, 2);
    *lp<u32>(L_OA + (a + 3 * (n >> 3)) * 160 + (g * 8 + (n & 7)) * 4) = pk((n & 8 ? acc[2] : acc[0]) * scale, (n & 8 ? acc[3] : acc[1]) * scale);
    if (a == 1) {
      asm volatile("s_waitcnt lgkmcnt(0)" ::: "memory");
      __builtin_amdgcn_s_barrier();
      int xg = late(sides, xcd, 16) + b * 2048 + lane * 16;
      u32 la = C_LN, ones = 0x3f803f80u;
      asm volatile("" : "+v"(xg), "+v"(la), "+v"(ones));
      __builtin_amdgcn_s_setprio(2);
      __builtin_amdgcn_s_barrier();
      WARM_AT(8, 9);
      v4u m0, m1;
#pragma nounroll
      for (u32 spin = 0; spin < SPIN_CAP; ++spin) {
        CFENCE();
        m0 = __builtin_amdgcn_raw_buffer_load_b128(rs, xg, 0, SC1);
        m1 = __builtin_amdgcn_raw_buffer_load_b128(rs, xg + 1024, 0, SC1);
        u32 bad = (m0.y ^ tag) | (m0.w ^ tag);
        asm("" : "+v"(bad));
        bad |= (m1.y ^ tag) | (m1.w ^ tag);
        asm("" : "+v"(bad));
        if (__builtin_amdgcn_ballot_w64(bad != 0) == 0) break;
      }
      WARM_AT(9, 10);
      const float s1 = dot2(m1.z, ones, dot2(m1.x, ones, dot2(m0.z, ones, dot2(m0.x, ones, 0.f))));
      const float s2 = dot2(m1.z, m1.z, dot2(m1.x, m1.x, dot2(m0.z, m0.z, dot2(m0.x, m0.x, 0.f))));
      float st = swap32_sum(s1, s2);
      st += dppf<XOR1>(st); st += dppf<XOR2>(st); st += dppf<HMIR>(st); st += dppf<ROR8>(st);
      st = swap16_sum(st, st) * (1.f / 512.f);
      WARM_AT(10, 11);
      const float mean = uf((u32)__builtin_amdgcn_readlane((int)fu(st), 0)), m2 = uf((u32)__builtin_amdgcn_readlane((int)fu(st), 32));
      float var = fmaf(-mean, mean, m2);
      if (__builtin_expect(__builtin_amdgcn_ballot_w64(!(var >= m2 * (1.f / 256.f))) != 0, 0)) {
        const u32 v[4] = {m0.x, m0.z, m1.x, m1.z};
        float q = 0.f;
#pragma unroll
        for (int e = 0; e < 4; ++e) { const float d0 = blo(v[e]) - mean, d1 = bhi(v[e]) - mean; q = fmaf(d0, d0, q); q = fmaf(d1, d1, q); }
        var = wave_sum(q) * (1.f / 512.f);
      }
      *lp<f2>(la) = f2{rbf(mean), __builtin_amdgcn_rcpf(rbf(__builtin_amdgcn_sqrtf(rbf(rbf(var) + 1e-5f))))};
      asm volatile("s_waitcnt lgkmcnt(0)" ::: "memory");
      __builtin_amdgcn_s_barrier();
      return;
    }
    __builtin_amdgcn_s_setprio(2);
    int io = late(sides, xcd, b * 4 + hh) + lane * 16, hg = late(sides, xcd, 17) + b * 512 + lane * 8;
    int xs = late(sides, (lane >> 2) & 7, 16) + b * 2048 + ow * 64 + (lane >> 5) * 32 + (lane & 3) * 8;
    float ga0 = blo(gap), ga1 = bhi(gap), xo0 = blo(xop), xo1 = bhi(xop);
    asm volatile("" : "+v"(io), "+v"(xs), "+v"(ga0), "+v"(ga1), "+v"(xo0), "+v"(xo1));
    u32 tl;
    {
      v2u hv;
      const v2u zero2 = {0u, 0u};
      if (EXPA) { asm volatile("s_waitcnt lgkmcnt(0)\n s_barrier" ::: "memory"); nap<EXPA>(); hv = v2u{0u, ~tag}; } else
      asm volatile("global_atomic_add_x2 %0, %1, %2, %3 sc0 sc1\n s_waitcnt lgkmcnt(0)\n s_barrier\n s_waitcnt vmcnt(0)"
                   : "=&v"(hv) : "v"(hg), "v"(zero2), "s"(ws) : "memory");
      STAMP(4);
      u64 miss = __builtin_amdgcn_ballot_w64(hv.y != tag);
#pragma nounroll
      for (u32 spin = 0; miss && spin < SPIN_CAP; ++spin) {
        asm volatile("s_mov_b64 exec, %4\n global_atomic_add_x2 %0, %1, %2, %3 sc0 sc1\n s_mov_b64 exec, -1\n s_waitcnt vmcnt(0)"
                     : "+v"(hv) : "v"(hg), "v"(zero2), "s"(ws), "s"(miss) : "memory");
        miss = __builtin_amdgcn_ballot_w64(hv.y != tag);
      }
      const u32 t0 = (u32)__builtin_amdgcn_readlane((int)hv.x, pid);
      int d = (int)(hv.x - t0);
      d = max(d, (int)dppu<XOR1>(d)); d = max(d, (int)dppu<XOR2>(d)); d = max(d, (int)dppu<HMIR>(d)); d = max(d, (int)dppu<ROR8>(d));
      tl = t0 + (u32)min(max(max(__builtin_amdgcn_readlane(d, 0), __builtin_amdgcn_readlane(d, 16)), max(__builtin_amdgcn_readlane(d, 32), __builtin_amdgcn_readlane(d, 48))), 2000);
    }
    STAMP(6);
    *(volatile LAS u64*)(L_STAMP + 30 * 8) = t_start + (u64)(tl - (u32)t_start);
    WARM_AT(2, 3);
    v4u m[4];
    u32 x1 = 0;
    u32 np = 0;
    if (!half) {
      wait_until(tl + T1);
#pragma nounroll
      for (u32 spin = 0; spin < SPIN_CAP; ++spin) {
        CFENCE();
#pragma unroll
        for (int i = 0; i < 4; ++i) m[i] = __builtin_amdgcn_raw_buffer_load_b128(rs, io + i * 1024, 0, SC1);
        WARM_AT(3, 4);
        ++np;
        if (tags_ok(m, tag)) break;
      }
      STAMP(15);
      WARM_AT(4, 5);
      float y0, y1;
      inbox_sum1(m, y0, y1);
      WARM_AT(5, 6);
      const u32 yb = inbox_sum2(y0, y1);
      WARM_AT(6, 7);
      const u32 t = pk(ga0 * blo(yb), ga1 * bhi(yb));
      x1 = pk(xo0 + blo(t), xo1 + bhi(t));
      __builtin_amdgcn_raw_buffer_store_b64(v2u{x1, tag}, rs, xs, 0, SC1);
      STAMP(16);
      *(volatile LAS u64*)(L_STAMP + 7 * 8) = *(volatile LAS u64*)L_STAMP + np * 100; np = 0;
    }
    WARM_AT(7, 8);
    wait_until(tl + T2);
    __builtin_amdgcn_s_barrier();
    if (half) {
      __builtin_amdgcn_s_barrier();
      nap<2500>();
      if (lane < 32) *(GAS u64*)(ws + W_DBG + (blockIdx.x * 32 + lane) * 8) = *(volatile LAS u64*)(L_STAMP + lane * 8);
      return;
    }
    float r0 = blo(x1), r1 = bhi(x1), gb0 = blo(gbp), gb1 = bhi(gbp);
    io += 4096;
    int io3 = lane >= 32 ? W_END : io + 3072;
    u32 tg3 = lane >= 32 ? 0u : tag, oa = (b * 512 + oc) * 2;
    asm volatile("" : "+v"(r0), "+v"(r1), "+v"(gb0), "+v"(gb1), "+v"(io), "+v"(io3), "+v"(tg3), "+v"(oa));
    __builtin_amdgcn_s_barrier();
    WARM_AT(11, 12);
    __builtin_amdgcn_s_sleep(D3 * 24 / 64);
    wait_until(tl + T3);
#pragma nounroll
    for (u32 spin = 0; spin < SPIN_CAP; ++spin) {
      CFENCE();
#pragma unroll
      for (int i = 0; i < 4; ++i) m[i] = __builtin_amdgcn_raw_buffer_load_b128(rs, i == 3 ? io3 : io + i * 1024, 0, SC1);
      WARM_AT(12, 13);
      ++np;
      if (tags_ok(m, tag, tg3)) break;
    }
    STAMP(19);
    WARM_AT(13, 14);
    float y0, y1;
    inbox_sum1(m, y0, y1);
    WARM_AT(14, 15);
    const u32 yb = inbox_sum2(y0, y1);
    WARM_AT(15, 16);
    const u32 t = pk(gb0 * blo(yb), gb1 * bhi(yb));
    if ((lane & 28) == 0) *(GAS u32*)(out + oa) = pk(r0 + blo(t), r1 + bhi(t));
    STAMP(20);
    *(volatile LAS u64*)(L_STAMP + 29 * 8) = *(volatile LAS u64*)L_STAMP + np * 100;
    if (lane < 32) *(GAS u64*)(ws + W_DBG + (blockIdx.x * 32 + lane) * 8) = *(volatile LAS u64*)(L_STAMP + lane * 8);
    return;
  }

  if (wave == 3) {
    u32 tref = (u32)__builtin_amdgcn_s_memrealtime() + T00;
    const u32 tag = (u32)__builtin_amdgcn_readfirstlane((int)__builtin_amdgcn_raw_buffer_load_b32(rs, W_EPOCH, 0, SC1)) + 1u;
    *(volatile LAS v2u*)C_TAG = v2u{tag, 1u};
    if (WARM) {
      u32 d0, d1, d2, d3, d4, d5, d6;
      asm volatile("s_load_dword %0, %7, 0\n s_load_dword %1, %8, 0\n s_load_dword %2, %9, 0\n s_load_dword %3, %10, 0\n s_load_dword %4, %11, 0\n s_load_dword %5, %12, 0\n"
                   "s_load_dword %6, %13, 0\n s_waitcnt lgkmcnt(0)"
                   : "=&s"(d0), "=&s"(d1), "=&s"(d2), "=&s"(d3), "=&s"(d4), "=&s"(d5), "=&s"(d6)
                   : "s"(vc + ((size_t)(b * 32 + h) << 21)), "s"(wo + (size_t)h * 131072), "s"(wg), "s"(wu), "s"(wd), "s"(out), "s"(ws + W_END - 64));
    }
    asm volatile("s_mov_b64 exec, 0\n s_branch .Lldr_copies\n .Lwarm_ret0:\n s_mov_b64 exec, -1" ::: "memory");
    if (WARM) asm volatile("s_mov_b64 exec, 0\n s_branch .Lwarm_1\n .Lwarm_ret1:\n s_mov_b64 exec, -1" ::: "memory");
    else asm volatile(".Lwarm_ret1:");
    int uref = 0, tau = TAU * 64 / 100, kp = 1, kr = 1;
    u32 lsum = 0, lcnt = 0, st = 0;
#pragma nounroll
    for (u32 spin = 0; spin < SPIN_CAP * 8; ++spin) {
      const v3u cc = *(volatile LAS v3u*)C_CONS;
      const int u = (int)lds_poll(C_LANDED);
      const u32 now = (u32)__builtin_amdgcn_s_memrealtime();
      const u32 c0 = __builtin_amdgcn_readfirstlane(cc.x), c1 = __builtin_amdgcn_readfirstlane(cc.y), pl = __builtin_amdgcn_readfirstlane(cc.z) ^ (0x88888888u >> (32 - 4 * NPRE));
      const u32 done = pl ? (__builtin_ctz(pl) & 28) * 2 : max(8u * NPRE, 2 * min(c0, c1));
      const int lag = (int)(now - tref) - (((u - uref) * tau) >> 6);
      const int lvl = min(max(LVL0 + ((lag * GAIN + 128) >> 8), (CLOSED && uref) ? 0 : LVL0), 7);
      *(volatile LAS u32*)C_CTL = ((done + 32) << 12) | (lvl << 7);
      lsum += lvl; ++lcnt;
      if (u >= 32 && st == 0) { STAMP(25); st = 1; }
      if (u >= 80 && st == 1) { STAMP(23); st = 2; }
      if (u >= 128 && st == 2) { STAMP(26); st = 3; }
      if (u >= 256 && st == 3) { STAMP(28); st = 4; }
      if (u >= 384 && st == 4) { STAMP(27); st = 5; }
      if (u >= 490) { *(volatile LAS u64*)(L_STAMP + 21 * 8) = t_start + lsum * 100 / lcnt; *(volatile LAS u64*)(L_STAMP + 22 * 8) = t_start + lvl * 100; }
      if (u >= 490 && done >= 476) break;
      if (CLOSED) {
        if (u >= 64 * kp) {
          if ((idx & 3) == 0 && lane == 0) __builtin_amdgcn_raw_buffer_store_b64(v2u{now, tag}, rs, W_PROG + kp * 512 + ((idx >> 2) * 8 + xcd) * 8, 0, SC1);
          ++kp;
        }
        if (u >= 64 * kr + RDLAG) {
          const v2u pr = __builtin_amdgcn_raw_buffer_load_b64(rs, W_PROG + kr * 512 + lane * 8, 0, SC1);
          const float ago = wave_sum(pr.y == tag ? (float)(int)(now - pr.x) : 0.f);
          const u32 tk = now - (u32)__builtin_amdgcn_readfirstlane((int)(ago * (1.f / 64.f)));
          if (uref) tau = min(max((int)(tk - tref), 800), 1000);
          tref = tk; uref = 64 * kr; ++kr;
        }
      }
      __builtin_amdgcn_s_sleep(3);
    }
    CFENCE();
    u32 wa = L_OA + (2 + 3 * (n >> 3)) * 160 + (g * 8 + (n & 7)) * 4;
    v4u bf[16];
#pragma unroll
    for (int r = 0; r < 4; ++r) {
      const v4u p4 = *lp<v4u>(L_SC + (r & 1) * 8192 + (126 + (r >> 1)) * 64 + g * 16);
#pragma unroll
      for (int i = 0; i < 4; ++i)
#pragma unroll
        for (int q = 0; q < 4; ++q) bf[4 * r + i][q] = p4[i] & (((n & 7) >> 1) == q ? ((n & 1) ? 0xffff0000u : 0x0000ffffu) : 0u);
      PIN4(bf + 4 * r);
    }
#pragma nounroll
    for (u32 spin = 0; lds_poll(C_SCNT) != 2u && spin < SPIN_CAP * 8; ++spin) { }
    CFENCE();
    f2 sc2 = *lp<f2>(C_SCALE);
    const __amdgpu_buffer_rsrc_t rv = __builtin_amdgcn_make_buffer_rsrc((void*)(vc + ((size_t)(b * 32 + h) << 21) + (31 << 16) + half * 32768 + 16384), 0, 16384, 0x00020000);
    asm volatile("" : "+v"(wa), "+v"(sc2));
#pragma nounroll
    for (u32 spin = 0; lds_poll(C_ISSUED) == 0u && spin < SPIN_CAP * 8; ++spin) { }
    CFENCE();
    v4u vv[16];
#pragma unroll
    for (int i = 0; i < 16; ++i) {
      if (LASTW < 9 && (i & 3) == 0) {
#pragma nounroll
        for (u32 spin = 0; (int)lds_poll(C_LANDED) < 509 - LASTW + (i >> 2) && spin < SPIN_CAP * 8; ++spin) { }
        CFENCE();
      }
      vv[i] = __builtin_amdgcn_raw_buffer_load_b128(rv, lane * 16 + (i & 1) * 1024 + ((i >> 2) & 1) * 2048, (i >> 3) * 4096 + ((i >> 1) & 1) * 8192, NT);
    }
    SBAR();
    f4 ac[2] = {zero4, zero4};
    float e0, e1;
#pragma unroll
    for (int i = 0; i < 16; ++i) {
      ac[(i >> 2) & 1] = mfma32(vv[i], bf[i], ac[(i >> 2) & 1]);
      SBAR();
      if (i == 11) { e0 = (n & 8 ? ac[0][2] : ac[0][0]) * sc2.x; e1 = (n & 8 ? ac[0][3] : ac[0][1]) * sc2.x; asm volatile("" : "+v"(e0), "+v"(e1)); }
    }
    STAMP(1);
    *lp<u32>(wa) = pk(fmaf(n & 8 ? ac[1][2] : ac[1][0], sc2.y, e0), fmaf(n & 8 ? ac[1][3] : ac[1][1], sc2.y, e1));
    asm volatile("s_waitcnt lgkmcnt(0)" ::: "memory");
    __builtin_amdgcn_s_barrier();
    return;
  }

  const int w = wave - 4;
  if (first_mod) mod_tile<false>(cnd, wmod, rs, b * 8 + xcd, w, lane);
  {
    v4u kr[NPRE][4];
    const u32 ad = w * 4096 + n * 256 + ((g ^ n) << 4);
#pragma unroll
    for (int r = 0; r < NPRE; ++r) {
#pragma nounroll
      for (u32 spin = 0; (int)lds_poll(C_LANDED) <= 8 * r + w && spin < SPIN_CAP * 8; ++spin) __builtin_amdgcn_s_sleep(8);
      CFENCE();
#pragma unroll
      for (int s = 0; s < 4; ++s) kr[r][s] = *lp<v4u>((ad ^ (s * 64)) + (r & 3) * 32768);
      asm volatile("" : "+v"(kr[r][0]), "+v"(kr[r][1]), "+v"(kr[r][2]), "+v"(kr[r][3]) :: "memory");
      lds_add(C_PULL, 1u << (4 * r));
    }
#pragma nounroll
    for (u32 spin = 0; lds_poll(C_QGOT) == 0u && spin < SPIN_CAP * 8; ++spin) __builtin_amdgcn_s_sleep(8);
    CFENCE();
    v4u qa[4];
#pragma unroll
    for (int s = 0; s < 4; ++s) qa[s] = *lp<v4u>(L_QV + 64 * s + 16 * g);
#pragma unroll
    for (int r = 0; r < NPRE; ++r) {
      f4 d = mfma32(qa[0], kr[r][0], zero4);
      d = mfma32(qa[1], kr[r][1], d); d = mfma32(qa[2], kr[r][2], d); d = mfma32(qa[3], kr[r][3], d);
      *lp<float>(L_SC + (w & 1) * 8192 + (4 * r + (w >> 1)) * 64 + ((n & 3) * 4 + (n >> 2)) * 4) = d[0];
    }
    lds_add(C_PRE, 1u);
  }
  const u32 tag = get_tag();
  const int fs = 7 * xcd + (pid & 7);
  v8u fo[2][4], fg[4];
  v4u fd[4];
  {
    const __amdgpu_buffer_rsrc_t ro = __builtin_amdgcn_make_buffer_rsrc((void*)(wo + (size_t)h * 131072), 0, (NOW & 1) ? 0 : 131072, 0x00020000);
    const int vo = 2 * g * 1024 + (64 * w + 4 * n) * 2;
#pragma unroll
    for (int s = 0; s < 4; ++s) {
      v2u r[8];
      wait_pos(WSTART + s * WSTEP);
#pragma unroll
      for (int j = 0; j < 8; ++j) { r[j] = __builtin_amdgcn_raw_buffer_load_b64(ro, vo + (j >> 2) * 1024, (32 * s + 8 * (j & 3)) * 1024, NT); PACE(j); }
      if (s & 1) tr8x4<1>(r, fo[s >> 1]); else tr8x4<0>(r, fo[s >> 1]);
      PIN4(fo[s >> 1]);
      SBAR();
    }
  }
  if (has_ffn) {
    const char* pg = (n < 8 ? wg : wu) + (fs * 32 + 4 * (n & 7)) * 2;
#pragma unroll
    for (int s = 0; s < 2; ++s) {
      v2u r[8];
      const char* pr = pg + (size_t)(64 * w + 32 * (g >> 1) + 4 * (g & 1) + 2 * s) * (FF * 2);
      wait_pos(WSTART + (4 + s) * WSTEP);
#pragma unroll
      for (int j = 0; j < 8; ++j) { r[j] = (NOW & 2) ? v2u{0u, 0u} : ldnt<v2u>(pr + (8 * (j >> 1) + (j & 1)) * (FF * 2)); PACE(j); }
      if (s) tr8x4<1>(r, fg); else tr8x4<0>(r, fg);
      PIN4(fg);
      SBAR();
    }
  } else {
    mod_tile<true>(cnd, wmod, rs, 16 + b * 8 + xcd, w, lane);
#pragma unroll
    for (int t = 0; t < 4; ++t) fg[t] = v8u{};
  }
  wait_pos(WSTART + 6 * WSTEP);
  {
    const __amdgpu_buffer_rsrc_t rd = __builtin_amdgcn_make_buffer_rsrc((void*)(wd + (size_t)fs * 32768), 0, ((NOW & 2) || !has_ffn) ? 0 : 32768, 0x00020000);
    const int vd = 8 * g * 1024 + (128 * (w & 3) + 64 * (w >> 2) + 4 * n) * 2;
    v2u r[8];
#pragma unroll
    for (int j = 0; j < 8; ++j) { r[j] = __builtin_amdgcn_raw_buffer_load_b64(rd, vd + (j & 3) * 1024, (j >> 2) * 4096, NT); PACE(j); }
    tr8x4(r, fd);
    PIN4(fd);
    SBAR();
  }
  const bool lo_part = n & 4;
  const int xg = 8 * g + 2 * (n & 3) + (n >> 3);
  const u32 gidx = n & 8 ? 0xeeeeu : 0x4444u;
  float cb0, cb1, sh;
  {
    v2u sv, cv; v4u xr;
    const int so = mod_off(b, 64 * w + 32 * ((xg >> 3) & 1) + 4 * ((xg >> 2) & 1) + 8 * (xg & 3) + 2 * (xg >> 4));
#pragma nounroll
    for (u32 spin = 0; spin < SPIN_CAP; ++spin) {
      CFENCE();
      sv = __builtin_amdgcn_raw_buffer_load_b64(rs, so, 3 * 8192, SC1);
      cv = __builtin_amdgcn_raw_buffer_load_b64(rs, so, 4 * 8192, SC1);
      xr = ldnt<v4u>(x + (b * 512 + 8 * lane) * 2);
      if (__all(((sv.y ^ tag) | (cv.y ^ tag)) == 0)) break;
      __builtin_amdgcn_s_sleep(64);
    }
    const u32 c1p = pk(1.f + blo(cv.x), 1.f + bhi(cv.x));
    cb0 = blo(c1p); cb1 = bhi(c1p);
    sh = rbf(wave_sum((blo(xr.x) + bhi(xr.x)) + (blo(xr.y) + bhi(xr.y)) + (blo(xr.z) + bhi(xr.z)) + (blo(xr.w) + bhi(xr.w))) * (1.f / 512.f));
    if (has_ffn) {
      *lp<float>(L_C12 + w * 256 + lane * 4) = gu_dot(lo_part ? 0u : c1p, gidx, fg);
      SBAR();
      *lp<float>(L_C12 + 2048 + w * 256 + lane * 4) = gu_dot(lo_part ? 0u : sv.x, gidx, fg);
    }
    asm volatile("" : "+v"(cb0), "+v"(cb1), "+v"(sh));
  }
  if (blockIdx.x == 0 && w == 7) {
#pragma nounroll
    for (u32 spin = 0; spin < SPIN_CAP; ++spin) {
      CFENCE();
      u32 bad = 0;
#pragma nounroll
      for (int i = 0; i < 8; ++i) {
        const v4u t = __builtin_amdgcn_raw_buffer_load_b128(rs, W_ML + lane * 16, i * 1024, SC1);
        bad |= (t.y ^ tag) | (t.w ^ tag);
      }
      if (__all(bad == 0)) break;
      __builtin_amdgcn_s_sleep(64);
    }
    if (lane == 0) __builtin_amdgcn_raw_buffer_store_b32(tag, rs, W_EPOCH, 0, SC1);
  }
  if (w == 0) STAMP(13);
  int mo = late(sides, w, b * 4 + ((lane >> 3) & 3)) + pid * 64 + (lane & 7) * 8;
  const int oi = 32 * (n >> 3) + 8 * g + (n & 7);
  u32 ra = L_C12 + ((oi & 3) * 16 + (oi >> 2)) * 4, oa = L_OA + (min(n & 3, 2) + 3 * ((n >> 2) & 1)) * 160 + g * 16;
  u32 oidx = n & 4 ? 0xeeeeu : 0x4444u;
  f4 d[4] = {};
  if (w == 6) {
    int ha = late(sides, lane & 7, 17) + b * 512 + pid * 8;
    asm volatile("" : "+v"(ha));
    wait_pos(500 - HINTK);
#pragma nounroll
    for (u32 spin = 0; (int)lds_poll(C_LANDED) < 512 - HINTK && spin < SPIN_CAP * 8; ++spin) { }
    nap<EXPC>();
    if (lane < 8) __builtin_amdgcn_raw_buffer_store_b64(v2u{now32(), tag}, rs, ha, 0, SC1);
  }
  asm volatile("s_waitcnt lgkmcnt(0)" : "+v"(ra), "+v"(oa), "+v"(mo), "+v"(oidx), "+v"(d[0]), "+v"(d[1]), "+v"(d[2]), "+v"(d[3]) :: "memory");
  __builtin_amdgcn_s_barrier();
  CYC(0);
  WARM_AT(16, 17);
  CFENCE();
  {
    const v4u a0 = *lp<v4u>(oa), a1 = *lp<v4u>(oa + 64);
    SBAR();
#pragma unroll
    for (int t = 0; t < 4; ++t) d[t] = smfmac(a0, fo[0][t], d[t], oidx);
#pragma unroll
    for (int t = 0; t < 4; ++t) d[t] = smfmac(a1, fo[1][t], d[t], oidx);
    WARM_AT(17, 18);
    const float y0 = swap16_sum((d[0][0] + d[0][1]) + d[0][2], (d[2][0] + d[2][1]) + d[2][2]), y1 = swap16_sum((d[1][0] + d[1][1]) + d[1][2], (d[3][0] + d[3][1]) + d[3][2]);
    nap<EXPB>();
    if (lane < 32) __builtin_amdgcn_raw_buffer_store_b64(v2u{pk(y0, y1), tag}, rs, mo, 0, SC1);
  }
  CYC(1);
  if (w == 0 && STAMPS == 1) STAMP(14);
  WARM_AT(18, 19);
  float c1 = sum8(ra), c2 = sum8(ra + 2048);
#pragma unroll
  for (int t = 0; t < 4; ++t) d[t] = zero4;
  u32 gi = gidx;
  u32 wa = L_GURED + w * 256 + lane * 4, la = C_LN;
  int xa = w * 256 + xg * 8 + late(sides, xcd, 16) + b * 2048;
  mo = has_ffn ? late(sides, (2 * w + (lane >> 5)) & 7, b * 4 + ((lane >> 3) & 3)) + 4096 + fs * 64 + (lane & 7) * 8 : W_END;
  asm volatile("" : "+v"(c1), "+v"(c2), "+v"(d[0]), "+v"(d[1]), "+v"(d[2]), "+v"(d[3]), "+v"(gi), "+v"(wa), "+v"(la), "+v"(xa), "+v"(mo));
#pragma unroll
  for (int t = 0; t < 4; ++t) *lp<v4u>(w * 4096 + t * 1024 + lane * 16) = fd[t];
  u32 fa = (w | 4) * 4096 + lane * 16;
  asm volatile("s_waitcnt lgkmcnt(0)" : "+v"(fa) :: "memory");
  WARM_AT(19, 20);
  __builtin_amdgcn_s_barrier();
  CYC(2);
  v4u fe[4];
  v2u m;
  u32 np = 0;
#pragma nounroll
  for (u32 spin = 0; spin < SPIN_CAP; ++spin) {
    CFENCE();
    m = __builtin_amdgcn_raw_buffer_load_b64(rs, xa, 0, SC1);
    SBAR();
#pragma unroll
    for (int t = 0; t < 4; ++t) fe[t] = *lp<v4u>(fa + t * 1024);
    PIN4(fe);
    ++np;
    if (__all(m.y == tag)) break;
  }
  CYC(3);
  if (w == 0 && STAMPS == 1) STAMP(17);
  WARM_AT(20, 21);
  {
    const float a0 = (blo(m.x) - sh) * cb0, a1 = (bhi(m.x) - sh) * cb1;
    const u32 hi = pk(a0, a1), lo = pk(a0 - blo(hi), a1 - bhi(hi));
    const u32 p = lo_part ? lo : hi;
    WARM_AT(21, 22);
    gu_mfma(p, gi, fg, d);
    CYC(4);
    WARM_AT(22, 23);
    *lp<float>(wa) = gu_fold(d);
  }
  CYC(5);
  asm volatile("s_waitcnt lgkmcnt(0)" ::: "memory");
  __builtin_amdgcn_s_barrier();
  if (w >= 4) return;
  CYC(6);
  CFENCE();
  {
    const f2 ln = *lp<f2>(la);
    const float gsum = sum8(ra + (L_GURED - L_C12));
    WARM_AT(23, 24);
    const float s = rbf(fmaf(ln.y, fmaf(sh - ln.x, c1, gsum), c2));
    CYC(7);
    const float up = dppf<ROR8>(s);
    const float z = s * fmaf(s * s, 0.044715f * 0.7978845608028654f * 2.f * LOG2E, 0.7978845608028654f * 2.f * LOG2E);
    const float ge = rbf(s * __builtin_amdgcn_rcpf(1.f + __builtin_amdgcn_exp2f(-z)));
    const float act = ge * up;
    WARM_AT(24, 25);
    const u32 ap = pk(act, dppf<XOR1>(act));
    const v4u aa = {dppu<0x150>(ap), dppu<0x152>(ap), dppu<0x154>(ap), dppu<0x156>(ap)};
    float y[8];
#pragma unroll
    for (int t = 0; t < 8; ++t) y[t] = mfma32(aa, t < 4 ? fd[t & 3] : fe[t & 3], zero4)[0];
    CYC(8);
    WARM_AT(25, ret1);
    const u32 p0 = pk(y[0], y[1]), p1 = pk(y[2], y[3]), p2 = pk(y[4], y[5]), p3 = pk(y[6], y[7]);
    const u32 p01 = (g & 1) ? p1 : p0, p23 = (g & 1) ? p3 : p2;
    __builtin_amdgcn_raw_buffer_store_b64(v2u{(g & 2) ? p23 : p01, tag}, rs, mo, 0, SC1);
  }
  CYC(9);
  if (w == 0) STAMP(18);
  if (w == 0) *(volatile LAS u64*)(L_STAMP + 8 * 8) = *(volatile LAS u64*)L_STAMP + np * 100;
}

}

namespace diag {
typedef unsigned u32; typedef unsigned long long u64;
__global__ void k_diag(const u64* h, int stat, int st) {
  if (threadIdx.x != 0) return;
  u64 t0 = ~0ull; for (int w = 0; w < 256; ++w) t0 = h[w * 32] < t0 ? h[w * 32] : t0;
  u64 mn = ~0ull, mx = 0, sum = 0; int n = 0;
  for (int w = 0; w < 256; ++w) { u64 v = h[w * 32 + st]; if (v < t0 || v - t0 > 100000) continue; v -= t0; mn = v < mn ? v : mn; mx = v > mx ? v : mx; sum += v; ++n; }
  u64 val = stat == 0 ? mn : stat == 1 ? (n ? sum / n : 0) : stat == 2 ? mx : 0;
  if (stat == 3) { val = h[st] * 100; for (int w = 0; w < 256; ++w) if (h[w * 32 + st] != ((h[st] + w) & 7)) val += 1000; if (val > 50000) val = 50000; }
  const u64 end = t0 + 30000 + val * 40;
  while (__builtin_amdgcn_s_memrealtime() < end) __builtin_amdgcn_s_sleep(4);
}
}
extern "C" void kernel_launch(void* const* d_in, const int* in_sizes, int n_in, void* d_out, int out_size, void* d_ws, size_t ws_size, hipStream_t stream) {
  hipLaunchKernelGGL(blk::fused_block, dim3(256), dim3(blk::NTHR), 0, stream,
                     (const char*)d_in[0], (const char*)d_in[1], (const char*)d_in[2], (const char*)d_in[3], (const char*)d_in[4], (const char*)d_in[6],
                     (const char*)d_in[7], (const char*)d_in[8], (const char*)d_in[9], (const char*)d_in[10], (char*)d_out, (char*)d_ws);
  diag::k_diag<<<1, 64, 0, stream>>>((const diag::u64*)((char*)d_ws + blk::W_DBG), 1, 25);
}
```

```cpp
#define STAMPS 3
#include <hip/hip_runtime.h>
#pragma clang diagnostic ignored "-Wint-to-pointer-cast"

#define WLEVELS "13,14,15,16,17,18,19,20,21,22,23,24,25,26,28,30"
#define LVL0 7
#define U0 4
#define TAU 1330
#define T00 330
#define GAIN 8
#define CLOSED 1
#define RDLAG 24
#define WSTART 0
#define WSTEP 0
#define WARM 1
#define WBOOST 0
#define PREF 0
#define MDELAY 256
#define CODEPF 1
#define TLBW 1
#define PF4 1
#define EXP 0
#define FIXSPAN 0
#define OUTPRE 0
#define OUTSC 0
#define GAIN2 20
#define G2AT 448
#define LMIN 5
#define LMIN2 0
#define CEXIT 492
#define LASTG 1
#define FASTN 8
#define DEPTH 3
#define STAG 3
#define LAZY 8
#define D1 40
#define CK0 44
#define RDLAG2 14
#define D2 320
#define NPROBE 8
#define TM 12
#define NPRE 8
#define CMOD 1u
#define HSLEEP 16
#define QSLEEP 4
#define HTIME 550
#define QDELAY 1500
#define WQFLY 3
#define PACEN 1
#define NOW 0
#define T1 88
#define T2 (T1 + 59)
#define T3 (T2 + 100)
#define D3 30
#define HINTK 1
#define CW 0
#define LASTW 6
#define EXPA 0
#define EXPB 0
#define EXPC 0
#define NUMA -1

namespace blk {

typedef unsigned short u16;
typedef unsigned u32;
typedef unsigned long long u64;
typedef u32 v4u __attribute__((ext_vector_type(4)));
typedef u32 v8u __attribute__((ext_vector_type(8)));
typedef __bf16 bf16x16 __attribute__((ext_vector_type(16)));
typedef u32 v3u __attribute__((ext_vector_type(3)));
typedef u32 v2u __attribute__((ext_vector_type(2)));
typedef float f4 __attribute__((ext_vector_type(4)));
typedef float f2 __attribute__((ext_vector_type(2)));
typedef __bf16 bf8 __attribute__((ext_vector_type(8)));
typedef __bf16 bf2 __attribute__((ext_vector_type(2)));
typedef short s4 __attribute__((ext_vector_type(4)));
#define GAS __attribute__((address_space(1)))
#define LAS __attribute__((address_space(3)))

constexpr int NTHR = 768;
constexpr int FF = 1792;
constexpr int L_SC = 131072;
constexpr int L_MODRED = L_SC + 16384;
constexpr int L_GURED = L_MODRED + 8192;
constexpr int L_OA = L_GURED + 4096;
constexpr int L_C12 = L_MODRED;
constexpr int L_HA = L_GURED;
constexpr int L_QV = L_OA + 960;
constexpr int L_SINK = L_QV + 256;
constexpr int L_CTRL = L_SINK + 256;
constexpr int C_CONS = L_CTRL, C_PULL = L_CTRL + 8, C_MODCNT = L_CTRL + 12, C_PRE = L_CTRL + 16, C_QGOT = L_CTRL + 20, C_TAG = L_CTRL + 24;
constexpr int C_HGOT = L_CTRL + 36, C_ISSUED = L_CTRL + 40, C_SCNT = L_CTRL + 44, C_SCALE = L_CTRL + 48, C_LN = L_CTRL + 56, C_PF = L_CTRL + 64, C_TL = L_CTRL + 68;
constexpr int C_CTL = L_CTRL + 32;
constexpr int C_LANDED = L_CTRL + 80;
constexpr int L_STAMP = C_LANDED + 256;
constexpr int LDS_BYTES = L_STAMP + (STAMPS ? 256 : 0);
constexpr int W_EPOCH = 0, W_PROG = 4096, W_SIDE = 12288, W_ML = 16384, W_PROBE = 196608;
constexpr int W_MOD = 65536;
constexpr int W_Q = 131072;
constexpr int W_LATE = 262144, W_END = W_LATE + 72 * 16384, W_DBG = W_END;
__device__ __forceinline__ int late(u32 sides, int x, int blk) { return W_LATE + ((x & 3) * 18 + blk) * 16384 + (int)((((u32)x >> 2) ^ (sides >> (x & 3))) & 1u) * 8192; }
constexpr int SC1 = 16, NT = 2;
constexpr u32 SPIN_CAP = 1u << 18;

__device__ __forceinline__ float blo(u32 u) { return __builtin_bit_cast(float, u << 16); }
__device__ __forceinline__ float bhi(u32 u) { return __builtin_bit_cast(float, u & 0xffff0000u); }
__device__ __forceinline__ u32 pk(float a, float b) { f2 p = {a, b}; return __builtin_bit_cast(u32, __builtin_convertvector(p, bf2)); }
__device__ __forceinline__ float rbf(float a) { return blo(pk(a, a)); }
__device__ __forceinline__ u32 fu(float a) { return __builtin_bit_cast(u32, a); }
__device__ __forceinline__ float uf(u32 a) { return __builtin_bit_cast(float, a); }
__device__ __forceinline__ f4 mfma32(v4u a, v4u b, f4 c) { return __builtin_amdgcn_mfma_f32_16x16x32_bf16(__builtin_bit_cast(bf8, a), __builtin_bit_cast(bf8, b), c, 0, 0, 0); }
__device__ __forceinline__ f4 mfma16(v2u a, v2u b, f4 c) { return __builtin_amdgcn_mfma_f32_16x16x16bf16_1k(__builtin_bit_cast(s4, a), __builtin_bit_cast(s4, b), c, 0, 0, 0); }
__device__ __forceinline__ float dot2(u32 a, u32 b, float c) { return __builtin_amdgcn_fdot2_f32_bf16(__builtin_bit_cast(bf2, a), __builtin_bit_cast(bf2, b), c, false); }
__device__ __forceinline__ f4 smfmac(v4u a, v8u b, f4 c, u32 idx) { return __builtin_amdgcn_smfmac_f32_16x16x64_bf16(__builtin_bit_cast(bf8, a), __builtin_bit_cast(bf16x16, b), c, (int)idx, 0, 0); }
template <int CTRL> __device__ __forceinline__ u32 dppu(u32 x) { return (u32)__builtin_amdgcn_mov_dpp((int)x, CTRL, 0xf, 0xf, true); }
template <int CTRL> __device__ __forceinline__ float dppf(float x) { return uf(dppu<CTRL>(fu(x))); }
constexpr int XOR1 = 0xB1, XOR2 = 0x4E, HMIR = 0x141, ROR4 = 0x124, ROR8 = 0x128;
__device__ __forceinline__ float xrow_sum(float x) {
  auto s = __builtin_amdgcn_permlane16_swap(fu(x), fu(x), false, false);
  x = uf(s[0]) + uf(s[1]);
  auto t = __builtin_amdgcn_permlane32_swap(fu(x), fu(x), false, false);
  return uf(t[0]) + uf(t[1]);
}
__device__ __forceinline__ float xrow_max(float x) {
  auto s = __builtin_amdgcn_permlane16_swap(fu(x), fu(x), false, false);
  x = fmaxf(uf(s[0]), uf(s[1]));
  auto t = __builtin_amdgcn_permlane32_swap(fu(x), fu(x), false, false);
  return fmaxf(uf(t[0]), uf(t[1]));
}
__device__ __forceinline__ float wave_sum(float x) { x += dppf<XOR1>(x); x += dppf<XOR2>(x); x += dppf<HMIR>(x); x += dppf<ROR8>(x); return xrow_sum(x); }
__device__ __forceinline__ float wave_max(float x) { x = fmaxf(x, dppf<XOR1>(x)); x = fmaxf(x, dppf<XOR2>(x)); x = fmaxf(x, dppf<HMIR>(x)); x = fmaxf(x, dppf<ROR8>(x)); return xrow_max(x); }
__device__ __forceinline__ u32 perm_lo(u32 r0, u32 r1) { return __builtin_amdgcn_perm(r1, r0, 0x05040100u); }
__device__ __forceinline__ u32 perm_hi(u32 r0, u32 r1) { return __builtin_amdgcn_perm(r1, r0, 0x07060302u); }
#define CFENCE() asm volatile("" ::: "memory")
#define SBAR() __builtin_amdgcn_sched_barrier(0)
#define PIN4(a) asm volatile("" : "+v"((a)[0]), "+v"((a)[1]), "+v"((a)[2]), "+v"((a)[3]))
template <typename T> __device__ __forceinline__ LAS T* lp(u32 off) { return (LAS T*)off; }
__device__ __forceinline__ u32 lds_poll(u32 off) { return (u32)__builtin_amdgcn_readfirstlane((int)*(volatile LAS u32*)off); }
__device__ __forceinline__ void lds_add(u32 off, u32 v) {
  asm volatile("s_mov_b64 exec, 1\n ds_add_u32 %0, %1\n s_mov_b64 exec, -1" :: "v"(off), "v"(v) : "memory");
}
template <typename T> __device__ __forceinline__ T ldnt(const void* p) { return __builtin_nontemporal_load((const GAS T*)p); }
template <typename T> __device__ __forceinline__ T ldg(const void* p) { return *(const GAS T*)p; }
__device__ __forceinline__ void tr8x4(const v2u (&r)[8], v4u (&f)[4]) {
#pragma unroll
  for (int q = 0; q < 4; ++q) {
    f[0][q] = perm_lo(r[2 * q].x, r[2 * q + 1].x); f[1][q] = perm_hi(r[2 * q].x, r[2 * q + 1].x);
    f[2][q] = perm_lo(r[2 * q].y, r[2 * q + 1].y); f[3][q] = perm_hi(r[2 * q].y, r[2 * q + 1].y);
  }
}
template <int HALF> __device__ __forceinline__ void tr8x4(const v2u (&r)[8], v8u (&f)[4]) {
#pragma unroll
  for (int q = 0; q < 4; ++q) {
    f[0][4 * HALF + q] = perm_lo(r[2 * q].x, r[2 * q + 1].x); f[1][4 * HALF + q] = perm_hi(r[2 * q].x, r[2 * q + 1].x);
    f[2][4 * HALF + q] = perm_lo(r[2 * q].y, r[2 * q + 1].y); f[3][4 * HALF + q] = perm_hi(r[2 * q].y, r[2 * q + 1].y);
  }
}
#define WARM_AT(k, nxt) do { SBAR(); asm volatile(".Lwarm_" #k ":\n s_cbranch_execz .Lwarm_" #nxt); SBAR(); } while (0)

#define CYC(i) do { } while (0)
#define STAMP(i) do { } while (0)

__device__ __forceinline__ int mod_off(int b, int c) { return W_MOD + (c >> 6) * 1024 + b * 256 + ((c & 63) >> 1) * 8; }
__device__ __forceinline__ float half_of(u32 v, int odd) { return odd ? bhi(v) : blo(v); }
__device__ __forceinline__ void delay(int cyc) {
  const u64 t0 = __builtin_amdgcn_s_memtime();
#pragma nounroll
  while ((int)(__builtin_amdgcn_s_memtime() - t0) < cyc) { }
}
template <int CYC> __device__ __forceinline__ void nap() {
#pragma unroll
  for (int i = 0; i < CYC / 8128; ++i) __builtin_amdgcn_s_sleep(127);
  if (CYC % 8128 >= 64) __builtin_amdgcn_s_sleep((CYC % 8128) / 64);
}
__device__ __forceinline__ u32 get_tag() {
  v2u t;
#pragma nounroll
  for (u32 spin = 0; spin < SPIN_CAP * 8; ++spin) { t = *(volatile LAS v2u*)C_TAG; if (__builtin_amdgcn_readfirstlane((int)t.y)) break; }
  return (u32)__builtin_amdgcn_readfirstlane((int)t.x);
}
__device__ __forceinline__ u32 now32() { return (u32)__builtin_amdgcn_s_memrealtime(); }
__device__ __forceinline__ void wait_until(u32 t) {
#pragma nounroll
  while ((int)(now32() - t) < 0) { }
}
__device__ __forceinline__ float swap32_sum(float a, float b) {
  auto t = __builtin_amdgcn_permlane32_swap(fu(a), fu(b), false, false);
  return uf(t[0]) + uf(t[1]);
}
__device__ __forceinline__ float swap16_sum(float a, float b) {
  auto t = __builtin_amdgcn_permlane16_swap(fu(a), fu(b), false, false);
  return uf(t[0]) + uf(t[1]);
}
__device__ __forceinline__ void inbox_sum1(const v4u (&m)[4], float& y0, float& y1) {
  const float a0 = (blo(m[0].x) + blo(m[1].x)) + (blo(m[2].x) + blo(m[3].x)), a1 = (bhi(m[0].x) + bhi(m[1].x)) + (bhi(m[2].x) + bhi(m[3].x));
  const float b0 = (blo(m[0].z) + blo(m[1].z)) + (blo(m[2].z) + blo(m[3].z)), b1 = (bhi(m[0].z) + bhi(m[1].z)) + (bhi(m[2].z) + bhi(m[3].z));
  y0 = swap32_sum(a0, b0); y1 = swap32_sum(a1, b1);
}
__device__ __forceinline__ u32 inbox_sum2(float y0, float y1) {
  float y = swap16_sum(y0, y1);
  y += dppf<ROR4>(y); y += dppf<ROR8>(y);
  auto t = __builtin_amdgcn_permlane16_swap(fu(y), fu(y), false, false);
  return pk(uf(t[0]), uf(t[1]));
}
__device__ __forceinline__ void load4(v4u (&m)[4], __amdgpu_buffer_rsrc_t rs, int o, int o3) {
  __builtin_assume(o >= 0 && o < (1 << 30));
#pragma unroll
  for (int i = 0; i < 4; ++i) m[i] = __builtin_amdgcn_raw_buffer_load_b128(rs, i == 3 ? o3 : o + i * 1024, 0, SC1);
}
__device__ __forceinline__ bool tags_ok(const v4u (&m)[4], u32 tag, u32 tag3) {
  u32 bad = ((m[0].y ^ tag) | (m[0].w ^ tag)) | ((m[1].y ^ tag) | (m[1].w ^ tag));
  asm("" : "+v"(bad));
  bad |= (m[2].y ^ tag) | (m[2].w ^ tag);
  asm("" : "+v"(bad));
  bad |= (m[3].y ^ tag3) | (m[3].w ^ tag3);
  asm("" : "+v"(bad));
  return __builtin_amdgcn_ballot_w64(bad != 0) == 0;
}
__device__ __forceinline__ bool tags_ok(v4u m0, v4u m1, u32 tag) {
  u32 bad = (m0.y ^ tag) | (m0.w ^ tag);
  asm("" : "+v"(bad));
  bad |= (m1.y ^ tag) | (m1.w ^ tag);
  asm("" : "+v"(bad));
  return __builtin_amdgcn_ballot_w64(bad != 0) == 0;
}
__device__ __forceinline__ bool tags_ok(const v4u (&m)[4], u32 tag) { return tags_ok(m, tag, tag); }
__device__ __forceinline__ void gu_mfma(u32 p, u32 idx, const v8u (&f)[4], f4 (&d)[4]) {
  const v4u a = {dppu<0x00>(p), dppu<0x55>(p), dppu<0xAA>(p), dppu<0xFF>(p)};
#pragma unroll
  for (int t = 0; t < 4; ++t) d[t] = smfmac(a, f[t], d[t], idx);
}
__device__ __forceinline__ float gu_fold(const f4 (&d)[4]) { return swap16_sum(swap32_sum(d[0][0], d[2][0]), swap32_sum(d[1][0], d[3][0])); }
__device__ __forceinline__ float gu_dot(u32 p, u32 idx, const v8u (&f)[4]) { f4 d[4] = {}; gu_mfma(p, idx, f, d); return gu_fold(d); }
__device__ __forceinline__ float sum8(u32 addr) {
  float p[8];
#pragma unroll
  for (int i = 0; i < 8; ++i) p[i] = *lp<float>(addr + i * 256);
  return ((p[0] + p[1]) + (p[2] + p[3])) + ((p[4] + p[5]) + (p[6] + p[7]));
}
__device__ __forceinline__ void wait_pos(int u) {
#pragma nounroll
  for (u32 spin = 0; (int)lds_poll(C_LANDED) < u && spin < SPIN_CAP * 8; ++spin) __builtin_amdgcn_s_sleep(4);
}
#define RETRY(ok, again, after) do { if (__builtin_expect(!(ok), 0)) { u32 spin_ = 0; _Pragma("nounroll") do { CFENCE(); again; NP_INC; } while (!(ok) && ++spin_ < SPIN_CAP); after; } } while (0)
#define NP_INC lds_add(L_STAMP + np * 8, 100u)
#define PACE(j) do { if (((j) % PACEN) == PACEN - 1) { asm volatile("s_waitcnt vmcnt(0)" ::: "memory"); SBAR(); } } while (0)
constexpr float LOG2E = 1.4426950408889634f;

template <bool PACED>
__device__ __forceinline__ void mod_tile(const char* cnd, const char* wmod, __amdgpu_buffer_rsrc_t rs, int cb, int w, int lane) {
  const int g = lane >> 4, n = lane & 15;
  const f4 zero4 = {0.f, 0.f, 0.f, 0.f};
  const v2u cv = ldnt<v2u>(cnd + ((n >> 2) * 512 + 64 * w + 16 * g + 4 * (n & 3)) * 2);
  const __amdgpu_buffer_rsrc_t rm = __builtin_amdgcn_make_buffer_rsrc((void*)(wmod + (size_t)(64 * w) * 6144 + cb * 128), 0, 64 * 6144, 0x00020000);
  v2u r[2][8];
  if (!PACED) {
#pragma nounroll
    for (u32 spin = 0; lds_poll(C_PF) == 0u && spin < SPIN_CAP * 8; ++spin) { }
    nap<MDELAY>();
    CFENCE();
  }
#pragma unroll
  for (int j = 0; j < 16; ++j) { r[j >> 3][j & 7] = __builtin_amdgcn_raw_buffer_load_b64(rm, 16 * g * 6144 + n * 8, j * 6144, NT); if (PACED) PACE(j); }
  SBAR();
  u32 sp[2];
#pragma unroll
  for (int e = 0; e < 2; ++e) {
    const float c0 = blo(cv[e]), c1 = bhi(cv[e]);
    sp[e] = pk(c0 * __builtin_amdgcn_rcpf(1.f + __builtin_amdgcn_exp2f(-c0 * LOG2E)), c1 * __builtin_amdgcn_rcpf(1.f + __builtin_amdgcn_exp2f(-c1 * LOG2E)));
  }
  const v4u a0 = {dppu<0x00>(sp[0]), dppu<0x00>(sp[1]), dppu<0x55>(sp[0]), dppu<0x55>(sp[1])};
  const v4u a1 = {dppu<0xAA>(sp[0]), dppu<0xAA>(sp[1]), dppu<0xFF>(sp[0]), dppu<0xFF>(sp[1])};
  v4u f0[4], f1[4];
  tr8x4(r[0], f0); tr8x4(r[1], f1);
  f4 pt;
#pragma unroll
  for (int t = 0; t < 4; ++t) pt[t] = mfma32(a1, f1[t], mfma32(a0, f0[t], zero4))[0];
  *lp<f4>(L_MODRED + w * 1024 + lane * 16) = pt;
  u32 old;
  asm volatile("s_mov_b64 exec, 1\n ds_add_rtn_u32 %0, %1, %2\n s_mov_b64 exec, -1\n s_waitcnt lgkmcnt(0)" : "=&v"(old) : "v"(C_MODCNT), "v"(1u) : "memory");
  if ((__builtin_amdgcn_readfirstlane((int)old) & 7) == 7) {
    f4 p[8];
#pragma unroll
    for (int ww = 0; ww < 8; ++ww) p[ww] = *lp<f4>(L_MODRED + ww * 1024 + lane * 16);
    const f4 v = ((p[0] + p[1]) + (p[2] + p[3])) + ((p[4] + p[5]) + (p[6] + p[7]));
    const u32 tag = get_tag();
    __builtin_amdgcn_raw_buffer_store_b128(v4u{pk(v[0], v[1]), tag, pk(v[2], v[3]), tag}, rs, lane * 16 + W_MOD + cb * 1024, 0, SC1);
    if (!PACED) STAMP(9);
  }
}

__global__ void __launch_bounds__(NTHR) fused_block(const char* x, const char* kc, const char* vc, const char* wq, const char* wo, const char* wg,
                                                    const char* wu, const char* wd, const char* cnd, const char* wmod, char* out, char* ws) {
  __shared__ __attribute__((aligned(1024))) char lds_mem[LDS_BYTES];
  const int lane = threadIdx.x & 63, wave = __builtin_amdgcn_readfirstlane(threadIdx.x >> 6);
  const int xcd = blockIdx.x & 7, idx = blockIdx.x >> 3;
  const int half = idx & 1, hh = (idx >> 1) & 3, b = idx >> 3;
  const int h = xcd + 8 * hh;
  const int pid = 8 * xcd + (idx & 7);
  const bool has_ffn = (idx & 7) < 7;
  const bool first_mod = !(EXP & 8) && (idx & 0x17) == 7;
  const int g = lane >> 4, n = lane & 15;
  const f4 zero4 = {0.f, 0.f, 0.f, 0.f};
  const u32 t_fix = FIXSPAN ? now32() : 0;
  const u64 t_start = __builtin_amdgcn_s_memrealtime();
#define PREFETCH(vo, base) asm volatile("s_mov_b32 m0, %2\n s_nop 0\n global_load_lds_dword %0, %1 nt\n global_load_lds_dword %0, %1 sc1" :: "v"(vo), "s"(base), "i"(L_SINK) : "memory")
  if (CODEPF) {
    u32 co = (idx * 8 + (lane & 7)) * 128, t0;
    asm volatile("s_cmp_lg_u32 %[wv], 3\n s_cbranch_scc1 1f\n s_getpc_b64 vcc\n .Lcode_base:\n"
                 "s_mov_b32 %[t0], .Lldr_exit-.Lcode_base\n s_max_u32 %[t0], %[t0], .Lwarm_15-.Lcode_base\n s_max_u32 %[t0], %[t0], .Lwarm_10-.Lcode_base\n"
                 "s_max_u32 %[t0], %[t0], .Lwarm_25-.Lcode_base\n s_max_u32 %[t0], %[t0], .Lwarm_26-.Lcode_base\n"
                 "v_min_u32 %[co], %[t0], %[co]\n s_mov_b32 m0, %[sink]\n s_nop 0\n global_load_lds_dword %[co], vcc nt\n global_load_lds_dword %[co], vcc sc1\n1:"
                 : [co] "+&v"(co), [t0] "=&s"(t0) : [wv] "s"(wave), [sink] "i"(L_SINK) : "memory", "scc", "vcc");
  }
  *lp<u32>(L_CTRL + 4 * min(threadIdx.x, STAMPS ? 147u : 63u)) = threadIdx.x == 8 ? (32u << 12) | (LVL0 * 128) : 0u;
  u32 vm = (16 * idx + n) * 6144 + g * 1024 + xcd * 128;
  u32 vq = (16 * idx + (lane >> 3)) * 8192 + (xcd + 8 * ((lane >> 1) & 3)) * 256 + (lane & 1) * 128;
  asm volatile("s_waitcnt lgkmcnt(0)\n s_barrier" : "+v"(vm), "+v"(vq) : "v"(lds_mem), "s"(ws), "s"(wq) : "memory");
  constexpr u32 HOLES = 0xA000u;
#define LANDED(cnt) asm volatile("s_waitcnt vmcnt(" #cnt ")" ::: "memory")
#define LD0(v, o) "buffer_load_dwordx4 %[" #v "], %[rs], %[src] offen offset:" #o " nt lds\n"
#define KV_RSRC(p) v4u{(u32)(u64)(p), (u32)((u64)(p) >> 32) & 0xffffu, (2u << 20) + 0x2000u, 0x00020000u}
  if (wave == 0) {
    __builtin_amdgcn_s_setprio(3);
    PREFETCH(vm, wmod); PREFETCH(vq, wq); PREFETCH(vq, wq + 8 * 8192);
    STAMP(5);
    u32 v1 = (xcd + 8 * (idx >> 3)) * 131072 + (idx & 7) * 16384 + lane * 128;
    if (PF4 && !first_mod) PREFETCH(v1, wo);
    u32 v2 = (16 * idx + (lane >> 2)) * (FF * 2) + ((448 * xcd) & ~127) + (lane & 3) * 128;
    u32 v3 = (224 * xcd + 7 * idx) * 1024 + min(lane, 55) * 128, v4 = vm + 4096 - (g & 2) * 1024;
    u32 o0 = g * 256 + ((n ^ g) << 4), o1 = o0 ^ 64, o2 = (o0 ^ 128) + 6144, o3 = (o0 ^ 192) + 6144;
    v4u rsrc = KV_RSRC(kc + ((size_t)(b * 32 + h) << 21) + half * 32768 - HOLES);
    asm volatile("" : "+v"(v1), "+v"(v2), "+v"(v3), "+v"(v4), "+v"(o0), "+v"(o1), "+v"(o2), "+v"(o3), "+s"(rsrc));
    if (first_mod) {
      LANDED(4);
      *(volatile LAS u32*)C_PF = 1u;
#pragma nounroll
      for (u32 spin = 0; lds_poll(C_MODCNT) < CMOD && spin < SPIN_CAP * 8; ++spin) { }
      if (PF4) PREFETCH(v1, wo);
    }
    if (PF4) {
      LANDED(6); STAMP(3);
      PREFETCH(v1, wo + 8192); LANDED(6);
      PREFETCH(v2, wg); LANDED(6);
      STAMP(23);
      *(volatile LAS u32*)C_PF = 2u;
      PREFETCH(v2, wu); LANDED(6);
    } else {
      LANDED(4); STAMP(3);
      PREFETCH(v1, wo); LANDED(4);
      PREFETCH(v1, wo + 8192); LANDED(4);
      STAMP(23);
      *(volatile LAS u32*)C_PF = 2u;
      PREFETCH(v2, wg); LANDED(4);
      PREFETCH(v2, wu); LANDED(4);
    }
    PREFETCH(v3, wd); PREFETCH(v4, wmod);
#define UNIT LD0(v0, 0) LD0(v1, 1024) LD0(v2, 2048) LD0(v3, 3072) "s_addk_i32 %[src], 0x800\n s_add_u32 m0, m0, 0x1000\n"
    u32 src = HOLES;
    asm volatile("s_mov_b32 m0, 0\n s_waitcnt vmcnt(6)\n" UNIT "s_waitcnt vmcnt(8)\n" UNIT "s_nop 0\n" UNIT "s_waitcnt vmcnt(14)\n" UNIT
                 : [src] "+&s"(src) : [v0] "v"(o0), [v1] "v"(o1), [v2] "v"(o2), [v3] "v"(o3), [rs] "s"(rsrc) : "memory", "scc");
    STAMP(31);
  }
  int role = wave;
  asm volatile("" : "+s"(role));

  if (role == 0) {
    const size_t reg_off = ((size_t)(b * 32 + h) << 21) + half * 32768 - HOLES;
    const u64 vb = (u64)(vc + reg_off);
    v4u rsrc = KV_RSRC(kc + reg_off);
    u32 o0 = g * 256 + ((n ^ g) << 4), o1 = o0 ^ 64, o2 = (o0 ^ 128) + 6144, o3 = (o0 ^ 192) + 6144;
    const u32 va = C_LANDED + lane * 4;
    u32 off = 0x4000u, src = HOLES | 0x4000u;
    u32 ctl = (32u << 12) | (LVL0 * 128);
#pragma nounroll
    for (u32 end = 1u << 20; end < (2u << 20); end += (1u << 20) - 0x4000u) {
      u32 cur = off, spin = SPIN_CAP * 8, blk, tmp, pcl, pch, pub;
#define LD(v, o) "s_waitcnt vmcnt(\\w)\n" LD0(v, o)
      asm volatile(
        "s_getpc_b64 vcc\n"
        ".Lldr_pc:\n s_add_u32 %[pcl], vcc_lo, .Lldr_copies-.Lldr_pc\n s_addc_u32 %[pch], vcc_hi, 0\n"
        ".Lldr_next:\n s_waitcnt lgkmcnt(4)\n"
        ".Lldr_dec:\n v_readfirstlane_b32 %[tmp], %[ctl]\n ds_read_b32 %[ctl], %[ca]\n s_add_u32 %[blk], %[off], 0x4000\n"
        "s_cmp_gt_u32 %[blk], %[tmp]\n s_cbranch_scc1 .Lldr_stall\n"
        "s_and_b32 %[tmp], %[tmp], 0x780\n s_add_u32 vcc_lo, %[pcl], %[tmp]\n s_addc_u32 vcc_hi, %[pch], 0\n s_lshr_b32 %[tmp], %[off], 12\n s_setpc_b64 vcc\n"
        ".Lldr_stall:\n s_waitcnt lgkmcnt(0)\n s_sub_u32 %[spin], %[spin], 1\n s_cbranch_scc0 .Lldr_dec\n s_branch .Lldr_exit\n"
        ".p2align 7\n.Lldr_copies:\n"
        ".irp w," WLEVELS "\n"
        "s_cbranch_execz 2f\n"
        "v_add_u32_e64 %[pub], %[tmp], -(\\w/4)\n"
        "1:\n s_and_b32 m0, %[off], 0x1ffff\n" LD(v0, 0) "s_addk_i32 %[off], 0x1000\n" LD(v1, 1024) LD(v2, 2048) LD(v3, 3072)
        "ds_write_b32 %[va], %[pub]\n s_addk_i32 %[src], 0x800\n s_or_b32 %[src], %[src], 0xa000\n v_add_u32 %[pub], 1, %[pub]\n"
        "s_cmp_lt_u32 %[off], %[blk]\n s_cbranch_scc1 1b\n s_cmp_lt_u32 %[off], %[end]\n s_cbranch_scc1 .Lldr_next\n s_branch .Lldr_exit\n"
        ".p2align 7\n2:\n.endr\n"
        "s_branch .Lwarm_ret0\n"
        ".Lldr_exit:\n s_waitcnt lgkmcnt(0)"
        : [off] "+&s"(cur), [src] "+&s"(src), [spin] "+&s"(spin), [blk] "=&s"(blk), [tmp] "=&s"(tmp), [pcl] "=&s"(pcl), [pch] "=&s"(pch), [pub] "=&v"(pub), [ctl] "+&v"(ctl)
        : [v0] "v"(o0), [v1] "v"(o1), [v2] "v"(o2), [v3] "v"(o3), [rs] "s"(rsrc), [end] "s"(end), [va] "v"(va), [ca] "v"(C_CTL) : "memory", "scc", "vcc");
      rsrc.x = (u32)vb; rsrc.y = (u32)(vb >> 32) & 0xffffu;
      o0 = o1 = lane * 16; o2 = o3 = lane * 16 + 6144;
      off = end; src = HOLES;
    }
    *(volatile LAS u32*)C_ISSUED = 1u;
#pragma unroll
    for (int k = 6; k >= 0; --k) {
      const u32 pb = 508 - k;
      asm volatile("s_waitcnt vmcnt(%2)\n ds_write_b32 %0, %1" :: "v"(va), "v"(pb), "i"(4 * k) : "memory");
    }
    *(volatile LAS u64*)L_STAMP = t_start;

    return;
  }

  const __amdgpu_buffer_rsrc_t rs = __builtin_amdgcn_make_buffer_rsrc((void*)ws, 0, W_END, 0x00020000);

  if (wave <= 2) {
    const int a = wave - 1;
    u32 tag;
    if (a == 0) {
#pragma nounroll
      for (u32 spin = 0; lds_poll(C_PF) < 2u && spin < SPIN_CAP * 8; ++spin) __builtin_amdgcn_s_sleep(1);
      const char* wt = wq + (h * 128 + (2 * b + half) * 16) * 2;
      const u32 vo = (64 * (lane >> 5) + 16 * ((lane >> 3) & 3) + ((lane >> 1) & 3)) * 8192 + (lane & 1) * 16;
#pragma nounroll
      for (int p = 0; p < ((EXP & 1) ? 0 : 16); ++p)
        asm volatile("s_mov_b32 m0, %2\n s_waitcnt vmcnt(%3)\n global_load_lds_dwordx4 %0, %1 nt"
                     :: "v"(vo), "s"(wt + (size_t)(128 * (p >> 2) + 4 * (p & 3)) * 8192), "s"(L_SC + p * 1024), "i"(WQFLY - 1) : "memory");
      asm volatile("s_waitcnt vmcnt(0)" ::: "memory");
      v4u fq[16];
#pragma unroll
      for (int p = 0; p < 16; ++p) {
        const v2u lo = __builtin_bit_cast(v2u, __builtin_amdgcn_ds_read_tr16_b64_v4i16(lp<s4>(L_SC + p * 1024 + lane * 8)));
        const v2u hi = __builtin_bit_cast(v2u, __builtin_amdgcn_ds_read_tr16_b64_v4i16(lp<s4>(L_SC + p * 1024 + 512 + lane * 8)));
        fq[p] = v4u{lo.x, lo.y, hi.x, hi.y};
      }
      PIN4(fq); PIN4(fq + 4); PIN4(fq + 8); PIN4(fq + 12);
      STAMP(10);
      tag = get_tag();
#pragma nounroll
      for (u32 spin = 0; lds_poll(C_HGOT) == 0u && spin < SPIN_CAP * 8; ++spin) __builtin_amdgcn_s_sleep(1);
      CFENCE();
      f4 acc[4] = {zero4, zero4, zero4, zero4};
#pragma unroll
      for (int sg = 0; sg < 4; ++sg) {
        const v4u ha = *lp<v4u>(L_HA + sg * 1024 + lane * 16);
#pragma unroll
        for (int e = 0; e < 4; ++e) acc[e] = mfma32(ha, fq[4 * sg + e], acc[e]);
      }
      const float qv = (acc[0][0] + acc[1][1]) + (acc[2][2] + acc[3][3]);
      const u32 pr = pk(qv, dppf<XOR1>(qv));
      if (!(EXP & 4) && !(n & 1)) __builtin_amdgcn_raw_buffer_store_b64(v2u{pr, tag}, rs, W_Q + (h * 4 + g) * 512 + (2 * b + half) * 64 + (n >> 1) * 8, 0, SC1);
      STAMP(12);
      nap<QDELAY>();
      v2u m = {pr, 0u};
#pragma nounroll
      for (u32 spin = 0; spin < ((EXP & 4) ? 0 : SPIN_CAP); ++spin) {
        CFENCE();
        m = __builtin_amdgcn_raw_buffer_load_b64(rs, lane * 8, W_Q + (h * 4 + b) * 512, SC1);
        if (__all(m.y == tag)) break;
        __builtin_amdgcn_s_sleep(QSLEEP);
      }
      *lp<u32>(L_QV + lane * 4) = m.x;
      CFENCE();
      *(volatile LAS u32*)C_QGOT = 1u;
    } else {
      const u32 t1 = (u32)__builtin_amdgcn_s_memrealtime() + HTIME;
      const int beta = n >> 2, kq = 16 * g + 4 * (n & 3);
      v2u xr[8];
#pragma unroll
      for (int i = 0; i < 8; ++i) xr[i] = ldnt<v2u>(x + (beta * 512 + 64 * i + kq) * 2);
      float s = 0.f;
#pragma unroll
      for (int i = 0; i < 8; ++i) s += (blo(xr[i].x) + bhi(xr[i].x)) + (blo(xr[i].y) + bhi(xr[i].y));
      s += dppf<XOR1>(s); s += dppf<XOR2>(s);
      const float mean = xrow_sum(s) * (1.f / 512.f);
      float q = 0.f;
#pragma unroll
      for (int i = 0; i < 8; ++i)
#pragma unroll
        for (int e = 0; e < 2; ++e) { const float d0 = blo(xr[i][e]) - mean, d1 = bhi(xr[i][e]) - mean; q = fmaf(d0, d0, q); q = fmaf(d1, d1, q); }
      q += dppf<XOR1>(q); q += dppf<XOR2>(q);
      const float mb = rbf(mean), rsd = __builtin_amdgcn_rcpf(rbf(__builtin_amdgcn_sqrtf(rbf(rbf(xrow_sum(q) * (1.f / 512.f)) + 1e-5f))));
#pragma unroll
      for (int i = 0; i < 8; ++i)
#pragma unroll
        for (int e = 0; e < 2; ++e) { const u32 d = pk(blo(xr[i][e]) - mb, bhi(xr[i][e]) - mb); xr[i][e] = pk(blo(d) * rsd, bhi(d) * rsd); }
      PIN4(xr); PIN4(xr + 4);
      tag = get_tag();
#pragma nounroll
      while ((int)((u32)__builtin_amdgcn_s_memrealtime() - t1) < 0) __builtin_amdgcn_s_sleep(2);
      v4u sa[8] = {}, ca[8] = {};
#pragma nounroll
      for (u32 spin = 0; spin < ((EXP & 2) ? 0 : SPIN_CAP); ++spin) {
        CFENCE();
        {
          u32 bad = 0;
#pragma unroll
          for (int i = 0; i < 8; ++i) {
            sa[i] = __builtin_amdgcn_raw_buffer_load_b128(rs, beta * 256 + kq * 4 + (i & 3) * 1024, W_MOD + (i >> 2) * 4096, SC1);
            ca[i] = __builtin_amdgcn_raw_buffer_load_b128(rs, beta * 256 + kq * 4 + (i & 3) * 1024, W_MOD + 8192 + (i >> 2) * 4096, SC1);
          }
          SBAR();
#pragma unroll
          for (int i = 0; i < 8; ++i) bad |= (sa[i].y ^ tag) | (sa[i].w ^ tag) | (ca[i].y ^ tag) | (ca[i].w ^ tag);
          if (__all(bad == 0)) break;
        }
        __builtin_amdgcn_s_sleep(HSLEEP);
      }
#pragma unroll
      for (int sg = 0; sg < 4; ++sg) {
        v4u ha;
#pragma unroll
        for (int r = 0; r < 4; ++r) {
          const int i = 2 * sg + (r >> 1), e = r & 1;
          const u32 c1 = pk(1.f + blo(ca[i][2 * e]), 1.f + bhi(ca[i][2 * e]));
          const u32 t = pk(blo(xr[i][e]) * blo(c1), bhi(xr[i][e]) * bhi(c1));
          ha[r] = pk(blo(t) + blo(sa[i][2 * e]), bhi(t) + bhi(sa[i][2 * e]));
        }
        *lp<v4u>(L_HA + sg * 1024 + lane * 16) = ha;
      }
      CFENCE();
      *(volatile LAS u32*)C_HGOT = 1u;
      STAMP(11);
#pragma nounroll
      for (u32 spin = 0; lds_poll(C_QGOT) == 0u && spin < SPIN_CAP * 8; ++spin) __builtin_amdgcn_s_sleep(4);
    }
    CFENCE();
    v4u qa[4];
#pragma unroll
    for (int s = 0; s < 4; ++s) qa[s] = *lp<v4u>(L_QV + 64 * s + 16 * g);
    if (a == 0) STAMP(2);
    const u32 sc = L_SC + a * 8192;
    int landed = 0;
    const u32 kofs = n * 256 + ((g ^ n) << 4);
    const u32 spos = sc + ((n & 3) * 4 + (n >> 2)) * 4;
#pragma nounroll
    for (int j = 4 * NPRE; j < 128; ++j) {
      const int u = 2 * j + a;
#pragma nounroll
      for (u32 spin = 0; landed <= u && spin < SPIN_CAP * 8; ++spin) landed = (int)lds_poll(C_LANDED);
      CFENCE();
      const u32 ad = ((u & 31) << 12) + kofs;
      v4u k0 = *lp<v4u>(ad), k1 = *lp<v4u>(ad ^ 64), k2 = *lp<v4u>(ad ^ 128), k3 = *lp<v4u>(ad ^ 192);
      asm volatile("" : "+v"(k0), "+v"(k1), "+v"(k2), "+v"(k3) :: "memory");
      *(volatile LAS u32*)(C_CONS + 4 * a) = j + 1;
      f4 d = mfma32(qa[0], k0, zero4);
      d = mfma32(qa[1], k1, d); d = mfma32(qa[2], k2, d); d = mfma32(qa[3], k3, d);
      *lp<float>(spos + j * 64) = d[0];
    }
#pragma nounroll
    for (u32 spin = 0; lds_poll(C_PRE) != 8u && spin < SPIN_CAP * 8; ++spin) { }
    CFENCE();
    float mw, lw;
    {
      f4 s[8];
      float mx = -INFINITY;
#pragma unroll
      for (int t = 0; t < 8; ++t) {
        s[t] = *lp<f4>(sc + lane * 16 + t * 1024);
        const u32 p0 = pk(s[t][0], s[t][1]), p1 = pk(s[t][2], s[t][3]);
        const u32 r0 = pk(blo(p0) * 0.08838834764831845f, bhi(p0) * 0.08838834764831845f), r1 = pk(blo(p1) * 0.08838834764831845f, bhi(p1) * 0.08838834764831845f);
        s[t] = f4{blo(r0), bhi(r0), blo(r1), bhi(r1)};
        mx = fmaxf(fmaxf(mx, fmaxf(s[t][0], s[t][1])), fmaxf(s[t][2], s[t][3]));
      }
      mw = wave_max(mx);
      float sum = 0.f;
#pragma unroll
      for (int t = 0; t < 8; ++t) {
        v4u pp;
#pragma unroll
        for (int e = 0; e < 4; ++e) { const float p = __builtin_amdgcn_exp2f((s[t][e] - mw) * LOG2E); sum += p; pp[e] = pk(p, p); }
        *lp<v4u>(sc + lane * 16 + t * 1024) = pp;
      }
      lw = wave_sum(sum);
    }
    const int mlbase = W_ML + (b * 32 + h) * 64;
    if (lane == 0) __builtin_amdgcn_raw_buffer_store_b128(v4u{fu(mw), tag, fu(lw), tag}, rs, mlbase + (half * 2 + a) * 16, 0, SC1);
    u32 msk[4];
#pragma unroll
    for (int q = 0; q < 4; ++q) msk[q] = ((n & 7) >> 1) == q ? ((n & 1) ? 0xffff0000u : 0x0000ffffu) : 0u;
    f4 acc = zero4;
    float scale = 0.f;
    u32 sides = 0;
    u32 gap = 0, gbp = 0, xop = 0;
    const int ow = pid >> 1;
    const int oc = 64 * (ow >> 2) + 32 * (ow & 1) + 8 * (lane & 3) + 4 * (lane >> 5) + (ow & 2);
#pragma nounroll
    for (int j = 0; j < 126; ++j) {
      const int u = 256 + 2 * j + a;
      if (j == 108) {
        v4u ml;
        const int go = mod_off(b, oc);
#pragma nounroll
        for (u32 spin = 0; spin < SPIN_CAP; ++spin) {
          CFENCE();
          ml = __builtin_amdgcn_raw_buffer_load_b128(rs, mlbase + (lane & 3) * 16, 0, SC1);
          const v2u ga = __builtin_amdgcn_raw_buffer_load_b64(rs, go, 2 * 8192, SC1), gb = __builtin_amdgcn_raw_buffer_load_b64(rs, go, 5 * 8192, SC1);
          const v2u sd = __builtin_amdgcn_raw_buffer_load_b64(rs, W_SIDE + (lane & 3) * 8, 0, SC1);
          xop = ldnt<u32>(x + (b * 512 + oc) * 2);
          gap = ga.x; gbp = gb.x;
          sides = (u32)__builtin_amdgcn_ballot_w64(sd.x & 1);
          if (__all(((ml.y ^ tag) | (ml.w ^ tag) | (ga.y ^ tag) | (gb.y ^ tag) | (sd.y ^ tag)) == 0)) break;
        }
        float M = uf(ml.x); M = fmaxf(M, dppf<XOR1>(M)); M = fmaxf(M, dppf<XOR2>(M));
        float L = uf(ml.z) * __builtin_amdgcn_exp2f((uf(ml.x) - M) * LOG2E);
        L += dppf<XOR1>(L); L += dppf<XOR2>(L);
        scale = __builtin_amdgcn_exp2f((mw - M) * LOG2E) / L;
        *(volatile LAS float*)(C_SCALE + 4 * a) = uf((u32)__builtin_amdgcn_readfirstlane((int)fu(scale)));
        lds_add(C_SCNT, 1u);
        if (OUTPRE && a == 0 && !half && (lane & 28) == 0) {
          GAS u32* op = (GAS u32*)(out + (b * 512 + oc) * 2);
          if (OUTPRE == 1) { const u32 t = *op; asm volatile("" :: "v"(t)); }
          if (OUTPRE == 2) *op = 0u;
          if (OUTPRE == 3) __hip_atomic_store(op, 0u, __ATOMIC_RELAXED, __HIP_MEMORY_SCOPE_AGENT);
        }
        asm volatile("" : "+v"(xop));
      }
      const v4u p4 = *lp<v4u>(sc + j * 64 + g * 16);
      v4u bf[4];
#pragma unroll
      for (int i = 0; i < 4; ++i) bf[i] = v4u{p4[i] & msk[0], p4[i] & msk[1], p4[i] & msk[2], p4[i] & msk[3]};
      asm volatile("" : "+v"(bf[0]), "+v"(bf[1]), "+v"(bf[2]), "+v"(bf[3]) :: "memory");
#pragma nounroll
      for (u32 spin = 0; landed <= u && spin < SPIN_CAP * 8; ++spin) landed = (int)lds_poll(C_LANDED);
      CFENCE();
      const u32 ad = ((u & 31) << 12) + lane * 16;
      v4u v0 = *lp<v4u>(ad), v1 = *lp<v4u>(ad + 1024), v2 = *lp<v4u>(ad + 2048), v3 = *lp<v4u>(ad + 3072);
      SBAR();
      acc = mfma32(v0, bf[0], acc); acc = mfma32(v1, bf[1], acc); acc = mfma32(v2, bf[2], acc);
      asm volatile("" : "+v"(v3) :: "memory");
      *(volatile LAS u32*)(C_CONS + 4 * a) = 128 + j + 1;
      acc = mfma32(v3, bf[3], acc);
    }
    WARM_AT(1, 2);
    *lp<u32>(L_OA + (a + 3 * (n >> 3)) * 160 + (g * 8 + (n & 7)) * 4) = pk((n & 8 ? acc[2] : acc[0]) * scale, (n & 8 ? acc[3] : acc[1]) * scale);
    if (a == 1) {
      asm volatile("s_waitcnt lgkmcnt(0)" ::: "memory");
      __builtin_amdgcn_s_barrier();
      int xg = late(sides, xcd, 16) + b * 2048 + lane * 16;
      int xg2 = xg + 1024;
      u32 la = C_LN, ones = 0x3f803f80u;
      asm volatile("" : "+v"(xg), "+v"(xg2), "+v"(la), "+v"(ones));
      __builtin_amdgcn_s_setprio(2);
      __builtin_amdgcn_s_barrier();
      WARM_AT(8, 9);
      CFENCE();
      v4u m0, m1;
      u32 np = 8;
#define XLOAD m0 = __builtin_amdgcn_raw_buffer_load_b128(rs, xg, 0, SC1), m1 = __builtin_amdgcn_raw_buffer_load_b128(rs, xg2, 0, SC1)
      XLOAD;
      RETRY(tags_ok(m0, m1, tag), XLOAD, );
      WARM_AT(9, 10);
      const float s1 = dot2(m1.z, ones, dot2(m1.x, ones, dot2(m0.z, ones, dot2(m0.x, ones, 0.f))));
      const float s2 = dot2(m1.z, m1.z, dot2(m1.x, m1.x, dot2(m0.z, m0.z, dot2(m0.x, m0.x, 0.f))));
      float st = swap32_sum(s1, s2);
      st += dppf<XOR1>(st); st += dppf<XOR2>(st); st += dppf<HMIR>(st); st += dppf<ROR8>(st);
      st = swap16_sum(st, st) * (1.f / 512.f);
      WARM_AT(10, 11);
      const float mean = uf((u32)__builtin_amdgcn_readlane((int)fu(st), 0)), m2 = uf((u32)__builtin_amdgcn_readlane((int)fu(st), 32));
      float var = fmaf(-mean, mean, m2);
      if (__builtin_expect(__builtin_amdgcn_ballot_w64(!(var >= m2 * (1.f / 256.f))) != 0, 0)) {
        const u32 v[4] = {m0.x, m0.z, m1.x, m1.z};
        float q = 0.f;
#pragma unroll
        for (int e = 0; e < 4; ++e) { const float d0 = blo(v[e]) - mean, d1 = bhi(v[e]) - mean; q = fmaf(d0, d0, q); q = fmaf(d1, d1, q); }
        var = wave_sum(q) * (1.f / 512.f);
      }
      *lp<f2>(la) = f2{rbf(mean), __builtin_amdgcn_rcpf(rbf(__builtin_amdgcn_sqrtf(rbf(rbf(var) + 1e-5f))))};
      asm volatile("s_waitcnt lgkmcnt(0)" ::: "memory");
      __builtin_amdgcn_s_barrier();
      return;
    }
    __builtin_amdgcn_s_setprio(2);
    int io = late(sides, xcd, b * 4 + hh) + lane * 16;
    int xs = late(sides, (lane >> 2) & 7, 16) + b * 2048 + ow * 64 + (lane >> 5) * 32 + (lane & 3) * 8;
    float ga0 = blo(gap), ga1 = bhi(gap), xo0 = blo(xop), xo1 = bhi(xop);
    asm volatile("" : "+v"(io), "+v"(xs), "+v"(ga0), "+v"(ga1), "+v"(xo0), "+v"(xo1));
    v4u m[4];
    asm volatile("s_waitcnt lgkmcnt(0)" ::: "memory");
    __builtin_amdgcn_s_barrier();
    STAMP(4);
    u32 tl = lds_poll(C_TL);
    STAMP(6);
    WARM_AT(2, 3);
    u32 x1 = 0;
    u32 np = 7;
    {
      const u32 own = now32() + D1 - T1;
      if ((int)(own - tl) > 0) tl = own;
    }
    wait_until(tl + T1);
    load4(m, rs, io, io + 3072);
    WARM_AT(3, 4);
    RETRY(tags_ok(m, tag), load4(m, rs, io, io + 3072), tl = now32() - (T1 + TM));
    if (!half) {
      STAMP(15);
      WARM_AT(4, 5);
      float y0, y1;
      inbox_sum1(m, y0, y1);
      WARM_AT(5, 6);
      const u32 yb = inbox_sum2(y0, y1);
      WARM_AT(6, 7);
      const u32 t = pk(ga0 * blo(yb), ga1 * bhi(yb));
      x1 = pk(xo0 + blo(t), xo1 + bhi(t));
      __builtin_amdgcn_raw_buffer_store_b64(v2u{x1, tag}, rs, xs, 0, SC1);
      STAMP(16);
    }
    WARM_AT(7, 8);
    wait_until(tl + T2);
    __builtin_amdgcn_s_barrier();
    if (half) {
      __builtin_amdgcn_s_barrier();
      nap<2500>();
      if (lane < 32) *(GAS u64*)(ws + W_DBG + (blockIdx.x * 32 + lane) * 8) = *(volatile LAS u64*)(L_STAMP + lane * 8);
      return;
    }
    float r0 = blo(x1), r1 = bhi(x1), gb0 = blo(gbp), gb1 = bhi(gbp);
    io += 4096;
    int io3 = lane >= 32 ? W_END : io + 3072;
    u32 tg3 = lane >= 32 ? 0u : tag, oa = (b * 512 + oc) * 2;
    asm volatile("" : "+v"(r0), "+v"(r1), "+v"(gb0), "+v"(gb1), "+v"(io), "+v"(io3), "+v"(tg3), "+v"(oa));
    __builtin_amdgcn_s_barrier();
    WARM_AT(11, 12);
    __builtin_amdgcn_s_sleep(D3 * 24 / 64);
    wait_until(tl + T3);
    load4(m, rs, io, io3);
    WARM_AT(12, 13);
    np = 29;
    RETRY(tags_ok(m, tag, tg3), load4(m, rs, io, io3), );
    STAMP(19);
    WARM_AT(13, 14);
    float y0, y1;
    inbox_sum1(m, y0, y1);
    WARM_AT(14, 15);
    const u32 yb = inbox_sum2(y0, y1);
    WARM_AT(15, 16);
    const u32 t = pk(gb0 * blo(yb), gb1 * bhi(yb));
    if (FIXSPAN) wait_until(t_fix + 20000);
    if ((lane & 28) == 0) {
      const u32 res = pk(r0 + blo(t), r1 + bhi(t));
      if (OUTSC == 1) __hip_atomic_store((GAS u32*)(out + oa), res, __ATOMIC_RELAXED, __HIP_MEMORY_SCOPE_AGENT);
      else if (OUTSC == 2) __builtin_nontemporal_store(res, (GAS u32*)(out + oa));
      else *(GAS u32*)(out + oa) = res;
    }
    STAMP(20);
    if (lane < 32) *(GAS u64*)(ws + W_DBG + (blockIdx.x * 32 + lane) * 8) = *(volatile LAS u64*)(L_STAMP + lane * 8);
    return;
  }

  if (wave == 3) {
    u32 tref = (u32)__builtin_amdgcn_s_memrealtime() + T00;
    const u32 epoch = __builtin_amdgcn_raw_buffer_load_b32(rs, W_EPOCH, 0, SC1);
    if (TLBW) {
      u32 d0;
#define TOUCH(i) "s_load_dword %0, %" #i ", 0\n"
      asm volatile(TOUCH(1) TOUCH(2) TOUCH(3) TOUCH(4) TOUCH(5) TOUCH(6) TOUCH(7) TOUCH(8) TOUCH(9) TOUCH(10) TOUCH(11) "s_waitcnt lgkmcnt(0)"
                   : "=&s"(d0)
                   : "s"(wo + (size_t)(idx >> 4) * (2 << 20)), "s"(wg), "s"(wu), "s"(wd), "s"(kc + ((size_t)(b * 32 + h) << 21)),
                     "s"(wq + (size_t)(1 - (idx >> 4)) * (2 << 20)), "s"(wg + FF * 1024 - 64), "s"(wu + FF * 1024 - 64), "s"(wd + FF * 1024 - 64),
                     "s"(vc + ((size_t)(b * 32 + h) << 21)), "s"(out) : "memory");
    }
    const u32 tag = (u32)__builtin_amdgcn_readfirstlane((int)epoch) + 1u;
    *(volatile LAS v2u*)C_TAG = v2u{tag, 1u};
    asm volatile("s_mov_b64 exec, 0\n s_branch .Lldr_copies\n .Lwarm_ret0:\n s_mov_b64 exec, -1" ::: "memory");
    int lvl = LVL0, uref = 0, tau = TAU * 64 / 100, kp = 1, kr = 1;
    u32 lsum = 0, lcnt = 0, st = 0;
#pragma nounroll
    for (u32 spin = 0; spin < SPIN_CAP * 8; ++spin) {
      const v3u cc = *(volatile LAS v3u*)C_CONS;
      const int u = (int)lds_poll(C_LANDED);
      const u32 now = (u32)__builtin_amdgcn_s_memrealtime();
      const u32 c0 = __builtin_amdgcn_readfirstlane(cc.x), c1 = __builtin_amdgcn_readfirstlane(cc.y), pl = __builtin_amdgcn_readfirstlane(cc.z) ^ (0x88888888u >> (32 - 4 * NPRE));
      const u32 done = pl ? (__builtin_ctz(pl) & 28) * 2 : max(8u * NPRE, 2 * min(c0, c1));
      const int lag = (int)(now - tref) - (((u - uref) * tau) >> 6);
      lvl = min(max(LVL0 + ((lag * (u < G2AT ? GAIN : GAIN2) + 128) >> 8), !uref ? LVL0 : u < G2AT ? LMIN : LMIN2), 15);
      *(volatile LAS u32*)C_CTL = ((done + 32) << 12) | (lvl << 7);
      lsum += lvl; ++lcnt;
      if (u >= 32 && st == 0) { STAMP(25); st = 1; }
      if (u >= 80 && st == 1) { STAMP(24); st = 2; }
      if (u >= 128 && st == 2) { STAMP(26); st = 3; }
      if (u >= 256 && st == 3) { STAMP(28); st = 4; }
      if (u >= 384 && st == 4) { STAMP(27); st = 5; }
      if (u >= CEXIT) { *(volatile LAS u64*)(L_STAMP + 21 * 8) = t_start + lsum * 100 / lcnt; *(volatile LAS u64*)(L_STAMP + 22 * 8) = t_start + lvl * 100; }
      if (u >= CEXIT && done >= 476 && kr == 9) break;
      if (u >= 64 * kp - CK0) {
        if ((idx & 3) == 0 && lane == 0) __builtin_amdgcn_raw_buffer_store_b64(v2u{now, tag}, rs, W_PROG + kp * 512 + ((idx >> 2) * 8 + xcd) * 8, 0, SC1);
        ++kp;
      }
      if (u >= 64 * kr - CK0 + (kr < 8 ? RDLAG : RDLAG2)) {
        const v2u pr = __builtin_amdgcn_raw_buffer_load_b64(rs, W_PROG + kr * 512 + lane * 8, 0, SC1);
        const float ago = wave_sum(pr.y == tag ? (float)(int)(now - pr.x) : 0.f);
        const u32 tk = now - (u32)__builtin_amdgcn_readfirstlane((int)(ago * (1.f / 64.f)));
        if (kr > 1) tau = min(max((int)(tk - tref), 800), 1000);
        tref = tk; uref = 64 * kr - CK0; ++kr;
      }
      __builtin_amdgcn_s_sleep(3);
    }
    *(volatile LAS u32*)C_TL = tref + (u32)(((512 - uref) * tau) >> 6);
    CFENCE();
    u32 wa = L_OA + (2 + 3 * (n >> 3)) * 160 + (g * 8 + (n & 7)) * 4;
    v4u bf[16];
#pragma unroll
    for (int r = 0; r < 4; ++r) {
      const v4u p4 = *lp<v4u>(L_SC + (r & 1) * 8192 + (126 + (r >> 1)) * 64 + g * 16);
#pragma unroll
      for (int i = 0; i < 4; ++i)
#pragma unroll
        for (int q = 0; q < 4; ++q) bf[4 * r + i][q] = p4[i] & (((n & 7) >> 1) == q ? ((n & 1) ? 0xffff0000u : 0x0000ffffu) : 0u);
      PIN4(bf + 4 * r);
    }
#pragma nounroll
    for (u32 spin = 0; lds_poll(C_SCNT) != 2u && spin < SPIN_CAP * 8; ++spin) { }
    CFENCE();
    f2 sc2 = *lp<f2>(C_SCALE);
    const __amdgpu_buffer_rsrc_t rv = __builtin_amdgcn_make_buffer_rsrc((void*)(vc + ((size_t)(b * 32 + h) << 21) + (31 << 16) + half * 32768 + 16384), 0, 16384, 0x00020000);
    asm volatile("" : "+v"(wa), "+v"(sc2));
#pragma nounroll
    for (u32 spin = 0; lds_poll(C_ISSUED) == 0u && spin < SPIN_CAP * 8; ++spin) { }
    CFENCE();
    v4u vv[16];
    const int lastp = 509 - max(LASTW + (LASTG * (lvl - LVL0) >> 2), 4);
#pragma unroll
    for (int i = 0; i < 16; ++i) {
      if ((i & 3) == 0) {
#pragma nounroll
        for (u32 spin = 0; (int)lds_poll(C_LANDED) < lastp + (i >> 2) && spin < SPIN_CAP * 8; ++spin) { }
        CFENCE();
      }
      vv[i] = __builtin_amdgcn_raw_buffer_load_b128(rv, lane * 16 + (i & 1) * 1024 + ((i >> 2) & 1) * 2048, (i >> 3) * 4096 + ((i >> 1) & 1) * 8192, NT);
    }
    SBAR();
    f4 ac[2] = {zero4, zero4};
    float e0, e1;
#pragma unroll
    for (int i = 0; i < 16; ++i) {
      ac[(i >> 2) & 1] = mfma32(vv[i], bf[i], ac[(i >> 2) & 1]);
      SBAR();
      if (i == 11) { e0 = (n & 8 ? ac[0][2] : ac[0][0]) * sc2.x; e1 = (n & 8 ? ac[0][3] : ac[0][1]) * sc2.x; asm volatile("" : "+v"(e0), "+v"(e1)); }
    }
    STAMP(1);
    WARM_AT(26, ret1);
    *lp<u32>(wa) = pk(fmaf(n & 8 ? ac[1][2] : ac[1][0], sc2.y, e0), fmaf(n & 8 ? ac[1][3] : ac[1][1], sc2.y, e1));
    asm volatile("s_waitcnt lgkmcnt(0)" ::: "memory");
    __builtin_amdgcn_s_barrier();
    return;
  }

  const int w = wave - 4;
  if (first_mod) mod_tile<false>(cnd, wmod, rs, b * 8 + xcd, w, lane);
  {
    v4u kr[NPRE][4];
    const u32 ad = w * 4096 + n * 256 + ((g ^ n) << 4);
#pragma unroll
    for (int r = 0; r < NPRE; ++r) {
#pragma nounroll
      for (u32 spin = 0; (int)lds_poll(C_LANDED) <= 8 * r + w && spin < SPIN_CAP * 8; ++spin) __builtin_amdgcn_s_sleep(8);
      CFENCE();
#pragma unroll
      for (int s = 0; s < 4; ++s) kr[r][s] = *lp<v4u>((ad ^ (s * 64)) + (r & 3) * 32768);
      asm volatile("" : "+v"(kr[r][0]), "+v"(kr[r][1]), "+v"(kr[r][2]), "+v"(kr[r][3]) :: "memory");
      lds_add(C_PULL, 1u << (4 * r));
    }
#pragma nounroll
    for (u32 spin = 0; lds_poll(C_QGOT) == 0u && spin < SPIN_CAP * 8; ++spin) __builtin_amdgcn_s_sleep(8);
    CFENCE();
    v4u qa[4];
#pragma unroll
    for (int s = 0; s < 4; ++s) qa[s] = *lp<v4u>(L_QV + 64 * s + 16 * g);
#pragma unroll
    for (int r = 0; r < NPRE; ++r) {
      f4 d = mfma32(qa[0], kr[r][0], zero4);
      d = mfma32(qa[1], kr[r][1], d); d = mfma32(qa[2], kr[r][2], d); d = mfma32(qa[3], kr[r][3], d);
      *lp<float>(L_SC + (w & 1) * 8192 + (4 * r + (w >> 1)) * 64 + ((n & 3) * 4 + (n >> 2)) * 4) = d[0];
    }
    lds_add(C_PRE, 1u);
  }
  const u32 tag = get_tag();
  if (blockIdx.x < 4 && w == 6) {
    if (lane < 2 * NPROBE) __builtin_amdgcn_raw_buffer_store_b32(0u, rs, W_PROBE + blockIdx.x * 16384 + (lane & 1) * 8192 + (((lane >> 1) * 2432) & 8064), 0, SC1);
    asm volatile("s_waitcnt vmcnt(0)" ::: "memory");
    u32 best[2] = {~0u, ~0u}, sum[2] = {0u, 0u};
#pragma nounroll
    for (int i = 0; i < NPROBE; ++i) {
#pragma unroll
      for (int p = 0; p < 2; ++p) {
        u64 t0, t1; u32 d;
        asm volatile("s_memtime %0\n s_waitcnt lgkmcnt(0)\n s_load_dword %2, %3, 0\n s_waitcnt lgkmcnt(0)\n s_memtime %1\n s_waitcnt lgkmcnt(0)"
                     : "=&s"(t0), "=&s"(t1), "=&s"(d) : "s"(ws + W_PROBE + blockIdx.x * 16384 + p * 8192 + ((i * 2432) & 8064)));
        best[p] = min(best[p], (u32)(t1 - t0)); sum[p] += (u32)(t1 - t0);
      }
    }
    *(volatile LAS u64*)(L_STAMP + 9 * 8) = t_start + best[0]; *(volatile LAS u64*)(L_STAMP + 10 * 8) = t_start + best[1];
    *(volatile LAS u64*)(L_STAMP + 11 * 8) = t_start + sum[0] / NPROBE; *(volatile LAS u64*)(L_STAMP + 12 * 8) = t_start + sum[1] / NPROBE;
    if (lane == 0) __builtin_amdgcn_raw_buffer_store_b64(v2u{NUMA >= 0 ? (NUMA >> blockIdx.x) & 1u : best[1] < best[0] ? 1u : 0u, tag}, rs, W_SIDE + blockIdx.x * 8, 0, SC1);
  }
  if (!has_ffn) mod_tile<true>(cnd, wmod, rs, 16 + b * 8 + xcd, w, lane);
  const int fs = 7 * xcd + (pid & 7);
  v8u fo[2][4], fg[4];
  v4u fd[4];
  {
    const __amdgpu_buffer_rsrc_t ro = __builtin_amdgcn_make_buffer_rsrc((void*)(wo + (size_t)h * 131072), 0, (NOW & 1) ? 0 : 131072, 0x00020000);
    const int vo = 2 * g * 1024 + (64 * w + 4 * n) * 2;
#pragma unroll
    for (int s = 0; s < 4; ++s) {
      v2u r[8];
#pragma unroll
      for (int j = 0; j < 8; ++j) { r[j] = __builtin_amdgcn_raw_buffer_load_b64(ro, vo + (j >> 2) * 1024, (32 * s + 8 * (j & 3)) * 1024, NT); PACE(j); }
      if (s & 1) tr8x4<1>(r, fo[s >> 1]); else tr8x4<0>(r, fo[s >> 1]);
      PIN4(fo[s >> 1]);
      SBAR();
    }
  }
  if (has_ffn) {
    const char* pg = (n < 8 ? wg : wu) + (fs * 32 + 4 * (n & 7)) * 2;
#pragma unroll
    for (int s = 0; s < 2; ++s) {
      v2u r[8];
      const char* pr = pg + (size_t)(64 * w + 32 * (g >> 1) + 4 * (g & 1) + 2 * s) * (FF * 2);
#pragma unroll
      for (int j = 0; j < 8; ++j) { r[j] = (NOW & 2) ? v2u{0u, 0u} : ldnt<v2u>(pr + (8 * (j >> 1) + (j & 1)) * (FF * 2)); PACE(j); }
      if (s) tr8x4<1>(r, fg); else tr8x4<0>(r, fg);
      PIN4(fg);
      SBAR();
    }
  } else {
#pragma unroll
    for (int t = 0; t < 4; ++t) fg[t] = v8u{};
  }
  {
    const __amdgpu_buffer_rsrc_t rd = __builtin_amdgcn_make_buffer_rsrc((void*)(wd + (size_t)fs * 32768), 0, ((NOW & 2) || !has_ffn) ? 0 : 32768, 0x00020000);
    const int vd = 8 * g * 1024 + (128 * (w & 3) + 64 * (w >> 2) + 4 * n) * 2;
    v2u r[8];
#pragma unroll
    for (int j = 0; j < 8; ++j) { r[j] = __builtin_amdgcn_raw_buffer_load_b64(rd, vd + (j & 3) * 1024, (j >> 2) * 4096, NT); PACE(j); }
    tr8x4(r, fd);
    PIN4(fd);
    SBAR();
  }
  const bool lo_part = n & 4;
  const int xg = 8 * g + 2 * (n & 3) + (n >> 3);
  const u32 gidx = n & 8 ? 0xeeeeu : 0x4444u;
  float cb0, cb1, sh;
  u32 sides = 0;
  {
    v2u sv, cv; v4u xr;
    const int so = mod_off(b, 64 * w + 32 * ((xg >> 3) & 1) + 4 * ((xg >> 2) & 1) + 8 * (xg & 3) + 2 * (xg >> 4));
#pragma nounroll
    for (u32 spin = 0; spin < SPIN_CAP; ++spin) {
      CFENCE();
      sv = __builtin_amdgcn_raw_buffer_load_b64(rs, so, 3 * 8192, SC1);
      cv = __builtin_amdgcn_raw_buffer_load_b64(rs, so, 4 * 8192, SC1);
      const v2u sd = __builtin_amdgcn_raw_buffer_load_b64(rs, W_SIDE + (lane & 3) * 8, 0, SC1);
      xr = ldnt<v4u>(x + (b * 512 + 8 * lane) * 2);
      sides = (u32)__builtin_amdgcn_ballot_w64(sd.x & 1);
      if (__all(((sv.y ^ tag) | (cv.y ^ tag) | (sd.y ^ tag)) == 0)) break;
      __builtin_amdgcn_s_sleep(64);
    }
    const u32 c1p = pk(1.f + blo(cv.x), 1.f + bhi(cv.x));
    cb0 = blo(c1p); cb1 = bhi(c1p);
    sh = rbf(wave_sum((blo(xr.x) + bhi(xr.x)) + (blo(xr.y) + bhi(xr.y)) + (blo(xr.z) + bhi(xr.z)) + (blo(xr.w) + bhi(xr.w))) * (1.f / 512.f));
    if (has_ffn) {
      *lp<float>(L_C12 + w * 256 + lane * 4) = gu_dot(lo_part ? 0u : c1p, gidx, fg);
      SBAR();
      *lp<float>(L_C12 + 2048 + w * 256 + lane * 4) = gu_dot(lo_part ? 0u : sv.x, gidx, fg);
    }
    asm volatile("" : "+v"(cb0), "+v"(cb1), "+v"(sh));
  }
  if (blockIdx.x == 0 && w == 7) {
#pragma nounroll
    for (u32 spin = 0; spin < SPIN_CAP; ++spin) {
      CFENCE();
      u32 bad = 0;
#pragma nounroll
      for (int i = 0; i < 8; ++i) {
        const v4u t = __builtin_amdgcn_raw_buffer_load_b128(rs, W_ML + lane * 16, i * 1024, SC1);
        bad |= (t.y ^ tag) | (t.w ^ tag);
      }
      if (__all(bad == 0)) break;
      __builtin_amdgcn_s_sleep(64);
    }
    if (lane == 0) __builtin_amdgcn_raw_buffer_store_b32(tag, rs, W_EPOCH, 0, SC1);
  }
  asm volatile("s_cmp_lg_u32 %0, %1\n s_cbranch_scc1 .Lwarm_skip\n s_mov_b64 exec, 0\n s_branch .Lwarm_1\n .Lwarm_ret1:\n s_mov_b64 exec, -1\n .Lwarm_skip:" :: "s"(w), "i"(WARM ? 5 : 64) : "memory", "scc");
  if (w == 0) STAMP(13);
  int mo = late(sides, w, b * 4 + ((lane >> 3) & 3)) + pid * 64 + (lane & 7) * 8;
  const int oi = 32 * (n >> 3) + 8 * g + (n & 7);
  u32 ra = L_C12 + ((oi & 3) * 16 + (oi >> 2)) * 4, oa = L_OA + (min(n & 3, 2) + 3 * ((n >> 2) & 1)) * 160 + g * 16;
  u32 oidx = n & 4 ? 0xeeeeu : 0x4444u;
  f4 d[4] = {};
  asm volatile("s_waitcnt lgkmcnt(0)" : "+v"(ra), "+v"(oa), "+v"(mo), "+v"(oidx), "+v"(d[0]), "+v"(d[1]), "+v"(d[2]), "+v"(d[3]) :: "memory");
  __builtin_amdgcn_s_barrier();
  CYC(0);
  WARM_AT(16, 17);
  CFENCE();
  {
    const v4u a0 = *lp<v4u>(oa), a1 = *lp<v4u>(oa + 64);
    SBAR();
#pragma unroll
    for (int t = 0; t < 4; ++t) d[t] = smfmac(a0, fo[0][t], d[t], oidx);
#pragma unroll
    for (int t = 0; t < 4; ++t) d[t] = smfmac(a1, fo[1][t], d[t], oidx);
    WARM_AT(17, 18);
    const float y0 = swap16_sum((d[0][0] + d[0][1]) + d[0][2], (d[2][0] + d[2][1]) + d[2][2]), y1 = swap16_sum((d[1][0] + d[1][1]) + d[1][2], (d[3][0] + d[3][1]) + d[3][2]);
    if (lane < 32) __builtin_amdgcn_raw_buffer_store_b64(v2u{pk(y0, y1), tag}, rs, mo, 0, SC1);
  }
  CYC(1);
  if (w == 0 && STAMPS == 1) STAMP(14);
  WARM_AT(18, 19);
  float c1 = sum8(ra), c2 = sum8(ra + 2048);
#pragma unroll
  for (int t = 0; t < 4; ++t) d[t] = zero4;
  u32 gi = gidx;
  u32 wa = L_GURED + w * 256 + lane * 4, la = C_LN;
  int xa = w * 256 + xg * 8 + late(sides, xcd, 16) + b * 2048;
  mo = has_ffn ? late(sides, (2 * w + (lane >> 5)) & 7, b * 4 + ((lane >> 3) & 3)) + 4096 + fs * 64 + (lane & 7) * 8 : W_END;
  asm volatile("" : "+v"(c1), "+v"(c2), "+v"(d[0]), "+v"(d[1]), "+v"(d[2]), "+v"(d[3]), "+v"(gi), "+v"(wa), "+v"(la), "+v"(xa), "+v"(mo));
#pragma unroll
  for (int t = 0; t < 4; ++t) *lp<v4u>(w * 4096 + t * 1024 + lane * 16) = fd[t];
  u32 fa = (w | 4) * 4096 + lane * 16;
  asm volatile("s_waitcnt lgkmcnt(0)" : "+v"(fa) :: "memory");
  WARM_AT(19, 20);
  __builtin_amdgcn_s_barrier();
  CYC(2);
  v4u fe[4];
  v2u m;
  u32 np = 8;
  CFENCE();
  m = __builtin_amdgcn_raw_buffer_load_b64(rs, xa, 0, SC1);
  SBAR();
#pragma unroll
  for (int t = 0; t < 4; ++t) fe[t] = *lp<v4u>(fa + t * 1024);
  PIN4(fe);
  RETRY(__builtin_amdgcn_ballot_w64(m.y != tag) == 0, m = __builtin_amdgcn_raw_buffer_load_b64(rs, xa, 0, SC1), );
  CYC(3);
  if (w == 0 && STAMPS == 1) STAMP(17);
  WARM_AT(20, 21);
  {
    const float a0 = (blo(m.x) - sh) * cb0, a1 = (bhi(m.x) - sh) * cb1;
    const u32 hi = pk(a0, a1), lo = pk(a0 - blo(hi), a1 - bhi(hi));
    const u32 p = lo_part ? lo : hi;
    WARM_AT(21, 22);
    gu_mfma(p, gi, fg, d);
    CYC(4);
    WARM_AT(22, 23);
    *lp<float>(wa) = gu_fold(d);
  }
  CYC(5);
  asm volatile("s_waitcnt lgkmcnt(0)" ::: "memory");
  __builtin_amdgcn_s_barrier();
  if (w >= 4) return;
  CYC(6);
  CFENCE();
  {
    const f2 ln = *lp<f2>(la);
    const float gsum = sum8(ra + (L_GURED - L_C12));
    WARM_AT(23, 24);
    const float s = rbf(fmaf(ln.y, fmaf(sh - ln.x, c1, gsum), c2));
    CYC(7);
    const float up = dppf<ROR8>(s);
    const float z = s * fmaf(s * s, 0.044715f * 0.7978845608028654f * 2.f * LOG2E, 0.7978845608028654f * 2.f * LOG2E);
    const float ge = rbf(s * __builtin_amdgcn_rcpf(1.f + __builtin_amdgcn_exp2f(-z)));
    const float act = ge * up;
    WARM_AT(24, 25);
    const u32 ap = pk(act, dppf<XOR1>(act));
    const v4u aa = {dppu<0x150>(ap), dppu<0x152>(ap), dppu<0x154>(ap), dppu<0x156>(ap)};
    float y[8];
#pragma unroll
    for (int t = 0; t < 8; ++t) y[t] = mfma32(aa, t < 4 ? fd[t & 3] : fe[t & 3], zero4)[0];
    CYC(8);
    WARM_AT(25, 26);
    const u32 p0 = pk(y[0], y[1]), p1 = pk(y[2], y[3]), p2 = pk(y[4], y[5]), p3 = pk(y[6], y[7]);
    const u32 p01 = (g & 1) ? p1 : p0, p23 = (g & 1) ? p3 : p2;
    __builtin_amdgcn_raw_buffer_store_b64(v2u{(g & 2) ? p23 : p01, tag}, rs, mo, 0, SC1);
  }
  CYC(9);
  if (w == 0) STAMP(18);
}

}

namespace diag {
typedef unsigned u32; typedef unsigned long long u64;
__global__ void k_diag(const u64* h, int stat, int st) {
  if (threadIdx.x != 0) return;
  u64 t0 = ~0ull; for (int w = 0; w < 256; ++w) t0 = h[w * 32] < t0 ? h[w * 32] : t0;
  u64 mn = ~0ull, mx = 0, sum = 0; int n = 0;
  for (int w = 0; w < 256; ++w) { u64 v = h[w * 32 + st]; if (v < t0 || v - t0 > 100000) continue; v -= t0; mn = v < mn ? v : mn; mx = v > mx ? v : mx; sum += v; ++n; }
  u64 val = stat == 0 ? mn : stat == 1 ? (n ? sum / n : 0) : stat == 2 ? mx : 0;
  if (stat == 3) { val = h[st] * 100; for (int w = 0; w < 256; ++w) if (h[w * 32 + st] != ((h[st] + w) & 7)) val += 1000; if (val > 50000) val = 50000; }
  if (stat == 5) { u64 a = 0, b2 = 0, c = 0; for (int w = 0; w < 256; ++w) { a += h[w * 32 + 7] != 0; b2 += h[w * 32 + 8] != 0; c += h[w * 32 + 29] != 0; } val = (a ? 400 : 0) + (b2 ? 200 : 0) + (c ? 100 : 0); }
  if (stat == 6) { u64 tl = ~0ull; for (int w = 0; w < 256; ++w) tl = h[w * 32 + 30] < tl ? h[w * 32 + 30] : tl; val = mx + t0 + 500 - tl; }
  const u64 end = t0 + 30000 + val * 40;
  while (__builtin_amdgcn_s_memrealtime() < end) __builtin_amdgcn_s_sleep(4);
}
}
extern "C" void kernel_launch(void* const* d_in, const int* in_sizes, int n_in, void* d_out, int out_size, void* d_ws, size_t ws_size, hipStream_t stream) {
  hipLaunchKernelGGL(blk::fused_block, dim3(256), dim3(blk::NTHR), 0, stream,
                     (const char*)d_in[0], (const char*)d_in[1], (const char*)d_in[2], (const char*)d_in[3], (const char*)d_in[4], (const char*)d_in[6],
                     (const char*)d_in[7], (const char*)d_in[8], (const char*)d_in[9], (const char*)d_in[10], (char*)d_out, (char*)d_ws);
  diag::k_diag<<<1, 64, 0, stream>>>((const diag::u64*)((char*)d_ws + blk::W_DBG), 5, 0);
}
```

```cpp
#include <hip/hip_runtime.h>
#pragma clang diagnostic ignored "-Wint-to-pointer-cast"

#define WLEVELS "13,14,15,16,17,18,19,20,21,22,23,24,25,26,28,30"
#define LVL0 7
#define U0 4
#define TAU 1330
#define T00 330
#define GAIN 12
#define CLOSED 1
#define RDLAG 24
#define WSTART 0
#define WSTEP 0
#define WARM 1
#define WBOOST 0
#define PREF 0
#define MDELAY 0
#define CODEPF 1
#define TLBW 3
#define PF4 0
#define EXP 0
#define FIXSPAN 0
#define OUTPRE 0
#define OUTSC 0
#define GAIN2 20
#define G2AT 448
#define LMIN 3
#define LMIN2 0
#define CEXIT 492
#define LASTG 1
#define FASTN 8
#define DEPTH 3
#define STAG 3
#define LAZY 8
#define D1 22
#define CK0 44
#define RDLAG2 14
#define D2 320
#define NPROBE 8
#define TM 30
#define JML 48
#define INJ 0
#define SHIFT 0
#define GL 40
#define XL 128
#define SMIN 70
#define LMAX 15
#define MODLATE 0
#define WPOL NT
#define MWAIT 4
#define WQEARLY 0
#define NPRE 8
#define CMOD 1u
#define HSLEEP 16
#define QSLEEP 24
#define HTIME 720
#define QDELAY 3000
#define WQFLY 4
#define PACEN 1
#define NOW 0
#define T1 80
#define T2 (T1 + 57)
#define T3 (T2 + 100)
#define D3 20
#define HINTK 1
#define CW 0
#define LASTW 6
#define EXPA 0
#define EXPB 0
#define EXPC 0
#define NUMA -1
#define STAMPS 0

namespace blk {

typedef unsigned short u16;
typedef unsigned u32;
typedef unsigned long long u64;
typedef u32 v4u __attribute__((ext_vector_type(4)));
typedef u32 v8u __attribute__((ext_vector_type(8)));
typedef __bf16 bf16x16 __attribute__((ext_vector_type(16)));
typedef u32 v3u __attribute__((ext_vector_type(3)));
typedef u32 v2u __attribute__((ext_vector_type(2)));
typedef float f4 __attribute__((ext_vector_type(4)));
typedef float f2 __attribute__((ext_vector_type(2)));
typedef __bf16 bf8 __attribute__((ext_vector_type(8)));
typedef __bf16 bf2 __attribute__((ext_vector_type(2)));
typedef short s4 __attribute__((ext_vector_type(4)));
#define GAS __attribute__((address_space(1)))
#define LAS __attribute__((address_space(3)))

constexpr int NTHR = 768;
constexpr int FF = 1792;
constexpr int L_SC = 131072;
constexpr int L_MODRED = L_SC + 16384;
constexpr int L_GURED = L_MODRED + 8192;
constexpr int L_OA = L_GURED + 4096;
constexpr int L_C12 = L_MODRED;
constexpr int L_HA = L_GURED;
constexpr int L_QV = L_OA + 960;
constexpr int L_SINK = L_QV + 256;
constexpr int L_CTRL = L_SINK + 256;
constexpr int C_CONS = L_CTRL, C_PULL = L_CTRL + 8, C_MODCNT = L_CTRL + 12, C_PRE = L_CTRL + 16, C_QGOT = L_CTRL + 20, C_TAG = L_CTRL + 24;
constexpr int C_HGOT = L_CTRL + 36, C_ISSUED = L_CTRL + 40, C_SCNT = L_CTRL + 44, C_SCALE = L_CTRL + 48, C_LN = L_CTRL + 56, C_PF = L_CTRL + 64, C_TL = L_CTRL + 68, C_YTAG = L_CTRL + 72;
constexpr int C_CTL = L_CTRL + 32;
constexpr int C_LANDED = L_CTRL + 80;
constexpr int L_STAMP = C_LANDED + 256;
constexpr int LDS_BYTES = L_STAMP + (STAMPS ? 256 : 0);
constexpr int W_EPOCH = 0, W_PROG = 4096, W_SIDE = 12288, W_ML = 16384, W_PROBE = 196608;
constexpr int W_MOD = 65536;
constexpr int W_Q = 131072;
constexpr int W_LATE = 262144, W_END = W_LATE + 72 * 16384, W_DBG = W_END;
__device__ __forceinline__ int late(u32 sides, int x, int blk) { return W_LATE + ((x & 3) * 18 + blk) * 16384 + (int)((((u32)x >> 2) ^ (sides >> (x & 3))) & 1u) * 8192; }
constexpr int SC1 = 16, NT = 2;
constexpr u32 SPIN_CAP = 1u << 18;

__device__ __forceinline__ float blo(u32 u) { return __builtin_bit_cast(float, u << 16); }
__device__ __forceinline__ float bhi(u32 u) { return __builtin_bit_cast(float, u & 0xffff0000u); }
__device__ __forceinline__ u32 pk(float a, float b) { f2 p = {a, b}; return __builtin_bit_cast(u32, __builtin_convertvector(p, bf2)); }
__device__ __forceinline__ float rbf(float a) { return blo(pk(a, a)); }
__device__ __forceinline__ u32 fu(float a) { return __builtin_bit_cast(u32, a); }
__device__ __forceinline__ float uf(u32 a) { return __builtin_bit_cast(float, a); }
__device__ __forceinline__ f4 mfma32(v4u a, v4u b, f4 c) { return __builtin_amdgcn_mfma_f32_16x16x32_bf16(__builtin_bit_cast(bf8, a), __builtin_bit_cast(bf8, b), c, 0, 0, 0); }
__device__ __forceinline__ f4 mfma16(v2u a, v2u b, f4 c) { return __builtin_amdgcn_mfma_f32_16x16x16bf16_1k(__builtin_bit_cast(s4, a), __builtin_bit_cast(s4, b), c, 0, 0, 0); }
__device__ __forceinline__ float dot2(u32 a, u32 b, float c) { return __builtin_amdgcn_fdot2_f32_bf16(__builtin_bit_cast(bf2, a), __builtin_bit_cast(bf2, b), c, false); }
__device__ __forceinline__ f4 smfmac(v4u a, v8u b, f4 c, u32 idx) { return __builtin_amdgcn_smfmac_f32_16x16x64_bf16(__builtin_bit_cast(bf8, a), __builtin_bit_cast(bf16x16, b), c, (int)idx, 0, 0); }
template <int CTRL> __device__ __forceinline__ u32 dppu(u32 x) { return (u32)__builtin_amdgcn_mov_dpp((int)x, CTRL, 0xf, 0xf, true); }
template <int CTRL> __device__ __forceinline__ float dppf(float x) { return uf(dppu<CTRL>(fu(x))); }
constexpr int XOR1 = 0xB1, XOR2 = 0x4E, HMIR = 0x141, ROR4 = 0x124, ROR8 = 0x128;
__device__ __forceinline__ float xrow_sum(float x) {
  auto s = __builtin_amdgcn_permlane16_swap(fu(x), fu(x), false, false);
  x = uf(s[0]) + uf(s[1]);
  auto t = __builtin_amdgcn_permlane32_swap(fu(x), fu(x), false, false);
  return uf(t[0]) + uf(t[1]);
}
__device__ __forceinline__ float xrow_max(float x) {
  auto s = __builtin_amdgcn_permlane16_swap(fu(x), fu(x), false, false);
  x = fmaxf(uf(s[0]), uf(s[1]));
  auto t = __builtin_amdgcn_permlane32_swap(fu(x), fu(x), false, false);
  return fmaxf(uf(t[0]), uf(t[1]));
}
__device__ __forceinline__ float wave_sum(float x) { x += dppf<XOR1>(x); x += dppf<XOR2>(x); x += dppf<HMIR>(x); x += dppf<ROR8>(x); return xrow_sum(x); }
__device__ __forceinline__ float wave_max(float x) { x = fmaxf(x, dppf<XOR1>(x)); x = fmaxf(x, dppf<XOR2>(x)); x = fmaxf(x, dppf<HMIR>(x)); x = fmaxf(x, dppf<ROR8>(x)); return xrow_max(x); }
__device__ __forceinline__ u32 perm_lo(u32 r0, u32 r1) { return __builtin_amdgcn_perm(r1, r0, 0x05040100u); }
__device__ __forceinline__ u32 perm_hi(u32 r0, u32 r1) { return __builtin_amdgcn_perm(r1, r0, 0x07060302u); }
#define CFENCE() asm volatile("" ::: "memory")
#define SBAR() __builtin_amdgcn_sched_barrier(0)
#define PIN4(a) asm volatile("" : "+v"((a)[0]), "+v"((a)[1]), "+v"((a)[2]), "+v"((a)[3]))
template <typename T> __device__ __forceinline__ LAS T* lp(u32 off) { return (LAS T*)off; }
__device__ __forceinline__ u32 lds_poll(u32 off) { return (u32)__builtin_amdgcn_readfirstlane((int)*(volatile LAS u32*)off); }
__device__ __forceinline__ void lds_add(u32 off, u32 v) {
  asm volatile("s_mov_b64 exec, 1\n ds_add_u32 %0, %1\n s_mov_b64 exec, -1" :: "v"(off), "v"(v) : "memory");
}
template <typename T> __device__ __forceinline__ T ldnt(const void* p) { return __builtin_nontemporal_load((const GAS T*)p); }
template <typename T> __device__ __forceinline__ T ldg(const void* p) { return *(const GAS T*)p; }
__device__ __forceinline__ void tr8x4(const v2u (&r)[8], v4u (&f)[4]) {
#pragma unroll
  for (int q = 0; q < 4; ++q) {
    f[0][q] = perm_lo(r[2 * q].x, r[2 * q + 1].x); f[1][q] = perm_hi(r[2 * q].x, r[2 * q + 1].x);
    f[2][q] = perm_lo(r[2 * q].y, r[2 * q + 1].y); f[3][q] = perm_hi(r[2 * q].y, r[2 * q + 1].y);
  }
}
template <int HALF> __device__ __forceinline__ void tr8x4(const v2u (&r)[8], v8u (&f)[4]) {
#pragma unroll
  for (int q = 0; q < 4; ++q) {
    f[0][4 * HALF + q] = perm_lo(r[2 * q].x, r[2 * q + 1].x); f[1][4 * HALF + q] = perm_hi(r[2 * q].x, r[2 * q + 1].x);
    f[2][4 * HALF + q] = perm_lo(r[2 * q].y, r[2 * q + 1].y); f[3][4 * HALF + q] = perm_hi(r[2 * q].y, r[2 * q + 1].y);
  }
}
#define ONE_PAGE(n) do { SBAR(); asm volatile("s_branch 1f\n .p2align 12,," #n "\n1:"); SBAR(); } while (0)
#define MARK(k) asm volatile(".Lmark_" #k ":")

#define CYC(i) do { } while (0)
#define STAMP(i) do { } while (0)

__device__ __forceinline__ int mod_off(int b, int c) { return W_MOD + (c >> 6) * 1024 + b * 256 + ((c & 63) >> 1) * 8; }
__device__ __forceinline__ float half_of(u32 v, int odd) { return odd ? bhi(v) : blo(v); }
__device__ __forceinline__ void delay(int cyc) {
  const u64 t0 = __builtin_amdgcn_s_memtime();
#pragma nounroll
  while ((int)(__builtin_amdgcn_s_memtime() - t0) < cyc) { }
}
template <int CYC> __device__ __forceinline__ void nap() {
#pragma unroll
  for (int i = 0; i < CYC / 8128; ++i) __builtin_amdgcn_s_sleep(127);
  if (CYC % 8128 >= 64) __builtin_amdgcn_s_sleep((CYC % 8128) / 64);
}
__device__ __forceinline__ u32 get_tag() {
  v2u t;
#pragma nounroll
  for (u32 spin = 0; spin < SPIN_CAP * 8; ++spin) { t = *(volatile LAS v2u*)C_TAG; if (__builtin_amdgcn_readfirstlane((int)t.y)) break; }
  return (u32)__builtin_amdgcn_readfirstlane((int)t.x);
}
__device__ __forceinline__ u32 now32() { return (u32)__builtin_amdgcn_s_memrealtime(); }
__device__ __forceinline__ void wait_until(u32 t) {
#pragma nounroll
  while ((int)(now32() - t) < 0) { }
}
__device__ __forceinline__ float swap32_sum(float a, float b) {
  auto t = __builtin_amdgcn_permlane32_swap(fu(a), fu(b), false, false);
  return uf(t[0]) + uf(t[1]);
}
__device__ __forceinline__ float swap16_sum(float a, float b) {
  auto t = __builtin_amdgcn_permlane16_swap(fu(a), fu(b), false, false);
  return uf(t[0]) + uf(t[1]);
}
__device__ __forceinline__ void inbox_sum1(const v4u (&m)[4], float& y0, float& y1) {
  const float a0 = (blo(m[0].x) + blo(m[1].x)) + (blo(m[2].x) + blo(m[3].x)), a1 = (bhi(m[0].x) + bhi(m[1].x)) + (bhi(m[2].x) + bhi(m[3].x));
  const float b0 = (blo(m[0].z) + blo(m[1].z)) + (blo(m[2].z) + blo(m[3].z)), b1 = (bhi(m[0].z) + bhi(m[1].z)) + (bhi(m[2].z) + bhi(m[3].z));
  y0 = swap32_sum(a0, b0); y1 = swap32_sum(a1, b1);
}
__device__ __forceinline__ u32 inbox_sum2(float y0, float y1) {
  float y = swap16_sum(y0, y1);
  y += dppf<ROR4>(y); y += dppf<ROR8>(y);
  auto t = __builtin_amdgcn_permlane16_swap(fu(y), fu(y), false, false);
  return pk(uf(t[0]), uf(t[1]));
}
__device__ __forceinline__ void load4(v4u (&m)[4], __amdgpu_buffer_rsrc_t rs, int o, int o3) {
  __builtin_assume(o >= 0 && o < (1 << 30));
#pragma unroll
  for (int i = 0; i < 4; ++i) m[i] = __builtin_amdgcn_raw_buffer_load_b128(rs, i == 3 ? o3 : o + i * 1024, 0, SC1);
}
__device__ __forceinline__ bool tags_ok(const v4u (&m)[4], u32 tag, u32 tag3) {
  u32 bad = ((m[0].y ^ tag) | (m[0].w ^ tag)) | ((m[1].y ^ tag) | (m[1].w ^ tag));
  asm("" : "+v"(bad));
  bad |= (m[2].y ^ tag) | (m[2].w ^ tag);
  asm("" : "+v"(bad));
  bad |= (m[3].y ^ tag3) | (m[3].w ^ tag3);
  asm("" : "+v"(bad));
  return __builtin_amdgcn_ballot_w64(bad != 0) == 0;
}
__device__ __forceinline__ bool tags_ok(v4u m0, v4u m1, u32 tag) {
  u32 bad = (m0.y ^ tag) | (m0.w ^ tag);
  asm("" : "+v"(bad));
  bad |= (m1.y ^ tag) | (m1.w ^ tag);
  asm("" : "+v"(bad));
  return __builtin_amdgcn_ballot_w64(bad != 0) == 0;
}
__device__ __forceinline__ bool tags_ok(const v4u (&m)[4], u32 tag) { return tags_ok(m, tag, tag); }
__device__ __forceinline__ void gu_mfma(u32 p, u32 idx, const v8u (&f)[4], f4 (&d)[4]) {
  const v4u a = {dppu<0x00>(p), dppu<0x55>(p), dppu<0xAA>(p), dppu<0xFF>(p)};
#pragma unroll
  for (int t = 0; t < 4; ++t) d[t] = smfmac(a, f[t], d[t], idx);
}
__device__ __forceinline__ float gu_fold(const f4 (&d)[4]) { return swap16_sum(swap32_sum(d[0][0], d[2][0]), swap32_sum(d[1][0], d[3][0])); }
__device__ __forceinline__ float gu_dot(u32 p, u32 idx, const v8u (&f)[4]) { f4 d[4] = {}; gu_mfma(p, idx, f, d); return gu_fold(d); }
__device__ __forceinline__ float sum8(u32 addr) {
  float p[8];
#pragma unroll
  for (int i = 0; i < 8; ++i) p[i] = *lp<float>(addr + i * 256);
  return ((p[0] + p[1]) + (p[2] + p[3])) + ((p[4] + p[5]) + (p[6] + p[7]));
}
__device__ __forceinline__ void wait_unit(int& landed, int u, u32 addr) {
  u32 t, spin = SPIN_CAP * 8;
  asm volatile("s_cmp_gt_i32 %[l], %[u]\n s_cbranch_scc1 2f\n"
               "1:\n ds_read_b32 %[t], %[a]\n s_sub_u32 %[s], %[s], 1\n s_cbranch_scc1 2f\n s_waitcnt lgkmcnt(0)\n"
               "v_readfirstlane_b32 %[l], %[t]\n s_cmp_le_i32 %[l], %[u]\n s_cbranch_scc1 1b\n2:\n s_waitcnt lgkmcnt(0)"
               : [l] "+&s"(landed), [t] "=&v"(t), [s] "+&s"(spin) : [u] "s"(u), [a] "v"(addr) : "memory", "scc");
  landed = __builtin_amdgcn_readfirstlane(landed);
}
__device__ __forceinline__ void wait_pos(int u) {
#pragma nounroll
  for (u32 spin = 0; (int)lds_poll(C_LANDED) < u && spin < SPIN_CAP * 8; ++spin) __builtin_amdgcn_s_sleep(4);
}
#define RETRY(ok, again, after) do { if (__builtin_expect(!(ok), 0)) { u32 spin_ = 0; _Pragma("nounroll") do { CFENCE(); again; NP_INC; } while (!(ok) && ++spin_ < SPIN_CAP); after; } } while (0)
#define NP_INC
#define PACE(j) do { if (((j) % PACEN) == PACEN - 1) { asm volatile("s_waitcnt vmcnt(0)" ::: "memory"); SBAR(); } } while (0)
constexpr float LOG2E = 1.4426950408889634f;

template <bool PACED>
__device__ __forceinline__ void mod_tile(const char* cnd, const char* wmod, __amdgpu_buffer_rsrc_t rs, int cb, int w, int lane) {
  const int g = lane >> 4, n = lane & 15;
  const f4 zero4 = {0.f, 0.f, 0.f, 0.f};
  const v2u cv = ldnt<v2u>(cnd + ((n >> 2) * 512 + 64 * w + 16 * g + 4 * (n & 3)) * 2);
  const __amdgpu_buffer_rsrc_t rm = __builtin_amdgcn_make_buffer_rsrc((void*)(wmod + (size_t)(64 * w) * 6144 + cb * 128), 0, (EXP & 64) ? 0 : 64 * 6144, 0x00020000);
  v2u r[2][8];
  if (!PACED) {
#pragma nounroll
    for (u32 spin = 0; lds_poll(C_PF) == 0u && spin < SPIN_CAP * 8; ++spin) { }
    nap<MDELAY>();
    CFENCE();
  }
#pragma unroll
  for (int j = 0; j < 16; ++j) { r[j >> 3][j & 7] = __builtin_amdgcn_raw_buffer_load_b64(rm, 16 * g * 6144 + n * 8, j * 6144, PACED ? WPOL : NT); if (PACED) PACE(j); }
  SBAR();
  u32 sp[2];
#pragma unroll
  for (int e = 0; e < 2; ++e) {
    const float c0 = blo(cv[e]), c1 = bhi(cv[e]);
    sp[e] = pk(c0 * __builtin_amdgcn_rcpf(1.f + __builtin_amdgcn_exp2f(-c0 * LOG2E)), c1 * __builtin_amdgcn_rcpf(1.f + __builtin_amdgcn_exp2f(-c1 * LOG2E)));
  }
  const v4u a0 = {dppu<0x00>(sp[0]), dppu<0x00>(sp[1]), dppu<0x55>(sp[0]), dppu<0x55>(sp[1])};
  const v4u a1 = {dppu<0xAA>(sp[0]), dppu<0xAA>(sp[1]), dppu<0xFF>(sp[0]), dppu<0xFF>(sp[1])};
  v4u f0[4], f1[4];
  tr8x4(r[0], f0); tr8x4(r[1], f1);
  f4 pt;
#pragma unroll
  for (int t = 0; t < 4; ++t) pt[t] = mfma32(a1, f1[t], mfma32(a0, f0[t], zero4))[0];
  *lp<f4>(L_MODRED + w * 1024 + lane * 16) = pt;
  u32 old;
  asm volatile("s_mov_b64 exec, 1\n ds_add_rtn_u32 %0, %1, %2\n s_mov_b64 exec, -1\n s_waitcnt lgkmcnt(0)" : "=&v"(old) : "v"(C_MODCNT), "v"(1u) : "memory");
  if ((__builtin_amdgcn_readfirstlane((int)old) & 7) == 7) {
    f4 p[8];
#pragma unroll
    for (int ww = 0; ww < 8; ++ww) p[ww] = *lp<f4>(L_MODRED + ww * 1024 + lane * 16);
    const f4 v = ((p[0] + p[1]) + (p[2] + p[3])) + ((p[4] + p[5]) + (p[6] + p[7]));
    const u32 tag = get_tag();
    __builtin_amdgcn_raw_buffer_store_b128(v4u{pk(v[0], v[1]), tag, pk(v[2], v[3]), tag}, rs, lane * 16 + W_MOD + cb * 1024, 0, SC1);
    if (!PACED) STAMP(9);
  }
}

__global__ void __launch_bounds__(NTHR) fused_block(const char* x, const char* kc, const char* vc, const char* wq, const char* wo, const char* wg,
                                                    const char* wu, const char* wd, const char* cnd, const char* wmod, char* out, char* ws) {
  __shared__ __attribute__((aligned(1024))) char lds_mem[LDS_BYTES];
  const int lane = threadIdx.x & 63, wave = __builtin_amdgcn_readfirstlane(threadIdx.x >> 6);
  const int xcd = blockIdx.x & 7, idx = blockIdx.x >> 3;
  const int half = idx & 1, hh = (idx >> 1) & 3, b = idx >> 3;
  const int h = xcd + 8 * hh;
  const int pid = 8 * xcd + (idx & 7);
  const bool has_ffn = (idx & 7) < 7;
  const bool first_mod = !(EXP & 8) && hh == 3 && b < 3;
  const int g = lane >> 4, n = lane & 15;
  const f4 zero4 = {0.f, 0.f, 0.f, 0.f};
  const u32 t_fix = FIXSPAN ? now32() : 0;
#define PREFETCH(vo, base) if (!(EXP & 16)) asm volatile("s_mov_b32 m0, %2\n s_nop 0\n global_load_lds_dword %0, %1 nt\n global_load_lds_dword %0, %1 sc1" :: "v"(vo), "s"(base), "i"(L_SINK) : "memory")
  if (CODEPF) {
    u32 co = (idx * 10 + min(lane, 9)) * 128, t0;
    asm volatile("s_cmp_lg_u32 %[wv], 3\n s_cbranch_scc1 1f\n s_getpc_b64 vcc\n .Lcode_base:\n"
                 "s_mov_b32 %[t0], .Lldr_exit-.Lcode_base\n s_max_u32 %[t0], %[t0], .Lmark_1-.Lcode_base\n s_max_u32 %[t0], %[t0], .Lmark_2-.Lcode_base\n"
                 "s_max_u32 %[t0], %[t0], .Lmark_3-.Lcode_base\n s_max_u32 %[t0], %[t0], .Lmark_4-.Lcode_base\n"
                 "v_min_u32 %[co], %[t0], %[co]\n s_mov_b32 m0, %[sink]\n s_nop 0\n global_load_lds_dword %[co], vcc nt\n global_load_lds_dword %[co], vcc sc1\n1:"
                 : [co] "+&v"(co), [t0] "=&s"(t0) : [wv] "s"(wave), [sink] "i"(L_SINK) : "memory", "scc", "vcc");
  }
  *lp<u32>(L_CTRL + 4 * min(threadIdx.x, STAMPS ? 147u : 63u)) = threadIdx.x == 8 ? (32u << 12) | (LVL0 * 128) : 0u;
  u32 vm = (16 * idx + ((lane >> 1) & 15)) * 6144 + (lane >> 5) * 2048 + xcd * 256 + (lane & 1) * 128;
  u32 vq = (16 * idx + (lane >> 3)) * 8192 + (xcd + 8 * ((lane >> 1) & 3)) * 256 + (lane & 1) * 128;
  asm volatile("s_waitcnt lgkmcnt(0)\n s_barrier" : "+v"(vm), "+v"(vq) : "v"(lds_mem), "s"(ws), "s"(wq) : "memory");
  constexpr u32 HOLES = 0xA000u;
#define LANDED(cnt) asm volatile("s_waitcnt vmcnt(" #cnt ")" ::: "memory")
#define LD0(v, o) "buffer_load_dwordx4 %[" #v "], %[rs], %[src] offen offset:" #o " nt lds\n"
#define KV_RSRC(p) v4u{(u32)(u64)(p), (u32)((u64)(p) >> 32) & 0xffffu, (2u << 20) + 0x2000u, 0x00020000u}
  if (wave == 0) {
    __builtin_amdgcn_s_setprio(3);
    PREFETCH(vm, wmod); PREFETCH(vq, wq); PREFETCH(vq, wq + 8 * 8192);
    STAMP(5);
    u32 v1 = (xcd + 8 * (idx >> 3)) * 131072 + (idx & 7) * 16384 + lane * 128;
    u32 v2 = (16 * idx + (lane >> 2)) * (FF * 2) + ((448 * xcd) & ~127) + (lane & 3) * 128;
    u32 v3 = (224 * xcd + 7 * idx) * 1024 + min(lane, 55) * 128, v4 = vm + 4096 - (lane >> 5) * 2048;
    u32 o0 = g * 256 + ((n ^ g) << 4), o1 = o0 ^ 64, o2 = (o0 ^ 128) + 6144, o3 = (o0 ^ 192) + 6144;
    v4u rsrc = KV_RSRC(kc + ((size_t)(b * 32 + h) << 21) + half * 32768 - HOLES);
    asm volatile("" : "+v"(v1), "+v"(v2), "+v"(v3), "+v"(v4), "+v"(o0), "+v"(o1), "+v"(o2), "+v"(o3), "+s"(rsrc));
    LANDED(4); STAMP(3);
    PREFETCH(v4, wmod); PREFETCH(v1, wo); LANDED(6);
    PREFETCH(v1, wo + 8192); LANDED(6);
    STAMP(23);
    if (!first_mod && WQEARLY) *(volatile LAS u32*)C_PF = 2u;
    PREFETCH(v2, wg); LANDED(4);
    PREFETCH(v2, wu); LANDED(4);
    PREFETCH(v3, wd);
    if (first_mod) {
      asm volatile("s_waitcnt vmcnt(%0)" :: "i"(MWAIT) : "memory");
      *(volatile LAS u32*)C_PF = 1u;
#pragma nounroll
      for (u32 spin = 0; lds_poll(C_MODCNT) < CMOD && spin < SPIN_CAP * 8; ++spin) { }
    }
    *(volatile LAS u32*)C_PF = 2u;
#define UNIT LD0(v0, 0) LD0(v1, 1024) LD0(v2, 2048) LD0(v3, 3072) "s_addk_i32 %[src], 0x800\n s_add_u32 m0, m0, 0x1000\n"
    u32 src = HOLES;
    asm volatile("s_mov_b32 m0, 0\n s_waitcnt vmcnt(4)\n" UNIT "s_nop 0\n" UNIT "s_waitcnt vmcnt(10)\n" UNIT "s_nop 0\n" UNIT
                 : [src] "+&s"(src) : [v0] "v"(o0), [v1] "v"(o1), [v2] "v"(o2), [v3] "v"(o3), [rs] "s"(rsrc) : "memory", "scc");
    STAMP(31);
  }
  if (SHIFT) asm volatile("s_branch 1f\n .fill %0, 4, 0xbf800000\n1:" :: "i"(SHIFT / 4 - 1));
  int role = wave;
  asm volatile("" : "+s"(role));

  if (role == 0) {
    const size_t reg_off = ((size_t)(b * 32 + h) << 21) + half * 32768 - HOLES;
    const u64 vb = (u64)(vc + reg_off);
    v4u rsrc = KV_RSRC(kc + reg_off);
    u32 o0 = (EXP & 32) ? lane * 16 : g * 256 + ((n ^ g) << 4), o1 = (EXP & 32) ? o0 : o0 ^ 64, o2 = ((EXP & 32) ? o0 : o0 ^ 128) + 6144, o3 = ((EXP & 32) ? o0 : o0 ^ 192) + 6144;
    const u32 va = C_LANDED + lane * 4;
    u32 off = 0x4000u, src = HOLES | 0x4000u;
    u32 ctl = (32u << 12) | (LVL0 * 128);
#pragma nounroll
    for (u32 end = 1u << 20; end < (2u << 20); end += (1u << 20) - 0x4000u) {
      u32 cur = off, spin = SPIN_CAP * 8, blk, tmp, pcl, pch, pub;
#define LD(v, o) "s_waitcnt vmcnt(\\w)\n" LD0(v, o)
      asm volatile(
        "s_getpc_b64 vcc\n"
        ".Lldr_pc:\n s_add_u32 %[pcl], vcc_lo, .Lldr_copies-.Lldr_pc\n s_addc_u32 %[pch], vcc_hi, 0\n"
        ".Lldr_next:\n s_waitcnt lgkmcnt(4)\n"
        ".Lldr_dec:\n v_readfirstlane_b32 %[tmp], %[ctl]\n ds_read_b32 %[ctl], %[ca]\n s_add_u32 %[blk], %[off], 0x4000\n"
        "s_cmp_gt_u32 %[blk], %[tmp]\n s_cbranch_scc1 .Lldr_stall\n"
        "s_and_b32 %[tmp], %[tmp], 0x780\n s_add_u32 vcc_lo, %[pcl], %[tmp]\n s_addc_u32 vcc_hi, %[pch], 0\n s_lshr_b32 %[tmp], %[off], 12\n s_setpc_b64 vcc\n"
        ".Lldr_stall:\n s_waitcnt lgkmcnt(0)\n s_sub_u32 %[spin], %[spin], 1\n s_cbranch_scc0 .Lldr_dec\n s_branch .Lldr_exit\n"
        ".p2align 7\n.Lldr_copies:\n"
        ".irp w," WLEVELS "\n"
        "v_add_u32_e64 %[pub], %[tmp], -(\\w/4)\n"
        "1:\n s_and_b32 m0, %[off], 0x1ffff\n" LD(v0, 0) "s_addk_i32 %[off], 0x1000\n" LD(v1, 1024) LD(v2, 2048) LD(v3, 3072)
        "ds_write_b32 %[va], %[pub]\n s_addk_i32 %[src], 0x800\n s_or_b32 %[src], %[src], 0xa000\n v_add_u32 %[pub], 1, %[pub]\n"
        "s_cmp_lt_u32 %[off], %[blk]\n s_cbranch_scc1 1b\n s_cmp_lt_u32 %[off], %[end]\n s_cbranch_scc1 .Lldr_next\n s_branch .Lldr_exit\n"
        ".p2align 7\n.endr\n"
        ".Lldr_exit:\n s_waitcnt lgkmcnt(0)"
        : [off] "+&s"(cur), [src] "+&s"(src), [spin] "+&s"(spin), [blk] "=&s"(blk), [tmp] "=&s"(tmp), [pcl] "=&s"(pcl), [pch] "=&s"(pch), [pub] "=&v"(pub), [ctl] "+&v"(ctl)
        : [v0] "v"(o0), [v1] "v"(o1), [v2] "v"(o2), [v3] "v"(o3), [rs] "s"(rsrc), [end] "s"(end), [va] "v"(va), [ca] "v"(C_CTL) : "memory", "scc", "vcc");
      rsrc.x = (u32)vb; rsrc.y = (u32)(vb >> 32) & 0xffffu;
      o0 = o1 = lane * 16; o2 = o3 = lane * 16 + 6144;
      off = end; src = HOLES;
    }
    *(volatile LAS u32*)C_ISSUED = 1u;
#pragma unroll
    for (int k = 6; k >= 0; --k) {
      const u32 pb = 508 - k;
      asm volatile("s_waitcnt vmcnt(%2)\n ds_write_b32 %0, %1" :: "v"(va), "v"(pb), "i"(4 * k) : "memory");
    }
    return;
  }

  const __amdgpu_buffer_rsrc_t rs = __builtin_amdgcn_make_buffer_rsrc((void*)ws, 0, W_END, 0x00020000);

  if (wave <= 2) {
    const int a = wave - 1;
    u32 tag;
    if (a == 0) {
#pragma nounroll
      for (u32 spin = 0; lds_poll(C_PF) < 2u && spin < SPIN_CAP * 8; ++spin) __builtin_amdgcn_s_sleep(1);
      const char* wt = wq + (h * 128 + (2 * b + half) * 16) * 2;
      const u32 vo = (64 * (lane >> 5) + 16 * ((lane >> 3) & 3) + ((lane >> 1) & 3)) * 8192 + (lane & 1) * 16;
#pragma nounroll
      for (int p = 0; p < ((EXP & 1) ? 0 : 16); ++p)
        asm volatile("s_mov_b32 m0, %2\n s_waitcnt vmcnt(%3)\n global_load_lds_dwordx4 %0, %1 nt"
                     :: "v"(vo), "s"(wt + (size_t)(128 * (p >> 2) + 4 * (p & 3)) * 8192), "s"(L_SC + p * 1024), "i"(WQFLY - 1) : "memory");
      asm volatile("s_waitcnt vmcnt(0)" ::: "memory");
      v4u fq[16];
#pragma unroll
      for (int p = 0; p < 16; ++p) {
        const v2u lo = __builtin_bit_cast(v2u, __builtin_amdgcn_ds_read_tr16_b64_v4i16(lp<s4>(L_SC + p * 1024 + lane * 8)));
        const v2u hi = __builtin_bit_cast(v2u, __builtin_amdgcn_ds_read_tr16_b64_v4i16(lp<s4>(L_SC + p * 1024 + 512 + lane * 8)));
        fq[p] = v4u{lo.x, lo.y, hi.x, hi.y};
      }
      PIN4(fq); PIN4(fq + 4); PIN4(fq + 8); PIN4(fq + 12);
      STAMP(10);
      tag = get_tag();
#pragma nounroll
      for (u32 spin = 0; lds_poll(C_HGOT) == 0u && spin < SPIN_CAP * 8; ++spin) __builtin_amdgcn_s_sleep(1);
      CFENCE();
      f4 acc[4] = {zero4, zero4, zero4, zero4};
#pragma unroll
      for (int sg = 0; sg < 4; ++sg) {
        const v4u ha = *lp<v4u>(L_HA + sg * 1024 + lane * 16);
#pragma unroll
        for (int e = 0; e < 4; ++e) acc[e] = mfma32(ha, fq[4 * sg + e], acc[e]);
      }
      const float qv = (acc[0][0] + acc[1][1]) + (acc[2][2] + acc[3][3]);
      const u32 pr = pk(qv, dppf<XOR1>(qv));
      if (!(EXP & 4) && !(n & 1)) __builtin_amdgcn_raw_buffer_store_b64(v2u{pr, tag}, rs, W_Q + (h * 4 + g) * 512 + (2 * b + half) * 64 + (n >> 1) * 8, 0, SC1);
      STAMP(12);
      nap<QDELAY>();
      v2u m = {pr, 0u};
#pragma nounroll
      for (u32 spin = 0; spin < ((EXP & 4) ? 0 : SPIN_CAP); ++spin) {
        CFENCE();
        m = __builtin_amdgcn_raw_buffer_load_b64(rs, lane * 8, W_Q + (h * 4 + b) * 512, SC1);
        if (__all(m.y == tag)) break;
        __builtin_amdgcn_s_sleep(QSLEEP);
      }
      *lp<u32>(L_QV + lane * 4) = m.x;
      CFENCE();
      *(volatile LAS u32*)C_QGOT = 1u;
    } else {
      const u32 t1 = (u32)__builtin_amdgcn_s_memrealtime() + HTIME;
      const int beta = n >> 2, kq = 16 * g + 4 * (n & 3);
      v2u xr[8];
#pragma unroll
      for (int i = 0; i < 8; ++i) xr[i] = ldnt<v2u>(x + (beta * 512 + 64 * i + kq) * 2);
      float s = 0.f;
#pragma unroll
      for (int i = 0; i < 8; ++i) s += (blo(xr[i].x) + bhi(xr[i].x)) + (blo(xr[i].y) + bhi(xr[i].y));
      s += dppf<XOR1>(s); s += dppf<XOR2>(s);
      const float mean = xrow_sum(s) * (1.f / 512.f);
      float q = 0.f;
#pragma unroll
      for (int i = 0; i < 8; ++i)
#pragma unroll
        for (int e = 0; e < 2; ++e) { const float d0 = blo(xr[i][e]) - mean, d1 = bhi(xr[i][e]) - mean; q = fmaf(d0, d0, q); q = fmaf(d1, d1, q); }
      q += dppf<XOR1>(q); q += dppf<XOR2>(q);
      const float mb = rbf(mean), rsd = __builtin_amdgcn_rcpf(rbf(__builtin_amdgcn_sqrtf(rbf(rbf(xrow_sum(q) * (1.f / 512.f)) + 1e-5f))));
#pragma unroll
      for (int i = 0; i < 8; ++i)
#pragma unroll
        for (int e = 0; e < 2; ++e) { const u32 d = pk(blo(xr[i][e]) - mb, bhi(xr[i][e]) - mb); xr[i][e] = pk(blo(d) * rsd, bhi(d) * rsd); }
      PIN4(xr); PIN4(xr + 4);
      tag = get_tag();
#pragma nounroll
      while ((int)((u32)__builtin_amdgcn_s_memrealtime() - t1) < 0) __builtin_amdgcn_s_sleep(2);
      v4u sa[8] = {}, ca[8] = {};
#pragma nounroll
      for (u32 spin = 0; spin < ((EXP & 2) ? 0 : SPIN_CAP); ++spin) {
        CFENCE();
        {
          u32 bad = 0;
#pragma unroll
          for (int i = 0; i < 8; ++i) {
            sa[i] = __builtin_amdgcn_raw_buffer_load_b128(rs, beta * 256 + kq * 4 + (i & 3) * 1024, W_MOD + (i >> 2) * 4096, SC1);
            ca[i] = __builtin_amdgcn_raw_buffer_load_b128(rs, beta * 256 + kq * 4 + (i & 3) * 1024, W_MOD + 8192 + (i >> 2) * 4096, SC1);
          }
          SBAR();
#pragma unroll
          for (int i = 0; i < 8; ++i) bad |= (sa[i].y ^ tag) | (sa[i].w ^ tag) | (ca[i].y ^ tag) | (ca[i].w ^ tag);
          if (__all(bad == 0)) break;
        }
        __builtin_amdgcn_s_sleep(HSLEEP);
      }
#pragma unroll
      for (int sg = 0; sg < 4; ++sg) {
        v4u ha;
#pragma unroll
        for (int r = 0; r < 4; ++r) {
          const int i = 2 * sg + (r >> 1), e = r & 1;
          const u32 c1 = pk(1.f + blo(ca[i][2 * e]), 1.f + bhi(ca[i][2 * e]));
          const u32 t = pk(blo(xr[i][e]) * blo(c1), bhi(xr[i][e]) * bhi(c1));
          ha[r] = pk(blo(t) + blo(sa[i][2 * e]), bhi(t) + bhi(sa[i][2 * e]));
        }
        *lp<v4u>(L_HA + sg * 1024 + lane * 16) = ha;
      }
      CFENCE();
      *(volatile LAS u32*)C_HGOT = 1u;
      STAMP(11);
#pragma nounroll
      for (u32 spin = 0; lds_poll(C_QGOT) == 0u && spin < SPIN_CAP * 8; ++spin) __builtin_amdgcn_s_sleep(4);
    }
    CFENCE();
    v4u qa[4];
#pragma unroll
    for (int s = 0; s < 4; ++s) qa[s] = *lp<v4u>(L_QV + 64 * s + 16 * g);
    if (a == 0) STAMP(2);
    const u32 sc = L_SC + a * 8192;
    int landed = 0;
    const u32 kofs = n * 256 + ((g ^ n) << 4);
    const u32 spos = sc + ((n & 3) * 4 + (n >> 2)) * 4;
#pragma nounroll
    for (int j = 4 * NPRE; j < 128; ++j) {
      const int u = 2 * j + a;
      wait_unit(landed, u, C_LANDED);
      const u32 ad = ((u & 31) << 12) + kofs;
      v4u k0 = *lp<v4u>(ad), k1 = *lp<v4u>(ad ^ 64), k2 = *lp<v4u>(ad ^ 128), k3 = *lp<v4u>(ad ^ 192);
      asm volatile("" : "+v"(k0), "+v"(k1), "+v"(k2), "+v"(k3) :: "memory");
      *(volatile LAS u32*)(C_CONS + 4 * a) = j + 1;
      f4 d = mfma32(qa[0], k0, zero4);
      d = mfma32(qa[1], k1, d); d = mfma32(qa[2], k2, d); d = mfma32(qa[3], k3, d);
      *lp<float>(spos + j * 64) = d[0];
    }
#pragma nounroll
    for (u32 spin = 0; lds_poll(C_PRE) != 8u && spin < SPIN_CAP * 8; ++spin) { }
    CFENCE();
    float mw, lw;
    {
      f4 s[8];
      float mx = -INFINITY;
#pragma unroll
      for (int t = 0; t < 8; ++t) {
        s[t] = *lp<f4>(sc + lane * 16 + t * 1024);
        const u32 p0 = pk(s[t][0], s[t][1]), p1 = pk(s[t][2], s[t][3]);
        const u32 r0 = pk(blo(p0) * 0.08838834764831845f, bhi(p0) * 0.08838834764831845f), r1 = pk(blo(p1) * 0.08838834764831845f, bhi(p1) * 0.08838834764831845f);
        s[t] = f4{blo(r0), bhi(r0), blo(r1), bhi(r1)};
        mx = fmaxf(fmaxf(mx, fmaxf(s[t][0], s[t][1])), fmaxf(s[t][2], s[t][3]));
      }
      mw = wave_max(mx);
      float sum = 0.f;
#pragma unroll
      for (int t = 0; t < 8; ++t) {
        v4u pp;
#pragma unroll
        for (int e = 0; e < 4; ++e) { const float p = __builtin_amdgcn_exp2f((s[t][e] - mw) * LOG2E); sum += p; pp[e] = pk(p, p); }
        *lp<v4u>(sc + lane * 16 + t * 1024) = pp;
      }
      lw = wave_sum(sum);
    }
    const int mlbase = W_ML + (b * 32 + h) * 64;
    if (lane == 0) __builtin_amdgcn_raw_buffer_store_b128(v4u{fu(mw), tag, fu(lw), tag}, rs, mlbase + (half * 2 + a) * 16, 0, SC1);
    u32 msk[4];
#pragma unroll
    for (int q = 0; q < 4; ++q) msk[q] = ((n & 7) >> 1) == q ? ((n & 1) ? 0xffff0000u : 0x0000ffffu) : 0u;
    f4 acc = zero4;
    float scale = 0.f;
    u32 sides = 0;
    u32 gap = 0, gbp = 0, xop = 0;
    const int ow = pid >> 1;
    const int oc = 64 * (ow >> 2) + 32 * (ow & 1) + 8 * (lane & 3) + 4 * (lane >> 5) + (ow & 2);
    v4u p4 = *lp<v4u>(sc + g * 16);
    u32 owa = L_OA + (a + 3 * (n >> 3)) * 160 + (g * 8 + (n & 7)) * 4;
    asm volatile("" : "+v"(owa));
    ONE_PAGE(3000);
#pragma nounroll
    for (int j = 0; j < 126; ++j) {
      const int u = 256 + 2 * j + a;
      if (j == JML) {
        v4u ml;
        const int go = mod_off(b, oc);
#pragma nounroll
        for (u32 spin = 0; spin < SPIN_CAP; ++spin) {
          CFENCE();
          ml = __builtin_amdgcn_raw_buffer_load_b128(rs, mlbase + (lane & 3) * 16, 0, SC1);
          const v2u ga = __builtin_amdgcn_raw_buffer_load_b64(rs, go, 2 * 8192, SC1), gb = __builtin_amdgcn_raw_buffer_load_b64(rs, go, 5 * 8192, SC1);
          const v2u sd = __builtin_amdgcn_raw_buffer_load_b64(rs, W_SIDE + (lane & 3) * 8, 0, SC1);
          xop = ldnt<u32>(x + (b * 512 + oc) * 2);
          gap = ga.x; gbp = gb.x;
          sides = (u32)__builtin_amdgcn_ballot_w64(sd.x & 1);
          if (__all(((ml.y ^ tag) | (ml.w ^ tag) | (ga.y ^ tag) | (gb.y ^ tag) | (sd.y ^ tag)) == 0)) break;
        }
        float M = uf(ml.x); M = fmaxf(M, dppf<XOR1>(M)); M = fmaxf(M, dppf<XOR2>(M));
        float L = uf(ml.z) * __builtin_amdgcn_exp2f((uf(ml.x) - M) * LOG2E);
        L += dppf<XOR1>(L); L += dppf<XOR2>(L);
        scale = __builtin_amdgcn_exp2f((mw - M) * LOG2E) / L;
        *(volatile LAS float*)(C_SCALE + 4 * a) = uf((u32)__builtin_amdgcn_readfirstlane((int)fu(scale)));
        lds_add(C_SCNT, 1u);
        if (OUTPRE && a == 0 && !half && (lane & 28) == 0) {
          GAS u32* op = (GAS u32*)(out + (b * 512 + oc) * 2);
          if (OUTPRE == 1) { const u32 t = *op; asm volatile("" :: "v"(t)); }
          if (OUTPRE == 2) *op = 0u;
          if (OUTPRE == 3) __hip_atomic_store(op, 0u, __ATOMIC_RELAXED, __HIP_MEMORY_SCOPE_AGENT);
        }
        asm volatile("" : "+v"(xop));
      }
      const u32 ad = ((u & 31) << 12) + lane * 16;
      wait_unit(landed, u, C_LANDED);
      v4u v0 = *lp<v4u>(ad), v1 = *lp<v4u>(ad + 1024), v2 = *lp<v4u>(ad + 2048), v3 = *lp<v4u>(ad + 3072);
      CFENCE();
      *(volatile LAS u32*)(C_CONS + 4 * a) = 128 + j + 1;
      const v4u pn = *lp<v4u>(sc + (j + 1) * 64 + g * 16);
      SBAR();
      v4u bf[4];
#pragma unroll
      for (int i = 0; i < 4; ++i) bf[i] = v4u{p4[i] & msk[0], p4[i] & msk[1], p4[i] & msk[2], p4[i] & msk[3]};
      SBAR();
      acc = mfma32(v0, bf[0], acc); acc = mfma32(v1, bf[1], acc); acc = mfma32(v2, bf[2], acc); acc = mfma32(v3, bf[3], acc);
      SBAR();
      p4 = pn;
    }
    *lp<u32>(owa) = pk((n & 8 ? acc[2] : acc[0]) * scale, (n & 8 ? acc[3] : acc[1]) * scale);
    if (a == 0) STAMP(6);
    asm volatile("s_waitcnt lgkmcnt(0)\n s_barrier" ::: "memory");
    SBAR();
    if (a == 1) {
      int xg = late(sides, xcd, 16) + b * 2048 + lane * 16;
      int xg2 = xg + 1024;
      u32 la = C_LN, ones = 0x3f803f80u;
      asm volatile("" : "+v"(xg), "+v"(xg2), "+v"(la), "+v"(ones));
      __builtin_amdgcn_s_setprio(2);
      __builtin_amdgcn_s_barrier();
      CFENCE();
      v4u m0, m1;
#define XLOAD m0 = __builtin_amdgcn_raw_buffer_load_b128(rs, xg, 0, SC1), m1 = __builtin_amdgcn_raw_buffer_load_b128(rs, xg2, 0, SC1)
      XLOAD;
      RETRY(tags_ok(m0, m1, tag), XLOAD, );
      const float s1 = dot2(m1.z, ones, dot2(m1.x, ones, dot2(m0.z, ones, dot2(m0.x, ones, 0.f))));
      const float s2 = dot2(m1.z, m1.z, dot2(m1.x, m1.x, dot2(m0.z, m0.z, dot2(m0.x, m0.x, 0.f))));
      float st = swap32_sum(s1, s2);
      st += dppf<XOR1>(st); st += dppf<XOR2>(st); st += dppf<HMIR>(st); st += dppf<ROR8>(st);
      st = swap16_sum(st, st) * (1.f / 512.f);
      MARK(2);
      const float mean = uf((u32)__builtin_amdgcn_readlane((int)fu(st), 0)), m2 = uf((u32)__builtin_amdgcn_readlane((int)fu(st), 32));
      float var = fmaf(-mean, mean, m2);
      if (__builtin_expect(__builtin_amdgcn_ballot_w64(!(var >= m2 * (1.f / 256.f))) != 0, 0)) {
        const u32 v[4] = {m0.x, m0.z, m1.x, m1.z};
        float q = 0.f;
#pragma unroll
        for (int e = 0; e < 4; ++e) { const float d0 = blo(v[e]) - mean, d1 = bhi(v[e]) - mean; q = fmaf(d0, d0, q); q = fmaf(d1, d1, q); }
        var = wave_sum(q) * (1.f / 512.f);
      }
      *lp<f2>(la) = f2{rbf(mean), __builtin_amdgcn_rcpf(rbf(__builtin_amdgcn_sqrtf(rbf(rbf(var) + 1e-5f))))};
      asm volatile("s_waitcnt lgkmcnt(0)" ::: "memory");
      __builtin_amdgcn_s_barrier();
      return;
    }
    __builtin_amdgcn_s_setprio(2);
    int io = late(sides, xcd, b * 4 + hh) + lane * 16;
    int xs = late(sides, (lane >> 2) & 7, 16) + b * 2048 + ow * 64 + (lane >> 5) * 32 + (lane & 3) * 8;
    float ga0 = blo(gap), ga1 = bhi(gap), xo0 = blo(xop), xo1 = bhi(xop);
    asm volatile("" : "+v"(io), "+v"(xs), "+v"(ga0), "+v"(ga1), "+v"(xo0), "+v"(xo1));
    v4u m[4];
    STAMP(4);
    u32 tl = lds_poll(C_TL);
    u32 x1 = 0;
    wait_until(now32() + D1);
    wait_until(tl + T1);
    load4(m, rs, io, io + 3072);
    if (__builtin_expect(!tags_ok(m, tag), 0)) {
      u32 k;
#pragma nounroll
      for (u32 spin = 0; spin < SPIN_CAP; ++spin) {
        k = max(max(m[0].y - tag, m[1].y - tag), max(m[2].y - tag, m[3].y - tag));
        if (__all(k < 64u && m[0].y == m[0].w && m[1].y == m[1].w && m[2].y == m[2].w && m[3].y == m[3].w)) break;
        CFENCE();
        load4(m, rs, io, io + 3072);
        NP_INC;
      }
      k = max(k, dppu<ROR4>(k)); k = max(k, dppu<ROR8>(k));
      const int km = max(max(__builtin_amdgcn_readlane(k, 0), __builtin_amdgcn_readlane(k, 16)), max(__builtin_amdgcn_readlane(k, 32), __builtin_amdgcn_readlane(k, 48))) & 63;
      if (km) tl += max(4 * km + (T1 - GL) + XL - T2, SMIN);
      const u32 t = now32() - (T1 + TM);
      if ((int)(t - tl) > 0) tl = t;
    }
    if (!half) {
      STAMP(15);
      float y0, y1;
      inbox_sum1(m, y0, y1);
      const u32 yb = inbox_sum2(y0, y1);
      const u32 t = pk(ga0 * blo(yb), ga1 * bhi(yb));
      x1 = pk(xo0 + blo(t), xo1 + bhi(t));
      __builtin_amdgcn_raw_buffer_store_b64(v2u{x1, tag}, rs, xs, 0, SC1);
      STAMP(16);
    }
    wait_until(tl + T2);
    __builtin_amdgcn_s_barrier();
    if (half) {
      return;
    }
    float r0 = blo(x1), r1 = bhi(x1), gb0 = blo(gbp), gb1 = bhi(gbp);
    io += 4096;
    int io3 = lane >= 32 ? W_END : io + 3072;
    u32 tg3 = lane >= 32 ? 0u : tag, oa = (b * 512 + oc) * 2;
    asm volatile("" : "+v"(r0), "+v"(r1), "+v"(gb0), "+v"(gb1), "+v"(io), "+v"(io3), "+v"(tg3), "+v"(oa));
    __builtin_amdgcn_s_barrier();
    __builtin_amdgcn_s_sleep(D3 * 24 / 64);
    wait_until(tl + T3);
    load4(m, rs, io, io3);
    RETRY(tags_ok(m, tag, tg3), load4(m, rs, io, io3), );
    STAMP(19);
    float y0, y1;
    inbox_sum1(m, y0, y1);
    const u32 yb = inbox_sum2(y0, y1);
    MARK(1);
    const u32 t = pk(gb0 * blo(yb), gb1 * bhi(yb));
    if (FIXSPAN) wait_until(t_fix + 20000);
    if ((lane & 28) == 0) {
      const u32 res = pk(r0 + blo(t), r1 + bhi(t));
      if (OUTSC == 1) __hip_atomic_store((GAS u32*)(out + oa), res, __ATOMIC_RELAXED, __HIP_MEMORY_SCOPE_AGENT);
      else if (OUTSC == 2) __builtin_nontemporal_store(res, (GAS u32*)(out + oa));
      else *(GAS u32*)(out + oa) = res;
    }
    STAMP(20);
    return;
  }

  if (wave == 3) {
    u32 tref = (u32)__builtin_amdgcn_s_memrealtime() + T00;
    const u32 epoch = __builtin_amdgcn_raw_buffer_load_b32(rs, W_EPOCH, 0, SC1);
#define TOUCH(p) do { u32 d_; asm volatile("s_load_dword %0, %1, 0\n s_waitcnt lgkmcnt(0)" : "=&s"(d_) : "s"(p) : "memory"); } while (0)
    if (TLBW & 4) TOUCH(kc + ((size_t)(b * 32 + h) << 21));
    const u32 tag = ((u32)__builtin_amdgcn_readfirstlane((int)epoch) + 64u) & ~63u;
    *(volatile LAS v2u*)C_TAG = v2u{tag, 1u};
    int lvl = LVL0, uref = 0, tau = TAU * 64 / 100, kp = 1, kr = 1;
#pragma nounroll
    for (u32 spin = 0; spin < SPIN_CAP * 8; ++spin) {
      const v3u cc = *(volatile LAS v3u*)C_CONS;
      const int u = (int)lds_poll(C_LANDED);
      const u32 now = (u32)__builtin_amdgcn_s_memrealtime();
      const u32 c0 = __builtin_amdgcn_readfirstlane(cc.x), c1 = __builtin_amdgcn_readfirstlane(cc.y), pl = __builtin_amdgcn_readfirstlane(cc.z) ^ (0x88888888u >> (32 - 4 * NPRE));
      const u32 done = pl ? (__builtin_ctz(pl) & 28) * 2 : max(8u * NPRE, 2 * min(c0, c1));
      const int lag = (int)(now - tref) - (((u - uref) * tau) >> 6);
      lvl = min(max(LVL0 + ((lag * (u < G2AT ? GAIN : GAIN2) + 128) >> 8), !uref ? LVL0 : u < G2AT ? LMIN : LMIN2), LMAX);
      *(volatile LAS u32*)C_CTL = ((done + 32) << 12) | (lvl << 7);
      if (u >= CEXIT && done >= 476 && kr == 9) break;
      if (u >= 64 * kp - CK0) {
        if ((idx & 3) == 0 && lane == 0) __builtin_amdgcn_raw_buffer_store_b64(v2u{now, tag}, rs, W_PROG + kp * 512 + ((idx >> 2) * 8 + xcd) * 8, 0, SC1);
        ++kp;
      }
      if (u >= 64 * kr - CK0 + (kr < 8 ? RDLAG : RDLAG2)) {
        const v2u pr = __builtin_amdgcn_raw_buffer_load_b64(rs, W_PROG + kr * 512 + lane * 8, 0, SC1);
        const float ago = wave_sum(pr.y == tag ? (float)(int)(now - pr.x) : 0.f);
        const u32 tk = now - (u32)__builtin_amdgcn_readfirstlane((int)(ago * (1.f / 64.f)));
        if (kr > 1) tau = min(max((int)(tk - tref), 800), 1000);
        tref = tk; uref = 64 * kr - CK0; ++kr;
      }
      __builtin_amdgcn_s_sleep(3);
    }
    const u32 tl = tref + (u32)(((512 - uref) * tau) >> 6);
    *(volatile LAS u32*)C_TL = tl;
    CFENCE();
    u32 wa = L_OA + (2 + 3 * (n >> 3)) * 160 + (g * 8 + (n & 7)) * 4;
    v4u bf[16];
#pragma unroll
    for (int r = 0; r < 4; ++r) {
      const v4u p4 = *lp<v4u>(L_SC + (r & 1) * 8192 + (126 + (r >> 1)) * 64 + g * 16);
#pragma unroll
      for (int i = 0; i < 4; ++i)
#pragma unroll
        for (int q = 0; q < 4; ++q) bf[4 * r + i][q] = p4[i] & (((n & 7) >> 1) == q ? ((n & 1) ? 0xffff0000u : 0x0000ffffu) : 0u);
      PIN4(bf + 4 * r);
    }
#pragma nounroll
    for (u32 spin = 0; lds_poll(C_SCNT) != 2u && spin < SPIN_CAP * 8; ++spin) { }
    CFENCE();
    f2 sc2 = *lp<f2>(C_SCALE);
    const __amdgpu_buffer_rsrc_t rv = __builtin_amdgcn_make_buffer_rsrc((void*)(vc + ((size_t)(b * 32 + h) << 21) + (31 << 16) + half * 32768 + 16384), 0, 16384, 0x00020000);
    asm volatile("" : "+v"(wa), "+v"(sc2));
#pragma nounroll
    for (u32 spin = 0; lds_poll(C_ISSUED) == 0u && spin < SPIN_CAP * 8; ++spin) { }
    CFENCE();
    ONE_PAGE(1000);
    v4u vv[16];
    const int lastp = 509 - max(LASTW + (LASTG * (lvl - LVL0) >> 2), 4);
#pragma unroll
    for (int i = 0; i < 16; ++i) {
      if ((i & 3) == 0) {
#pragma nounroll
        for (u32 spin = 0; (int)lds_poll(C_LANDED) < lastp + (i >> 2) && spin < SPIN_CAP * 8; ++spin) { }
        CFENCE();
      }
      vv[i] = __builtin_amdgcn_raw_buffer_load_b128(rv, lane * 16 + (i & 1) * 1024 + ((i >> 2) & 1) * 2048, (i >> 3) * 4096 + ((i >> 1) & 1) * 8192, NT);
    }
    SBAR();
    f4 ac[2] = {zero4, zero4};
    float e0, e1;
#pragma unroll
    for (int i = 0; i < 16; ++i) {
      ac[(i >> 2) & 1] = mfma32(vv[i], bf[i], ac[(i >> 2) & 1]);
      SBAR();
      if (i == 11) { e0 = (n & 8 ? ac[0][2] : ac[0][0]) * sc2.x; e1 = (n & 8 ? ac[0][3] : ac[0][1]) * sc2.x; asm volatile("" : "+v"(e0), "+v"(e1)); }
    }
    if (INJ && (blockIdx.x & 63) == ((tag * 2654435761u) >> 26)) wait_until(lds_poll(C_TL) + INJ);
    STAMP(1);
    MARK(4);
    const u32 tland = now32();
    *lp<u32>(wa) = pk(fmaf(n & 8 ? ac[1][2] : ac[1][0], sc2.y, e0), fmaf(n & 8 ? ac[1][3] : ac[1][1], sc2.y, e1));
    *(volatile LAS u32*)C_YTAG = tag + (u32)min(max(((int)(tland - tl) - (T1 - GL) + 3) >> 2, 0), 63);
    asm volatile("s_waitcnt lgkmcnt(0)" ::: "memory");
    __builtin_amdgcn_s_barrier();
    return;
  }

  const int w = wave - 4;
  if (first_mod) mod_tile<false>(cnd, wmod, rs, 16 * b + 2 * xcd + half, w, lane);
  {
    v4u kr[NPRE][4];
    const u32 ad = w * 4096 + n * 256 + ((g ^ n) << 4);
#pragma unroll
    for (int r = 0; r < NPRE; ++r) {
#pragma nounroll
      for (u32 spin = 0; (int)lds_poll(C_LANDED) <= 8 * r + w && spin < SPIN_CAP * 8; ++spin) __builtin_amdgcn_s_sleep(8);
      CFENCE();
#pragma unroll
      for (int s = 0; s < 4; ++s) kr[r][s] = *lp<v4u>((ad ^ (s * 64)) + (r & 3) * 32768);
      asm volatile("" : "+v"(kr[r][0]), "+v"(kr[r][1]), "+v"(kr[r][2]), "+v"(kr[r][3]) :: "memory");
      lds_add(C_PULL, 1u << (4 * r));
    }
#pragma nounroll
    for (u32 spin = 0; lds_poll(C_QGOT) == 0u && spin < SPIN_CAP * 8; ++spin) __builtin_amdgcn_s_sleep(8);
    CFENCE();
    v4u qa[4];
#pragma unroll
    for (int s = 0; s < 4; ++s) qa[s] = *lp<v4u>(L_QV + 64 * s + 16 * g);
#pragma unroll
    for (int r = 0; r < NPRE; ++r) {
      f4 d = mfma32(qa[0], kr[r][0], zero4);
      d = mfma32(qa[1], kr[r][1], d); d = mfma32(qa[2], kr[r][2], d); d = mfma32(qa[3], kr[r][3], d);
      *lp<float>(L_SC + (w & 1) * 8192 + (4 * r + (w >> 1)) * 64 + ((n & 3) * 4 + (n >> 2)) * 4) = d[0];
    }
    lds_add(C_PRE, 1u);
  }
  const u32 tag = get_tag();
  if (blockIdx.x < 4 && w == 6) {
    if (lane < 2 * NPROBE) __builtin_amdgcn_raw_buffer_store_b32(0u, rs, W_PROBE + blockIdx.x * 16384 + (lane & 1) * 8192 + (((lane >> 1) * 2432) & 8064), 0, SC1);
    asm volatile("s_waitcnt vmcnt(0)" ::: "memory");
    u32 best[2] = {~0u, ~0u}, sum[2] = {0u, 0u};
#pragma nounroll
    for (int i = 0; i < NPROBE; ++i) {
#pragma unroll
      for (int p = 0; p < 2; ++p) {
        u64 t0, t1; u32 d;
        asm volatile("s_memtime %0\n s_waitcnt lgkmcnt(0)\n s_load_dword %2, %3, 0\n s_waitcnt lgkmcnt(0)\n s_memtime %1\n s_waitcnt lgkmcnt(0)"
                     : "=&s"(t0), "=&s"(t1), "=&s"(d) : "s"(ws + W_PROBE + blockIdx.x * 16384 + p * 8192 + ((i * 2432) & 8064)));
        best[p] = min(best[p], (u32)(t1 - t0)); sum[p] += (u32)(t1 - t0);
      }
    }
    if (lane == 0) __builtin_amdgcn_raw_buffer_store_b64(v2u{NUMA >= 0 ? (NUMA >> blockIdx.x) & 1u : best[1] < best[0] ? 1u : 0u, tag}, rs, W_SIDE + blockIdx.x * 8, 0, SC1);
  }
  if ((TLBW & 1) && w == 4) TOUCH(out);
  if ((TLBW & 2) && w == 5) TOUCH(vc + ((size_t)(b * 32 + h) << 21));
  const int fs = 7 * xcd + (pid & 7);
  v8u fo[2][4], fg[4];
  v4u fd[4];
  {
    const __amdgpu_buffer_rsrc_t ro = __builtin_amdgcn_make_buffer_rsrc((void*)(wo + (size_t)h * 131072), 0, (NOW & 1) ? 0 : 131072, 0x00020000);
    const int vo = 2 * g * 1024 + (64 * w + 4 * n) * 2;
#pragma unroll
    for (int s = 0; s < 4; ++s) {
      v2u r[8];
#pragma unroll
      for (int j = 0; j < 8; ++j) { r[j] = __builtin_amdgcn_raw_buffer_load_b64(ro, vo + (j >> 2) * 1024, (32 * s + 8 * (j & 3)) * 1024, WPOL); PACE(j); }
      if (s & 1) tr8x4<1>(r, fo[s >> 1]); else tr8x4<0>(r, fo[s >> 1]);
      PIN4(fo[s >> 1]);
      SBAR();
    }
  }
  if (has_ffn) {
    const char* pg = (n < 8 ? wg : wu) + (fs * 32 + 4 * (n & 7)) * 2;
#pragma unroll
    for (int s = 0; s < 2; ++s) {
      v2u r[8];
      const char* pr = pg + (size_t)(64 * w + 32 * (g >> 1) + 4 * (g & 1) + 2 * s) * (FF * 2);
#pragma unroll
      for (int j = 0; j < 8; ++j) { r[j] = (NOW & 2) ? v2u{0u, 0u} : ldnt<v2u>(pr + (8 * (j >> 1) + (j & 1)) * (FF * 2)); PACE(j); }
      if (s) tr8x4<1>(r, fg); else tr8x4<0>(r, fg);
      PIN4(fg);
      SBAR();
    }
  } else {
#pragma unroll
    for (int t = 0; t < 4; ++t) fg[t] = v8u{};
  }
  {
    const __amdgpu_buffer_rsrc_t rd = __builtin_amdgcn_make_buffer_rsrc((void*)(wd + (size_t)fs * 32768), 0, ((NOW & 2) || !has_ffn) ? 0 : 32768, 0x00020000);
    const int vd = 8 * g * 1024 + (128 * (w & 3) + 64 * (w >> 2) + 4 * n) * 2;
    v2u r[8];
#pragma unroll
    for (int j = 0; j < 8; ++j) { r[j] = __builtin_amdgcn_raw_buffer_load_b64(rd, vd + (j & 3) * 1024, (j >> 2) * 4096, NT); PACE(j); }
    tr8x4(r, fd);
    PIN4(fd);
    SBAR();
  }
  const bool lo_part = n & 4;
  const int xg = 8 * g + 2 * (n & 3) + (n >> 3);
  const u32 gidx = n & 8 ? 0xeeeeu : 0x4444u;
  float cb0, cb1, sh;
  u32 sides = 0;
  {
    v2u sv, cv; v4u xr;
    const int so = mod_off(b, 64 * w + 32 * ((xg >> 3) & 1) + 4 * ((xg >> 2) & 1) + 8 * (xg & 3) + 2 * (xg >> 4));
#pragma nounroll
    for (u32 spin = 0; spin < SPIN_CAP; ++spin) {
      CFENCE();
      sv = __builtin_amdgcn_raw_buffer_load_b64(rs, so, 3 * 8192, SC1);
      cv = __builtin_amdgcn_raw_buffer_load_b64(rs, so, 4 * 8192, SC1);
      const v2u sd = __builtin_amdgcn_raw_buffer_load_b64(rs, W_SIDE + (lane & 3) * 8, 0, SC1);
      xr = ldnt<v4u>(x + (b * 512 + 8 * lane) * 2);
      sides = (u32)__builtin_amdgcn_ballot_w64(sd.x & 1);
      if (__all(((sv.y ^ tag) | (cv.y ^ tag) | (sd.y ^ tag)) == 0)) break;
      __builtin_amdgcn_s_sleep(64);
    }
    const u32 c1p = pk(1.f + blo(cv.x), 1.f + bhi(cv.x));
    cb0 = blo(c1p); cb1 = bhi(c1p);
    sh = rbf(wave_sum((blo(xr.x) + bhi(xr.x)) + (blo(xr.y) + bhi(xr.y)) + (blo(xr.z) + bhi(xr.z)) + (blo(xr.w) + bhi(xr.w))) * (1.f / 512.f));
#pragma nounroll
    for (u32 spin = 0; lds_poll(C_PRE) != 8u && spin < SPIN_CAP * 8; ++spin) { }
    CFENCE();
    {
      *lp<float>(L_C12 + w * 256 + lane * 4) = gu_dot(lo_part ? 0u : c1p, gidx, fg);
      SBAR();
      *lp<float>(L_C12 + 2048 + w * 256 + lane * 4) = gu_dot(lo_part ? 0u : sv.x, gidx, fg);
    }
    asm volatile("" : "+v"(cb0), "+v"(cb1), "+v"(sh));
  }
  if (blockIdx.x == 0 && w == 7) {
#pragma nounroll
    for (u32 spin = 0; spin < SPIN_CAP; ++spin) {
      CFENCE();
      u32 bad = 0;
#pragma nounroll
      for (int i = 0; i < 8; ++i) {
        const v4u t = __builtin_amdgcn_raw_buffer_load_b128(rs, W_ML + lane * 16, i * 1024, SC1);
        bad |= (t.y ^ tag) | (t.w ^ tag);
      }
      if (__all(bad == 0)) break;
      __builtin_amdgcn_s_sleep(64);
    }
    if (lane == 0) __builtin_amdgcn_raw_buffer_store_b32(tag, rs, W_EPOCH, 0, SC1);
  }
  if (w == 0) STAMP(13);
  int mo = late(sides, w, b * 4 + ((lane >> 3) & 3)) + pid * 64 + (lane & 7) * 8;
  const int oi = 32 * (n >> 3) + 8 * g + (n & 7);
  u32 ra = L_C12 + ((oi & 3) * 16 + (oi >> 2)) * 4, oa = L_OA + (min(n & 3, 2) + 3 * ((n >> 2) & 1)) * 160 + g * 16;
  u32 oidx = n & 4 ? 0xeeeeu : 0x4444u, ya = C_YTAG;
  f4 d[4] = {};
  asm volatile("s_waitcnt lgkmcnt(0)" : "+v"(ra), "+v"(oa), "+v"(mo), "+v"(oidx), "+v"(ya), "+v"(d[0]), "+v"(d[1]), "+v"(d[2]), "+v"(d[3]) :: "memory");
  ONE_PAGE(1600);
  __builtin_amdgcn_s_barrier();
  CYC(0);
  CFENCE();
  {
    const v4u a0 = *lp<v4u>(oa), a1 = *lp<v4u>(oa + 64);
    const u32 ytag = *lp<u32>(ya);
    SBAR();
#pragma unroll
    for (int t = 0; t < 4; ++t) d[t] = smfmac(a0, fo[0][t], d[t], oidx);
#pragma unroll
    for (int t = 0; t < 4; ++t) d[t] = smfmac(a1, fo[1][t], d[t], oidx);
    const float y0 = swap16_sum((d[0][0] + d[0][1]) + d[0][2], (d[2][0] + d[2][1]) + d[2][2]), y1 = swap16_sum((d[1][0] + d[1][1]) + d[1][2], (d[3][0] + d[3][1]) + d[3][2]);
    if (lane < 32) __builtin_amdgcn_raw_buffer_store_b64(v2u{pk(y0, y1), ytag}, rs, mo, 0, SC1);
  }
  CYC(1);
  if (w == 0 && STAMPS == 1) STAMP(14);
  float c1 = sum8(ra), c2 = sum8(ra + 2048);
#pragma unroll
  for (int t = 0; t < 4; ++t) d[t] = zero4;
  u32 gi = gidx;
  u32 wa = L_GURED + w * 256 + lane * 4, la = C_LN;
  int xa = w * 256 + xg * 8 + late(sides, xcd, 16) + b * 2048;
  mo = has_ffn ? late(sides, (2 * w + (lane >> 5)) & 7, b * 4 + ((lane >> 3) & 3)) + 4096 + fs * 64 + (lane & 7) * 8 : W_END;
  asm volatile("" : "+v"(c1), "+v"(c2), "+v"(d[0]), "+v"(d[1]), "+v"(d[2]), "+v"(d[3]), "+v"(gi), "+v"(wa), "+v"(la), "+v"(xa), "+v"(mo));
#pragma unroll
  for (int t = 0; t < 4; ++t) *lp<v4u>(w * 4096 + t * 1024 + lane * 16) = fd[t];
  u32 fa = (w | 4) * 4096 + lane * 16;
  asm volatile("s_waitcnt lgkmcnt(0)" : "+v"(fa) :: "memory");
  __builtin_amdgcn_s_barrier();
  CYC(2);
  v4u fe[4];
  v2u m;
  CFENCE();
  m = __builtin_amdgcn_raw_buffer_load_b64(rs, xa, 0, SC1);
  SBAR();
#pragma unroll
  for (int t = 0; t < 4; ++t) fe[t] = *lp<v4u>(fa + t * 1024);
  PIN4(fe);
  RETRY(__builtin_amdgcn_ballot_w64(m.y != tag) == 0, m = __builtin_amdgcn_raw_buffer_load_b64(rs, xa, 0, SC1), );
  CYC(3);
  if (w == 0 && STAMPS == 1) STAMP(17);
  {
    const float a0 = (blo(m.x) - sh) * cb0, a1 = (bhi(m.x) - sh) * cb1;
    const u32 hi = pk(a0, a1), lo = pk(a0 - blo(hi), a1 - bhi(hi));
    const u32 p = lo_part ? lo : hi;
    gu_mfma(p, gi, fg, d);
    CYC(4);
    *lp<float>(wa) = gu_fold(d);
  }
  CYC(5);
  asm volatile("s_waitcnt lgkmcnt(0)" ::: "memory");
  __builtin_amdgcn_s_barrier();
  if (w >= 4) return;
  CYC(6);
  CFENCE();
  {
    const f2 ln = *lp<f2>(la);
    const float gsum = sum8(ra + (L_GURED - L_C12));
    const float s = rbf(fmaf(ln.y, fmaf(sh - ln.x, c1, gsum), c2));
    CYC(7);
    const float up = dppf<ROR8>(s);
    const float z = s * fmaf(s * s, 0.044715f * 0.7978845608028654f * 2.f * LOG2E, 0.7978845608028654f * 2.f * LOG2E);
    const float ge = rbf(s * __builtin_amdgcn_rcpf(1.f + __builtin_amdgcn_exp2f(-z)));
    const float act = ge * up;
    const u32 ap = pk(act, dppf<XOR1>(act));
    const v4u aa = {dppu<0x150>(ap), dppu<0x152>(ap), dppu<0x154>(ap), dppu<0x156>(ap)};
    float y[8];
#pragma unroll
    for (int t = 0; t < 8; ++t) y[t] = mfma32(aa, t < 4 ? fd[t & 3] : fe[t & 3], zero4)[0];
    CYC(8);
    MARK(3);
    const u32 p0 = pk(y[0], y[1]), p1 = pk(y[2], y[3]), p2 = pk(y[4], y[5]), p3 = pk(y[6], y[7]);
    const u32 p01 = (g & 1) ? p1 : p0, p23 = (g & 1) ? p3 : p2;
    __builtin_amdgcn_raw_buffer_store_b64(v2u{(g & 2) ? p23 : p01, tag}, rs, mo, 0, SC1);
  }
  CYC(9);
  if (w == 0) STAMP(18);
}

}

extern "C" void kernel_launch(void* const* d_in, const int* in_sizes, int n_in, void* d_out, int out_size, void* d_ws, size_t ws_size, hipStream_t stream) {
  hipLaunchKernelGGL(blk::fused_block, dim3(256), dim3(blk::NTHR), 0, stream,
                     (const char*)d_in[0], (const char*)d_in[1], (const char*)d_in[2], (const char*)d_in[3], (const char*)d_in[4], (const char*)d_in[6],
                     (const char*)d_in[7], (const char*)d_in[8], (const char*)d_in[9], (const char*)d_in[10], (char*)d_out, (char*)d_ws);
}
```
